```python
import torch

def _softmax(x, axis=-1):
    return torch.softmax(x, dim=axis)
def _gelu(x):
    return torch.nn.functional.gelu(x, approximate="tanh")
def _silu(x):
    return torch.nn.functional.silu(x)
def _sigmoid(x):
    return torch.sigmoid(x)
def _layernorm(x, axis=-1, eps=1e-5):
    m = x.mean(dim=axis, keepdim=True)
    v = x.var(dim=axis, keepdim=True, unbiased=False)
    return (x - m) / torch.sqrt(v + eps)
def _scatter_add(tgt, idx, src, axis=0):
    return tgt.clone().index_add_(axis, idx, src)
def _segment_sum(src, seg, n):
    out = torch.zeros((n,) + src.shape[1:], dtype=src.dtype)
    return out.index_add_(0, seg, src)
def _pad(x, pw):
    flat = tuple(v for pair in reversed(pw) for v in pair)
    return torch.nn.functional.pad(x, flat)

kernel_name = 'parallel_sum__mha_block_cached__glu_gated_bilinear__decode'

def setup_inputs():

    _w = (lambda a: (lambda t: t.to(torch.bfloat16) if t.is_floating_point() else t)(torch.as_tensor(a)))
    def _A_setup():
        B, Sq, D = 4, 1, 640
        H, Sk, Dh = 32, 4096, 128
        return {
            "x": _w((lambda *s: torch.randn(*s, dtype=torch.bfloat16) * 0.05)(B, Sq, D)),
            "k_cache": _w((lambda *s: torch.randn(*s, dtype=torch.bfloat16) * 1.6)(B, H, Sk, Dh)),
            "v_cache": _w((lambda *s: torch.randn(*s, dtype=torch.bfloat16) * 0.8)(B, H, Sk, Dh)),
            "wq": _w((lambda *s: torch.randn(*s, dtype=torch.bfloat16) * 1.6)(D, H * Dh) * (D ** -0.5)),
            "wo": _w((lambda *s: torch.randn(*s, dtype=torch.bfloat16) * 0.8)(H * Dh, D) * ((H * Dh) ** -0.5)),
        }
    _A_in = _A_setup()

    def _B_setup():
        B, S, D, F = 4, 1, 640, 1728
        return {
            "x": _w((lambda *s: torch.randn(*s, dtype=torch.bfloat16) * 0.05)(B, S, D)),
            "w_gate": _w((lambda *s: torch.randn(*s, dtype=torch.bfloat16) * 0.4)(D, F) * (D ** -0.5)),
            "w_up": _w((lambda *s: torch.randn(*s, dtype=torch.bfloat16) * 0.4)(D, F) * (D ** -0.5)),
            "w_down": _w((lambda *s: torch.randn(*s, dtype=torch.bfloat16) * 0.4)(F, D) * (F ** -0.5)),
        }
    _B_in = _B_setup()

    return {
        "A_x": _A_in["x"],
        "A_k_cache": _A_in["k_cache"],
        "A_v_cache": _A_in["v_cache"],
        "A_wq": _A_in["wq"],
        "A_wo": _A_in["wo"],
        "B_x": _B_in["x"],
        "B_w_gate": _B_in["w_gate"],
        "B_w_up": _B_in["w_up"],
        "B_w_down": _B_in["w_down"],
    }

def reference(A_x, A_k_cache, A_v_cache, A_wq, A_wo, B_x, B_w_gate, B_w_up, B_w_down):
    def _A_ref(x, k_cache, v_cache, wq, wo):
        B, Sq, D = x.shape
        H, Dh = 32, 128
        q = (lambda x, *d: x.permute(*d))((x @ wq).reshape(B, Sq, H, Dh), 0, 2, 1, 3)
        scores = torch.einsum("bhqd,bhkd->bhqk", q, k_cache) * (Dh ** -0.5)
        p = _softmax(scores, axis=-1)
        o = torch.einsum("bhqk,bhkd->bhqd", p, v_cache)
        return (lambda x, *d: x.permute(*d))(o, 0, 2, 1, 3).reshape(B, Sq, H * Dh) @ wo

    def _B_ref(x, w_gate, w_up, w_down):
        g = torch.einsum("bsd,df->bsf", x, w_gate)
        u = torch.einsum("bsd,df->bsf", x, w_up)
        h = g * u
        return torch.einsum("bsf,fd->bsd", h, w_down)

    x = A_x
    xn = _layernorm(x, axis=-1)
    return x + _A_ref(xn, A_k_cache, A_v_cache, A_wq, A_wo) + _B_ref(xn, B_w_gate, B_w_up, B_w_down)
```

```cpp
#include <hip/hip_runtime.h>
#include <hip/hip_bf16.h>
#include <stdint.h>

#define RING 20
#define KV_NT 1
#define CHUNK_MAP 0
#define MIMIC 1

typedef unsigned u32x4 __attribute__((ext_vector_type(4)));
typedef unsigned u32x2 __attribute__((ext_vector_type(2)));
typedef float f32x4 __attribute__((ext_vector_type(4)));
typedef float f32x2 __attribute__((ext_vector_type(2)));
typedef short bf16x8 __attribute__((ext_vector_type(8)));
typedef __bf16 bf16x2_t __attribute__((ext_vector_type(2)));
#define GAS __attribute__((address_space(1)))
#define LAS __attribute__((address_space(3)))

constexpr int NB = 4, DM = 640, NH = 32, SK = 4096, DH = 128, FF = 1728;
constexpr int NSL = FF / 64;
constexpr int NWG = 256;
constexpr float LOG2E = 1.4426950408889634f;
constexpr float QSCALE = 0.08838834764831845f;

constexpr size_t WS_Y = 0;
constexpr size_t WS_ML = WS_Y + (size_t)NWG * DM;
constexpr size_t WS_F = WS_ML + (size_t)NWG * 2;

constexpr int L_XN = 0;
constexpr int L_QP = L_XN + NB * DM * 2;
constexpr int L_ACC = L_QP + 8 * 128 * 4;
constexpr int L_ML = L_ACC + 8 * 128 * 4;
constexpr int L_CNT = L_ML + 64;
constexpr int L_H = L_CNT + 16;
constexpr int L_BIG = L_H + NB * 64 * 2;
constexpr int L_END = L_BIG + 8 * 2 * DM * 4;

__device__ __forceinline__ float bflo(unsigned u) { return __builtin_bit_cast(float, u << 16); }
__device__ __forceinline__ float bfhi(unsigned u) { return __builtin_bit_cast(float, u & 0xffff0000u); }
__device__ __forceinline__ unsigned pk2(float lo, float hi) {
  bf16x2_t r;
  r[0] = (__bf16)lo;
  r[1] = (__bf16)hi;
  return __builtin_bit_cast(unsigned, r);
}
__device__ __forceinline__ float rbf(float x) { return (float)(__bf16)x; }
__device__ __forceinline__ float dot2(unsigned a, unsigned b, float c) {
  return __builtin_amdgcn_fdot2_f32_bf16(__builtin_bit_cast(bf16x2_t, a), __builtin_bit_cast(bf16x2_t, b), c, false);
}
template <int CTRL> __device__ __forceinline__ float dppf(float x) {
  return __builtin_bit_cast(float, __builtin_amdgcn_mov_dpp(__builtin_bit_cast(int, x), CTRL, 0xf, 0xf, true));
}
template <int CTRL> __device__ __forceinline__ unsigned dppu(unsigned x) {
  return (unsigned)__builtin_amdgcn_mov_dpp((int)x, CTRL, 0xf, 0xf, true);
}
constexpr int XOR1 = 0xB1, XOR2 = 0x4E, XOR7 = 0x141, XOR8 = 0x128, ROR4 = 0x124;
template <int CTRL> __device__ __forceinline__ float tr_merge(float lo, float hi, bool up) {
  const float t1 = lo + dppf<CTRL>(lo);
  const float t2 = hi + dppf<CTRL>(hi);
  return up ? t2 : t1;
}
__device__ __forceinline__ float row16_sum(float x) {
  x += dppf<XOR1>(x); x += dppf<XOR2>(x); x += dppf<XOR7>(x); x += dppf<XOR8>(x);
  return x;
}
__device__ __forceinline__ float row16_max(float x) {
  x = fmaxf(x, dppf<XOR1>(x)); x = fmaxf(x, dppf<XOR2>(x)); x = fmaxf(x, dppf<XOR7>(x)); x = fmaxf(x, dppf<XOR8>(x));
  return x;
}
__device__ __forceinline__ float xrow_max(float x) {
  auto s = __builtin_amdgcn_permlane16_swap(__float_as_uint(x), __float_as_uint(x), false, false);
  x = fmaxf(__uint_as_float(s[0]), __uint_as_float(s[1]));
  auto t = __builtin_amdgcn_permlane32_swap(__float_as_uint(x), __float_as_uint(x), false, false);
  return fmaxf(__uint_as_float(t[0]), __uint_as_float(t[1]));
}
__device__ __forceinline__ float xrow_sum(float x) {
  auto s = __builtin_amdgcn_permlane16_swap(__float_as_uint(x), __float_as_uint(x), false, false);
  x = __uint_as_float(s[0]) + __uint_as_float(s[1]);
  auto t = __builtin_amdgcn_permlane32_swap(__float_as_uint(x), __float_as_uint(x), false, false);
  return __uint_as_float(t[0]) + __uint_as_float(t[1]);
}
__device__ __forceinline__ f32x4 mfma(u32x4 a, u32x4 b, f32x4 c) {
  return __builtin_amdgcn_mfma_f32_16x16x32_bf16(__builtin_bit_cast(bf16x8, a), __builtin_bit_cast(bf16x8, b), c, 0, 0, 0);
}
__device__ __forceinline__ u32x4 ld_nt(const GAS u32x4* p) {
  return __builtin_nontemporal_load(p);
}
#define WG_BARRIER()                                   \
  do {                                                 \
    asm volatile("s_waitcnt lgkmcnt(0)" ::: "memory"); \
    __builtin_amdgcn_s_barrier();                      \
    asm volatile("" ::: "memory");                     \
  } while (0)
#define SB() __builtin_amdgcn_sched_barrier(0)

struct Sel { unsigned s[4]; };
__device__ __forceinline__ Sel make_sel(int lane) {
  const int m = lane & 15;
  const unsigned src = (m & 8) ? 0x0706u : 0x0504u;
  const unsigned placed = (m & 1) ? ((src << 16) | 0x0c0cu) : (0x0c0c0000u | src);
  Sel r;
#pragma unroll
  for (int jj = 0; jj < 4; ++jj) r.s[jj] = (((m & 7) >> 1) == jj) ? placed : 0x0c0c0c0cu;
  return r;
}
__device__ __forceinline__ u32x4 make_a(unsigned X, const Sel& s) {
  u32x4 a;
#pragma unroll
  for (int jj = 0; jj < 4; ++jj) a[jj] = __builtin_amdgcn_perm(X, 0u, s.s[jj]);
  return a;
}
__device__ __forceinline__ unsigned split_hi_lo(float v) {
  const float h = rbf(v);
  return pk2(h, v - h);
}

#define chunk_w(w) ((w) * 64)
#define chunk_i(i) (i)

__device__ __forceinline__ void ln_row(const unsigned (&xr)[5], LAS unsigned* dst, int lane) {
  float sum = 0.f;
#pragma unroll
  for (int k = 0; k < 5; ++k) sum += bflo(xr[k]) + bfhi(xr[k]);
  const float mean = xrow_sum(row16_sum(sum)) * (1.0f / DM);
  float ss = 0.f;
#pragma unroll
  for (int k = 0; k < 5; ++k) {
    const float a = bflo(xr[k]) - mean, bb = bfhi(xr[k]) - mean;
    ss = fmaf(a, a, ss);
    ss = fmaf(bb, bb, ss);
  }
  const float var = xrow_sum(row16_sum(ss)) * (1.0f / DM);
  const float mu = rbf(mean);
  const float rinv = 1.0f / rbf(sqrtf(rbf(rbf(var) + 1e-5f)));
#pragma unroll
  for (int k = 0; k < 5; ++k) {
    dst[lane + 64 * k] = pk2(rbf(bflo(xr[k]) - mu) * rinv, rbf(bfhi(xr[k]) - mu) * rinv);
  }
}

__global__ void __launch_bounds__(1024)
k_main(const GAS unsigned short* __restrict__ x, const GAS unsigned short* __restrict__ kc, const GAS unsigned short* __restrict__ vc,
       const GAS unsigned short* __restrict__ wq, const GAS unsigned short* __restrict__ wo, const GAS unsigned short* __restrict__ wg,
       const GAS unsigned short* __restrict__ wu, const GAS unsigned short* __restrict__ wd, GAS float* __restrict__ ws) {
  __shared__ __attribute__((aligned(16))) unsigned char smem[L_END];
  LAS unsigned char* L = (LAS unsigned char*)smem;
  const int tid = threadIdx.x, lane = tid & 63, wave = __builtin_amdgcn_readfirstlane(tid >> 6);
  const int g = lane >> 4, c = lane & 15;
  const int bid = blockIdx.x;
  const int jx = bid >> 3;
  const int h = (bid & 7) * 4 + (jx >> 3), b = (jx >> 1) & 3, s = jx & 1;
  const int slot = (b * NH + h) * 2 + s;
  const Sel sel = make_sel(lane);

  if (wave >= 8) {
    const int w = wave - 8;
    const size_t base = ((size_t)(b * NH + h) * SK + (size_t)s * (SK / 2)) * DH;
    const __amdgpu_buffer_rsrc_t rk = __builtin_amdgcn_make_buffer_rsrc((void*)(kc + base), 0, SK / 2 * DH * 2, 0x00020000);
    const __amdgpu_buffer_rsrc_t rv = __builtin_amdgcn_make_buffer_rsrc((void*)(vc + base), 0, SK / 2 * DH * 2, 0x00020000);
    const int voff = lane * 16 + chunk_w(w) * 1024;
#define STREAM_LD(n) __builtin_amdgcn_raw_buffer_load_b128((n) < 64 ? rk : rv, voff, chunk_i((n) & 63) * 1024, KV_NT ? 2 : 0)
    u32x4 ring[RING];
#pragma unroll
    for (int i = 0; i < RING; ++i) ring[i] = STREAM_LD(i);
    SB();
    WG_BARRIER();
    u32x4 q;
    {
      f32x4 q0 = {0.f, 0.f, 0.f, 0.f}, q1 = {0.f, 0.f, 0.f, 0.f};
#pragma unroll
      for (int hw = 0; hw < 8; ++hw) {
        q0 += *(const LAS f32x4*)(L + L_QP + (hw * 128 + 8 * c) * 4);
        q1 += *(const LAS f32x4*)(L + L_QP + (hw * 128 + 8 * c + 4) * 4);
      }
      q[0] = pk2(q0[0], q0[1]); q[1] = pk2(q0[2], q0[3]); q[2] = pk2(q1[0], q1[1]); q[3] = pk2(q1[2], q1[3]);
    }
    float sc[4];
#define MERGE(LO, HI, CTRL, BIT) tr_merge<CTRL>((LO), (HI), (c & (BIT)) != 0)
#pragma unroll
    for (int grp = 0; grp < 4; ++grp) {
      float d1 = 0.f, d2 = 0.f, d4 = 0.f, d8 = 0.f;
#pragma unroll
      for (int t = 0; t < 16; ++t) {
        const int i = grp * 16 + t;
        const u32x4 cur = ring[i % RING];
        float a = dot2(cur[0], q[0], 0.f);
        a = dot2(cur[1], q[1], a);
        a = dot2(cur[2], q[2], a);
        a = dot2(cur[3], q[3], a);
        ring[i % RING] = STREAM_LD(i + RING);
        if (!(t & 1)) d1 = a;
        else {
          a = MERGE(d1, a, XOR8, 8);
          if (!(t & 2)) d2 = a;
          else {
            a = MERGE(d2, a, XOR7, 4);
            if (!(t & 4)) d4 = a;
            else {
              a = MERGE(d4, a, XOR2, 2);
              if (!(t & 8)) d8 = a;
              else {
                a = MERGE(d8, a, XOR1, 1);
                sc[grp] = rbf(rbf(a) * QSCALE) * LOG2E;
              }
            }
          }
        }
        SB();
      }
    }
#undef MERGE
    float mw = fmaxf(fmaxf(sc[0], sc[1]), fmaxf(sc[2], sc[3]));
    mw = xrow_max(row16_max(mw));
    unsigned pk[4];
    float lw = 0.f;
#pragma unroll
    for (int grp = 0; grp < 4; ++grp) {
      const float p = __builtin_amdgcn_exp2f(sc[grp] - mw);
      lw += p;
      pk[grp] = split_hi_lo(p);
    }
    lw = xrow_sum(row16_sum(lw));
    if (lane == 0) {
      *(LAS float*)(L + L_ML + w * 4) = mw;
      *(LAS float*)(L + L_ML + 32 + w * 4) = lw;
      asm volatile("s_waitcnt lgkmcnt(0)" ::: "memory");
      __hip_atomic_fetch_add((LAS unsigned*)(L + L_CNT + 4), 1u, __ATOMIC_RELAXED, __HIP_MEMORY_SCOPE_WORKGROUP);
    }
    SB();
    f32x4 acc = {0.f, 0.f, 0.f, 0.f};
    float ew = 0.f;
#define BREV4(T) ((((T) & 1) << 3) | (((T) & 2) << 1) | (((T) & 4) >> 1) | (((T) & 8) >> 3))
#define PV_STEP(T)                                                     \
  {                                                                    \
    const int i = grp * 16 + (T), n = 64 + i;                          \
    const u32x4 cur = ring[n % RING];                                  \
    const unsigned X = dppu<0x150 + BREV4(T)>(pk[grp]);                \
    acc = mfma(make_a(X, sel), cur, acc);                              \
    if (n + RING < 128) ring[n % RING] = STREAM_LD(n + RING);          \
    SB();                                                              \
  }
#pragma unroll
    for (int grp = 0; grp < 4; ++grp) {
      if (grp == 3) {
        while (__hip_atomic_load((LAS unsigned*)(L + L_CNT + 4), __ATOMIC_RELAXED, __HIP_MEMORY_SCOPE_WORKGROUP) < 8u) __builtin_amdgcn_s_sleep(1);
        asm volatile("" ::: "memory");
        const f32x4 m0 = *(const LAS f32x4*)(L + L_ML), m1 = *(const LAS f32x4*)(L + L_ML + 16);
        const float M = fmaxf(fmaxf(fmaxf(m0[0], m0[1]), fmaxf(m0[2], m0[3])), fmaxf(fmaxf(m1[0], m1[1]), fmaxf(m1[2], m1[3])));
        ew = __builtin_amdgcn_exp2f(mw - M);
        if (w == 0) {
          const f32x4 l0 = *(const LAS f32x4*)(L + L_ML + 32), l1 = *(const LAS f32x4*)(L + L_ML + 48);
          float Ls = 0.f;
#pragma unroll
          for (int k = 0; k < 4; ++k) Ls = fmaf(__builtin_amdgcn_exp2f(m0[k] - M), l0[k], Ls);
#pragma unroll
          for (int k = 0; k < 4; ++k) Ls = fmaf(__builtin_amdgcn_exp2f(m1[k] - M), l1[k], Ls);
          if (lane == 0) *(GAS f32x2*)(ws + WS_ML + slot * 2) = f32x2{M, Ls};
        }
        SB();
      }
      PV_STEP(0) PV_STEP(1) PV_STEP(2) PV_STEP(3) PV_STEP(4) PV_STEP(5) PV_STEP(6) PV_STEP(7)
      PV_STEP(8) PV_STEP(9) PV_STEP(10) PV_STEP(11) PV_STEP(12) PV_STEP(13) PV_STEP(14) PV_STEP(15)
    }
#undef PV_STEP
#pragma unroll
    for (int k = 0; k < 4; ++k) {
      auto t = __builtin_amdgcn_permlane32_swap(__float_as_uint(acc[k]), __float_as_uint(acc[k]), false, false);
      acc[k] = (__uint_as_float(t[0]) + __uint_as_float(t[1])) * ew;
    }
    if (g < 2) *(LAS f32x4*)(L + L_ACC + (w * 128 + 8 * c + 4 * g) * 4) = acc;
    WG_BARRIER();
    return;
  }

  const int hw = wave;
  u32x4 wr[20];
  unsigned xr[5];
#pragma unroll
  for (int k = 0; k < 5; ++k) xr[k] = ((const GAS unsigned*)(x + b * DM))[lane + 64 * k];
  {
    const GAS u32x4* p = (const GAS u32x4*)(wq + (size_t)(80 * hw + 20 * g) * (NH * DH) + h * DH + 8 * c);
#pragma unroll
    for (int i = 0; i < 20; ++i) wr[i] = p[(size_t)i * (NH * DH / 8)];
  }
  SB();
  if (tid == 0) *(LAS u32x2*)(L + L_CNT) = u32x2{0u, 0u};
  ln_row(xr, (LAS unsigned*)(L + L_XN + b * DM * 2), lane);
  asm volatile("s_waitcnt lgkmcnt(0)" ::: "memory");
  {
    unsigned xp[10];
#pragma unroll
    for (int k = 0; k < 5; ++k) {
      const u32x2 t = *(const LAS u32x2*)(L + L_XN + (b * DM + 80 * hw + 20 * g + 4 * k) * 2);
      xp[2 * k] = t[0];
      xp[2 * k + 1] = t[1];
    }
    f32x4 acc = {0.f, 0.f, 0.f, 0.f};
#pragma unroll
    for (int i = 0; i < 20; ++i) {
      const unsigned X = (i & 1) ? (xp[i >> 1] >> 16) : (xp[i >> 1] & 0xffffu);
      acc = mfma(make_a(X, sel), wr[i], acc);
    }
    if (g < 2) *(LAS f32x4*)(L + L_QP + (hw * 128 + 8 * c + 4 * g) * 4) = acc;
  }
  WG_BARRIER();

  if (bid < NSL) {
    const int f0 = 64 * bid;
    {
      const GAS unsigned short* wcat = (c < 8) ? wg : wu;
      const GAS u32x4* p = (const GAS u32x4*)(wcat + (size_t)(80 * hw + 20 * g) * FF + f0 + 8 * (c & 7));
#pragma unroll
      for (int i = 0; i < 20; ++i) wr[i] = ld_nt(p + (size_t)i * (FF / 8));
    }
    SB();
    for (int bb = 0; bb < NB; ++bb) {
      unsigned xq[5];
#pragma unroll
      for (int k = 0; k < 5; ++k) xq[k] = ((const GAS unsigned*)(x + bb * DM))[lane + 64 * k];
      ln_row(xq, (LAS unsigned*)(L + L_XN + bb * DM * 2), lane);
    }
    asm volatile("s_waitcnt lgkmcnt(0)" ::: "memory");
    f32x4 ga[2] = {{0.f, 0.f, 0.f, 0.f}, {0.f, 0.f, 0.f, 0.f}};
#pragma unroll
    for (int mf = 0; mf < 2; ++mf) {
      unsigned xp[10];
#pragma unroll
      for (int k = 0; k < 5; ++k) {
        const u32x2 t = *(const LAS u32x2*)(L + L_XN + ((2 * mf + (c >> 3)) * DM + 80 * hw + 20 * g + 4 * k) * 2);
        xp[2 * k] = t[0];
        xp[2 * k + 1] = t[1];
      }
#pragma unroll
      for (int i = 0; i < 20; ++i) {
        const unsigned X = __builtin_amdgcn_perm(xp[i >> 1], 0u, (i & 1) ? 0x07060706u : 0x05040504u);
        ga[mf] = mfma(make_a(X, sel), wr[i], ga[mf]);
      }
      *(LAS f32x4*)(L + L_BIG + ((hw * 4 + 2 * mf + (g >> 1)) * 128 + 8 * c + 4 * (g & 1)) * 4) = ga[mf];
    }
    if (hw < 5) {
      const GAS u32x4* p = (const GAS u32x4*)(wd + (size_t)(f0 + 16 * g) * DM + 128 * hw + 8 * c);
#pragma unroll
      for (int i = 0; i < 16; ++i) wr[i] = ld_nt(p + (size_t)i * (DM / 8));
    }
    SB();
    asm volatile("s_waitcnt lgkmcnt(0)" ::: "memory");
    if (lane == 0) __hip_atomic_fetch_add((LAS unsigned*)(L + L_CNT), 1u, __ATOMIC_RELAXED, __HIP_MEMORY_SCOPE_WORKGROUP);
    if (hw < 5) {
      while (__hip_atomic_load((LAS unsigned*)(L + L_CNT), __ATOMIC_RELAXED, __HIP_MEMORY_SCOPE_WORKGROUP) < 8u) __builtin_amdgcn_s_sleep(2);
      asm volatile("" ::: "memory");
      f32x4 gs = {0.f, 0.f, 0.f, 0.f}, us = {0.f, 0.f, 0.f, 0.f};
#pragma unroll
      for (int w2 = 0; w2 < 8; ++w2) {
        gs += *(const LAS f32x4*)(L + L_BIG + ((w2 * 4 + g) * 128 + 4 * c) * 4);
        us += *(const LAS f32x4*)(L + L_BIG + ((w2 * 4 + g) * 128 + 64 + 4 * c) * 4);
      }
      u32x2 hv;
      hv[0] = pk2(rbf(gs[0]) * rbf(us[0]), rbf(gs[1]) * rbf(us[1]));
      hv[1] = pk2(rbf(gs[2]) * rbf(us[2]), rbf(gs[3]) * rbf(us[3]));
      *(LAS u32x2*)(L + L_H + (g * 64 + 4 * c) * 2) = hv;
      asm volatile("s_waitcnt lgkmcnt(0)" ::: "memory");
#pragma unroll
      for (int mf = 0; mf < 2; ++mf) {
        const u32x4 h0 = *(const LAS u32x4*)(L + L_H + ((2 * mf + (c >> 3)) * 64 + 16 * g) * 2);
        const u32x4 h1 = *(const LAS u32x4*)(L + L_H + ((2 * mf + (c >> 3)) * 64 + 16 * g + 8) * 2);
        f32x4 acc = {0.f, 0.f, 0.f, 0.f};
#pragma unroll
        for (int i = 0; i < 16; ++i) {
          const unsigned pr = (i < 8) ? h0[(i >> 1) & 3] : h1[(i >> 1) & 3];
          const unsigned X = __builtin_amdgcn_perm(pr, 0u, (i & 1) ? 0x07060706u : 0x05040504u);
          acc = mfma(make_a(X, sel), wr[i], acc);
        }
        *(GAS f32x4*)(ws + WS_F + (size_t)(bid * NB + 2 * mf + (g >> 1)) * DM + 128 * hw + 8 * c + 4 * (g & 1)) = acc;
      }
    }
  }

  {
    const GAS u32x4* p = (const GAS u32x4*)(wo + (size_t)(h * DH + 16 * hw + 4 * g) * DM + 8 * c);
#pragma unroll
    for (int j = 0; j < 4; ++j)
#pragma unroll
      for (int cb = 0; cb < 5; ++cb) wr[j * 5 + cb] = p[j * (DM / 8) + cb * 16];
  }
  SB();
  WG_BARRIER();
  {
    f32x4 a8[8];
#pragma unroll
    for (int w = 0; w < 8; ++w) a8[w] = *(const LAS f32x4*)(L + L_ACC + (16 * hw + 4 * g) * 4 + w * 512);
    const f32x4 o = ((a8[0] + a8[1]) + (a8[2] + a8[3])) + ((a8[4] + a8[5]) + (a8[6] + a8[7]));
    f32x4 y[5];
#pragma unroll
    for (int cb = 0; cb < 5; ++cb) y[cb] = f32x4{0.f, 0.f, 0.f, 0.f};
#pragma unroll
    for (int j = 0; j < 4; ++j) {
      const u32x4 a = make_a(split_hi_lo(o[j]), sel);
#pragma unroll
      for (int cb = 0; cb < 5; ++cb) y[cb] = mfma(a, wr[j * 5 + cb], y[cb]);
    }
#pragma unroll
    for (int cb = 0; cb < 5; ++cb)
      *(LAS f32x4*)(L + L_BIG + ((hw * 2 + (g >> 1)) * DM + 128 * cb + 8 * c + 4 * (g & 1)) * 4) = y[cb];
  }
  WG_BARRIER();
  if (tid < DM / 4) {
    f32x4 t = {0.f, 0.f, 0.f, 0.f};
#pragma unroll
    for (int p = 0; p < 16; ++p) t += *(const LAS f32x4*)(L + L_BIG + (p * DM + 4 * tid) * 4);
    *(GAS f32x4*)(ws + WS_Y + (size_t)slot * DM + 4 * tid) = t;
  }
}

__global__ void __launch_bounds__(256)
k_final(const GAS unsigned short* __restrict__ x, const GAS float* __restrict__ ws, GAS unsigned short* __restrict__ out) {
  __shared__ __attribute__((aligned(16))) float red[2][16][64];
  const int tid = threadIdx.x, grp = tid >> 4, c = tid & 15;
  const int b = blockIdx.x / 10, col = (blockIdx.x % 10) * 64 + 4 * c;
  f32x4 ya[4], fa[2];
  f32x4 ml[2];
#pragma unroll
  for (int k = 0; k < 2; ++k) {
    const int slot = (b * NH + 2 * grp + k) * 2;
    ml[k] = *(const GAS f32x4*)(ws + WS_ML + slot * 2);
    ya[2 * k] = *(const GAS f32x4*)(ws + WS_Y + (size_t)slot * DM + col);
    ya[2 * k + 1] = *(const GAS f32x4*)(ws + WS_Y + (size_t)(slot + 1) * DM + col);
  }
  fa[0] = *(const GAS f32x4*)(ws + WS_F + (size_t)(grp * NB + b) * DM + col);
  fa[1] = (grp + 16 < NSL) ? *(const GAS f32x4*)(ws + WS_F + (size_t)((grp + 16) * NB + b) * DM + col) : f32x4{0.f, 0.f, 0.f, 0.f};
  f32x4 a = {0.f, 0.f, 0.f, 0.f};
#pragma unroll
  for (int k = 0; k < 2; ++k) {
    const float M = fmaxf(ml[k][0], ml[k][2]);
    const float e0 = __builtin_amdgcn_exp2f(ml[k][0] - M), e1 = __builtin_amdgcn_exp2f(ml[k][2] - M);
    const float inv = 1.0f / (e0 * ml[k][1] + e1 * ml[k][3]);
    a += ya[2 * k] * (e0 * inv) + ya[2 * k + 1] * (e1 * inv);
  }
  *(f32x4*)&red[0][grp][4 * c] = a;
  *(f32x4*)&red[1][grp][4 * c] = fa[0] + fa[1];
  __syncthreads();
  if (tid < 64) {
    float sa = 0.f, sf = 0.f;
#pragma unroll
    for (int k = 0; k < 16; ++k) { sa += red[0][k][tid]; sf += red[1][k][tid]; }
    const int idx = b * DM + (blockIdx.x % 10) * 64 + tid;
    const float xv = __builtin_bit_cast(float, (unsigned)x[idx] << 16);
    const float r = rbf(xv + rbf(sa)) + rbf(sf);
    out[idx] = (unsigned short)(pk2(r, 0.f) & 0xffffu);
  }
}

extern "C" void kernel_launch(void* const* d_in, const int* in_sizes, int n_in, void* d_out, int out_size, void* d_ws,
                              size_t ws_size, hipStream_t stream) {
  (void)in_sizes; (void)n_in; (void)out_size; (void)ws_size;
  typedef const GAS unsigned short* cp;
  k_main<<<NWG, 1024, 0, stream>>>((cp)d_in[0], (cp)d_in[1], (cp)d_in[2], (cp)d_in[3], (cp)d_in[4], (cp)d_in[6], (cp)d_in[7],
                                   (cp)d_in[8], (GAS float*)d_ws);
  k_final<<<40, 256, 0, stream>>>((cp)d_in[0], (const GAS float*)d_ws, (GAS unsigned short*)d_out);
}
```

```cpp
#include <hip/hip_runtime.h>
#include <hip/hip_bf16.h>
#include <stdint.h>

#define RING 16
#define KV_NT 1
#define CHUNK_MAP 2
#define MIMIC 1

typedef unsigned u32x4 __attribute__((ext_vector_type(4)));
typedef unsigned u32x2 __attribute__((ext_vector_type(2)));
typedef float f32x4 __attribute__((ext_vector_type(4)));
typedef float f32x2 __attribute__((ext_vector_type(2)));
typedef short bf16x8 __attribute__((ext_vector_type(8)));
typedef __bf16 bf16x2_t __attribute__((ext_vector_type(2)));
#define GAS __attribute__((address_space(1)))
#define LAS __attribute__((address_space(3)))

constexpr int NB = 4, DM = 640, NH = 32, SK = 4096, DH = 128, FF = 1728;
constexpr int NSL = FF / 64;
constexpr int NWG = 256;
constexpr float LOG2E = 1.4426950408889634f;
constexpr float QSCALE = 0.08838834764831845f;

constexpr size_t WS_Y = 0;
constexpr size_t WS_ML = WS_Y + (size_t)NWG * DM;
constexpr size_t WS_F = WS_ML + (size_t)NWG * 2;

constexpr int L_XN = 0;
constexpr int L_QP = L_XN + NB * DM * 2;
constexpr int L_ACC = L_QP + 8 * 128 * 4;
constexpr int L_ML = L_ACC + 8 * 128 * 4;
constexpr int L_CNT = L_ML + 64;
constexpr int L_H = L_CNT + 16;
constexpr int L_BIG = L_H + NB * 64 * 2;
constexpr int L_END = L_BIG + 8 * 2 * DM * 4;

__device__ __forceinline__ float bflo(unsigned u) { return __builtin_bit_cast(float, u << 16); }
__device__ __forceinline__ float bfhi(unsigned u) { return __builtin_bit_cast(float, u & 0xffff0000u); }
__device__ __forceinline__ unsigned pk2(float lo, float hi) {
  bf16x2_t r;
  r[0] = (__bf16)lo;
  r[1] = (__bf16)hi;
  return __builtin_bit_cast(unsigned, r);
}
__device__ __forceinline__ float rbf(float x) { return (float)(__bf16)x; }
__device__ __forceinline__ float dot2(unsigned a, unsigned b, float c) {
  return __builtin_amdgcn_fdot2_f32_bf16(__builtin_bit_cast(bf16x2_t, a), __builtin_bit_cast(bf16x2_t, b), c, false);
}
template <int CTRL> __device__ __forceinline__ float dppf(float x) {
  return __builtin_bit_cast(float, __builtin_amdgcn_mov_dpp(__builtin_bit_cast(int, x), CTRL, 0xf, 0xf, true));
}
template <int CTRL> __device__ __forceinline__ unsigned dppu(unsigned x) {
  return (unsigned)__builtin_amdgcn_mov_dpp((int)x, CTRL, 0xf, 0xf, true);
}
constexpr int XOR1 = 0xB1, XOR2 = 0x4E, XOR7 = 0x141, XOR8 = 0x128, ROR4 = 0x124;
template <int CTRL> __device__ __forceinline__ float tr_merge(float lo, float hi, bool up) {
  const float t1 = lo + dppf<CTRL>(lo);
  const float t2 = hi + dppf<CTRL>(hi);
  return up ? t2 : t1;
}
__device__ __forceinline__ float row16_sum(float x) {
  x += dppf<XOR1>(x); x += dppf<XOR2>(x); x += dppf<XOR7>(x); x += dppf<XOR8>(x);
  return x;
}
__device__ __forceinline__ float row16_max(float x) {
  x = fmaxf(x, dppf<XOR1>(x)); x = fmaxf(x, dppf<XOR2>(x)); x = fmaxf(x, dppf<XOR7>(x)); x = fmaxf(x, dppf<XOR8>(x));
  return x;
}
__device__ __forceinline__ float xrow_max(float x) {
  auto s = __builtin_amdgcn_permlane16_swap(__float_as_uint(x), __float_as_uint(x), false, false);
  x = fmaxf(__uint_as_float(s[0]), __uint_as_float(s[1]));
  auto t = __builtin_amdgcn_permlane32_swap(__float_as_uint(x), __float_as_uint(x), false, false);
  return fmaxf(__uint_as_float(t[0]), __uint_as_float(t[1]));
}
__device__ __forceinline__ float xrow_sum(float x) {
  auto s = __builtin_amdgcn_permlane16_swap(__float_as_uint(x), __float_as_uint(x), false, false);
  x = __uint_as_float(s[0]) + __uint_as_float(s[1]);
  auto t = __builtin_amdgcn_permlane32_swap(__float_as_uint(x), __float_as_uint(x), false, false);
  return __uint_as_float(t[0]) + __uint_as_float(t[1]);
}
__device__ __forceinline__ f32x4 mfma(u32x4 a, u32x4 b, f32x4 c) {
  return __builtin_amdgcn_mfma_f32_16x16x32_bf16(__builtin_bit_cast(bf16x8, a), __builtin_bit_cast(bf16x8, b), c, 0, 0, 0);
}
__device__ __forceinline__ u32x4 ld_nt(const GAS u32x4* p) {
  return __builtin_nontemporal_load(p);
}
#define WG_BARRIER()                                   \
  do {                                                 \
    asm volatile("s_waitcnt lgkmcnt(0)" ::: "memory"); \
    __builtin_amdgcn_s_barrier();                      \
    asm volatile("" ::: "memory");                     \
  } while (0)
#define SB() __builtin_amdgcn_sched_barrier(0)
#define STAMP(wid, k)

struct Sel { unsigned s[4]; };
__device__ __forceinline__ Sel make_sel(int lane) {
  const int m = lane & 15;
  const unsigned src = (m & 8) ? 0x0706u : 0x0504u;
  const unsigned placed = (m & 1) ? ((src << 16) | 0x0c0cu) : (0x0c0c0000u | src);
  Sel r;
#pragma unroll
  for (int jj = 0; jj < 4; ++jj) r.s[jj] = (((m & 7) >> 1) == jj) ? placed : 0x0c0c0c0cu;
  return r;
}
__device__ __forceinline__ u32x4 make_a(unsigned X, const Sel& s) {
  u32x4 a;
#pragma unroll
  for (int jj = 0; jj < 4; ++jj) a[jj] = __builtin_amdgcn_perm(X, 0u, s.s[jj]);
  return a;
}
__device__ __forceinline__ unsigned split_hi_lo(float v) {
  const float h = rbf(v);
  return pk2(h, v - h);
}

#define chunk_w(w) ((w) * 4)
#define chunk_i(i) (((i) >> 2) * 32 + ((i) & 3))

__device__ __forceinline__ void ln_row(const unsigned (&xr)[5], LAS unsigned* dst, int lane) {
  float sum = 0.f;
#pragma unroll
  for (int k = 0; k < 5; ++k) sum += bflo(xr[k]) + bfhi(xr[k]);
  const float mean = xrow_sum(row16_sum(sum)) * (1.0f / DM);
  float ss = 0.f;
#pragma unroll
  for (int k = 0; k < 5; ++k) {
    const float a = bflo(xr[k]) - mean, bb = bfhi(xr[k]) - mean;
    ss = fmaf(a, a, ss);
    ss = fmaf(bb, bb, ss);
  }
  const float var = xrow_sum(row16_sum(ss)) * (1.0f / DM);
  const float mu = rbf(mean);
  const float rinv = 1.0f / rbf(sqrtf(rbf(rbf(var) + 1e-5f)));
#pragma unroll
  for (int k = 0; k < 5; ++k) {
    dst[lane + 64 * k] = pk2(rbf(bflo(xr[k]) - mu) * rinv, rbf(bfhi(xr[k]) - mu) * rinv);
  }
}

__global__ void __launch_bounds__(1024)
k_main(const GAS unsigned short* __restrict__ x, const GAS unsigned short* __restrict__ kc, const GAS unsigned short* __restrict__ vc,
       const GAS unsigned short* __restrict__ wq, const GAS unsigned short* __restrict__ wo, const GAS unsigned short* __restrict__ wg,
       const GAS unsigned short* __restrict__ wu, const GAS unsigned short* __restrict__ wd, GAS float* __restrict__ ws) {
  __shared__ __attribute__((aligned(16))) unsigned char smem[L_END];
  LAS unsigned char* L = (LAS unsigned char*)smem;
  const int tid = threadIdx.x, lane = tid & 63, wave = __builtin_amdgcn_readfirstlane(tid >> 6);
  const int g = lane >> 4, c = lane & 15;
  const int bid = blockIdx.x;
  const int jx = bid >> 3;
  const int h = (bid & 7) * 4 + (jx >> 3), b = (jx >> 1) & 3, s = jx & 1;
  const int slot = (b * NH + h) * 2 + s;
  STAMP(bid * 16 + wave, 0);
  const Sel sel = make_sel(lane);

  if (wave >= 8) {
    const int w = wave - 8;
    const size_t base = ((size_t)(b * NH + h) * SK + (size_t)s * (SK / 2)) * DH;
    const __amdgpu_buffer_rsrc_t rk = __builtin_amdgcn_make_buffer_rsrc((void*)(kc + base), 0, SK / 2 * DH * 2, 0x00020000);
    const __amdgpu_buffer_rsrc_t rv = __builtin_amdgcn_make_buffer_rsrc((void*)(vc + base), 0, SK / 2 * DH * 2, 0x00020000);
    const int voff = lane * 16 + chunk_w(w) * 1024;
#define STREAM_LD(n) __builtin_amdgcn_raw_buffer_load_b128((n) < 64 ? rk : rv, voff, chunk_i((n) & 63) * 1024, KV_NT ? 2 : 0)
    u32x4 ring[RING];
#pragma unroll
    for (int i = 0; i < RING; ++i) ring[i] = STREAM_LD(i);
    SB();
    WG_BARRIER();
    STAMP(bid * 16 + wave, 1);
    u32x4 q;
    {
      f32x4 q0 = {0.f, 0.f, 0.f, 0.f}, q1 = {0.f, 0.f, 0.f, 0.f};
#pragma unroll
      for (int hw = 0; hw < 8; ++hw) {
        q0 += *(const LAS f32x4*)(L + L_QP + (hw * 128 + 8 * c) * 4);
        q1 += *(const LAS f32x4*)(L + L_QP + (hw * 128 + 8 * c + 4) * 4);
      }
      q[0] = pk2(q0[0], q0[1]); q[1] = pk2(q0[2], q0[3]); q[2] = pk2(q1[0], q1[1]); q[3] = pk2(q1[2], q1[3]);
    }
    float sc[4];
#define MERGE(LO, HI, CTRL, BIT) tr_merge<CTRL>((LO), (HI), (c & (BIT)) != 0)
#pragma unroll
    for (int grp = 0; grp < 4; ++grp) {
      float d1 = 0.f, d2 = 0.f, d4 = 0.f, d8 = 0.f;
#pragma unroll
      for (int t = 0; t < 16; ++t) {
        const int i = grp * 16 + t;
        const u32x4 cur = ring[i % RING];
        float a = dot2(cur[0], q[0], 0.f);
        a = dot2(cur[1], q[1], a);
        a = dot2(cur[2], q[2], a);
        a = dot2(cur[3], q[3], a);
        ring[i % RING] = STREAM_LD(i + RING);
        if (!(t & 1)) d1 = a;
        else {
          a = MERGE(d1, a, XOR8, 8);
          if (!(t & 2)) d2 = a;
          else {
            a = MERGE(d2, a, XOR7, 4);
            if (!(t & 4)) d4 = a;
            else {
              a = MERGE(d4, a, XOR2, 2);
              if (!(t & 8)) d8 = a;
              else {
                a = MERGE(d8, a, XOR1, 1);
                sc[grp] = rbf(rbf(a) * QSCALE) * LOG2E;
              }
            }
          }
        }
        SB();
      }
    }
#undef MERGE
    STAMP(bid * 16 + wave, 2);
    float mw = fmaxf(fmaxf(sc[0], sc[1]), fmaxf(sc[2], sc[3]));
    mw = xrow_max(row16_max(mw));
    unsigned pk[4];
    float lw = 0.f;
#pragma unroll
    for (int grp = 0; grp < 4; ++grp) {
      const float p = __builtin_amdgcn_exp2f(sc[grp] - mw);
      lw += p;
      pk[grp] = split_hi_lo(p);
    }
    lw = xrow_sum(row16_sum(lw));
    if (lane == 0) {
      *(LAS float*)(L + L_ML + w * 4) = mw;
      *(LAS float*)(L + L_ML + 32 + w * 4) = lw;
      asm volatile("s_waitcnt lgkmcnt(0)" ::: "memory");
      __hip_atomic_fetch_add((LAS unsigned*)(L + L_CNT + 4), 1u, __ATOMIC_RELAXED, __HIP_MEMORY_SCOPE_WORKGROUP);
    }
    SB();
    f32x4 acc = {0.f, 0.f, 0.f, 0.f};
    float ew = 0.f;
#define BREV4(T) ((((T) & 1) << 3) | (((T) & 2) << 1) | (((T) & 4) >> 1) | (((T) & 8) >> 3))
#define PV_STEP(T)                                                     \
  {                                                                    \
    const int i = grp * 16 + (T), n = 64 + i;                          \
    const u32x4 cur = ring[n % RING];                                  \
    const unsigned X = dppu<0x150 + BREV4(T)>(pk[grp]);                \
    acc = mfma(make_a(X, sel), cur, acc);                              \
    if (n + RING < 128) ring[n % RING] = STREAM_LD(n + RING);          \
    SB();                                                              \
  }
#pragma unroll
    for (int grp = 0; grp < 4; ++grp) {
      if (grp == 3) {
        while (__hip_atomic_load((LAS unsigned*)(L + L_CNT + 4), __ATOMIC_RELAXED, __HIP_MEMORY_SCOPE_WORKGROUP) < 8u) __builtin_amdgcn_s_sleep(1);
        asm volatile("" ::: "memory");
        const f32x4 m0 = *(const LAS f32x4*)(L + L_ML), m1 = *(const LAS f32x4*)(L + L_ML + 16);
        const float M = fmaxf(fmaxf(fmaxf(m0[0], m0[1]), fmaxf(m0[2], m0[3])), fmaxf(fmaxf(m1[0], m1[1]), fmaxf(m1[2], m1[3])));
        ew = __builtin_amdgcn_exp2f(mw - M);
        if (w == 0) {
          const f32x4 l0 = *(const LAS f32x4*)(L + L_ML + 32), l1 = *(const LAS f32x4*)(L + L_ML + 48);
          float Ls = 0.f;
#pragma unroll
          for (int k = 0; k < 4; ++k) Ls = fmaf(__builtin_amdgcn_exp2f(m0[k] - M), l0[k], Ls);
#pragma unroll
          for (int k = 0; k < 4; ++k) Ls = fmaf(__builtin_amdgcn_exp2f(m1[k] - M), l1[k], Ls);
          if (lane == 0) *(GAS f32x2*)(ws + WS_ML + slot * 2) = f32x2{M, Ls};
        }
        SB();
      }
      PV_STEP(0) PV_STEP(1) PV_STEP(2) PV_STEP(3) PV_STEP(4) PV_STEP(5) PV_STEP(6) PV_STEP(7)
      PV_STEP(8) PV_STEP(9) PV_STEP(10) PV_STEP(11) PV_STEP(12) PV_STEP(13) PV_STEP(14) PV_STEP(15)
    }
#undef PV_STEP
    STAMP(bid * 16 + wave, 3);
#pragma unroll
    for (int k = 0; k < 4; ++k) {
      auto t = __builtin_amdgcn_permlane32_swap(__float_as_uint(acc[k]), __float_as_uint(acc[k]), false, false);
      acc[k] = (__uint_as_float(t[0]) + __uint_as_float(t[1])) * ew;
    }
    if (g < 2) *(LAS f32x4*)(L + L_ACC + (w * 128 + 8 * c + 4 * g) * 4) = acc;
    WG_BARRIER();
    return;
  }

  const int hw = wave;
  u32x4 wr[20];
  unsigned xr[5];
#pragma unroll
  for (int k = 0; k < 5; ++k) xr[k] = ((const GAS unsigned*)(x + b * DM))[lane + 64 * k];
  {
    const GAS u32x4* p = (const GAS u32x4*)(wq + (size_t)(80 * hw + 20 * g) * (NH * DH) + h * DH + 8 * c);
#pragma unroll
    for (int i = 0; i < 20; ++i) wr[i] = p[(size_t)i * (NH * DH / 8)];
  }
  SB();
  if (tid == 0) *(LAS u32x2*)(L + L_CNT) = u32x2{0u, 0u};
  ln_row(xr, (LAS unsigned*)(L + L_XN + b * DM * 2), lane);
  asm volatile("s_waitcnt lgkmcnt(0)" ::: "memory");
  {
    unsigned xp[10];
#pragma unroll
    for (int k = 0; k < 5; ++k) {
      const u32x2 t = *(const LAS u32x2*)(L + L_XN + (b * DM + 80 * hw + 20 * g + 4 * k) * 2);
      xp[2 * k] = t[0];
      xp[2 * k + 1] = t[1];
    }
    f32x4 acc = {0.f, 0.f, 0.f, 0.f};
#pragma unroll
    for (int i = 0; i < 20; ++i) {
      const unsigned X = (i & 1) ? (xp[i >> 1] >> 16) : (xp[i >> 1] & 0xffffu);
      acc = mfma(make_a(X, sel), wr[i], acc);
    }
    if (g < 2) *(LAS f32x4*)(L + L_QP + (hw * 128 + 8 * c + 4 * g) * 4) = acc;
  }
  STAMP(bid * 16 + wave, 1);
  WG_BARRIER();

  if (bid < NSL) {
    const int f0 = 64 * bid;
    {
      const GAS unsigned short* wcat = (c < 8) ? wg : wu;
      const GAS u32x4* p = (const GAS u32x4*)(wcat + (size_t)(80 * hw + 20 * g) * FF + f0 + 8 * (c & 7));
#pragma unroll
      for (int i = 0; i < 20; ++i) wr[i] = ld_nt(p + (size_t)i * (FF / 8));
    }
    SB();
    for (int bb = 0; bb < NB; ++bb) {
      unsigned xq[5];
#pragma unroll
      for (int k = 0; k < 5; ++k) xq[k] = ((const GAS unsigned*)(x + bb * DM))[lane + 64 * k];
      ln_row(xq, (LAS unsigned*)(L + L_XN + bb * DM * 2), lane);
    }
    asm volatile("s_waitcnt lgkmcnt(0)" ::: "memory");
    f32x4 ga[2] = {{0.f, 0.f, 0.f, 0.f}, {0.f, 0.f, 0.f, 0.f}};
#pragma unroll
    for (int mf = 0; mf < 2; ++mf) {
      unsigned xp[10];
#pragma unroll
      for (int k = 0; k < 5; ++k) {
        const u32x2 t = *(const LAS u32x2*)(L + L_XN + ((2 * mf + (c >> 3)) * DM + 80 * hw + 20 * g + 4 * k) * 2);
        xp[2 * k] = t[0];
        xp[2 * k + 1] = t[1];
      }
#pragma unroll
      for (int i = 0; i < 20; ++i) {
        const unsigned X = __builtin_amdgcn_perm(xp[i >> 1], 0u, (i & 1) ? 0x07060706u : 0x05040504u);
        ga[mf] = mfma(make_a(X, sel), wr[i], ga[mf]);
      }
      *(LAS f32x4*)(L + L_BIG + ((hw * 4 + 2 * mf + (g >> 1)) * 128 + 8 * c + 4 * (g & 1)) * 4) = ga[mf];
    }
    if (hw < 5) {
      const GAS u32x4* p = (const GAS u32x4*)(wd + (size_t)(f0 + 16 * g) * DM + 128 * hw + 8 * c);
#pragma unroll
      for (int i = 0; i < 16; ++i) wr[i] = ld_nt(p + (size_t)i * (DM / 8));
    }
    SB();
    asm volatile("s_waitcnt lgkmcnt(0)" ::: "memory");
    if (lane == 0) __hip_atomic_fetch_add((LAS unsigned*)(L + L_CNT), 1u, __ATOMIC_RELAXED, __HIP_MEMORY_SCOPE_WORKGROUP);
    if (hw < 5) {
      while (__hip_atomic_load((LAS unsigned*)(L + L_CNT), __ATOMIC_RELAXED, __HIP_MEMORY_SCOPE_WORKGROUP) < 8u) __builtin_amdgcn_s_sleep(2);
      asm volatile("" ::: "memory");
      f32x4 gs = {0.f, 0.f, 0.f, 0.f}, us = {0.f, 0.f, 0.f, 0.f};
#pragma unroll
      for (int w2 = 0; w2 < 8; ++w2) {
        gs += *(const LAS f32x4*)(L + L_BIG + ((w2 * 4 + g) * 128 + 4 * c) * 4);
        us += *(const LAS f32x4*)(L + L_BIG + ((w2 * 4 + g) * 128 + 64 + 4 * c) * 4);
      }
      u32x2 hv;
      hv[0] = pk2(rbf(gs[0]) * rbf(us[0]), rbf(gs[1]) * rbf(us[1]));
      hv[1] = pk2(rbf(gs[2]) * rbf(us[2]), rbf(gs[3]) * rbf(us[3]));
      *(LAS u32x2*)(L + L_H + (g * 64 + 4 * c) * 2) = hv;
      asm volatile("s_waitcnt lgkmcnt(0)" ::: "memory");
#pragma unroll
      for (int mf = 0; mf < 2; ++mf) {
        const u32x4 h0 = *(const LAS u32x4*)(L + L_H + ((2 * mf + (c >> 3)) * 64 + 16 * g) * 2);
        const u32x4 h1 = *(const LAS u32x4*)(L + L_H + ((2 * mf + (c >> 3)) * 64 + 16 * g + 8) * 2);
        f32x4 acc = {0.f, 0.f, 0.f, 0.f};
#pragma unroll
        for (int i = 0; i < 16; ++i) {
          const unsigned pr = (i < 8) ? h0[(i >> 1) & 3] : h1[(i >> 1) & 3];
          const unsigned X = __builtin_amdgcn_perm(pr, 0u, (i & 1) ? 0x07060706u : 0x05040504u);
          acc = mfma(make_a(X, sel), wr[i], acc);
        }
        *(GAS f32x4*)(ws + WS_F + (size_t)(bid * NB + 2 * mf + (g >> 1)) * DM + 128 * hw + 8 * c + 4 * (g & 1)) = acc;
      }
    }
  }

  STAMP(bid * 16 + wave, 2);
  {
    const GAS u32x4* p = (const GAS u32x4*)(wo + (size_t)(h * DH + 16 * hw + 4 * g) * DM + 8 * c);
#pragma unroll
    for (int j = 0; j < 4; ++j)
#pragma unroll
      for (int cb = 0; cb < 5; ++cb) wr[j * 5 + cb] = p[j * (DM / 8) + cb * 16];
  }
  SB();
  WG_BARRIER();
  STAMP(bid * 16 + wave, 3);
  {
    f32x4 a8[8];
#pragma unroll
    for (int w = 0; w < 8; ++w) a8[w] = *(const LAS f32x4*)(L + L_ACC + (16 * hw + 4 * g) * 4 + w * 512);
    const f32x4 o = ((a8[0] + a8[1]) + (a8[2] + a8[3])) + ((a8[4] + a8[5]) + (a8[6] + a8[7]));
    f32x4 y[5];
#pragma unroll
    for (int cb = 0; cb < 5; ++cb) y[cb] = f32x4{0.f, 0.f, 0.f, 0.f};
#pragma unroll
    for (int j = 0; j < 4; ++j) {
      const u32x4 a = make_a(split_hi_lo(o[j]), sel);
#pragma unroll
      for (int cb = 0; cb < 5; ++cb) y[cb] = mfma(a, wr[j * 5 + cb], y[cb]);
    }
#pragma unroll
    for (int cb = 0; cb < 5; ++cb)
      *(LAS f32x4*)(L + L_BIG + ((hw * 2 + (g >> 1)) * DM + 128 * cb + 8 * c + 4 * (g & 1)) * 4) = y[cb];
  }
  WG_BARRIER();
  if (tid < DM / 4) {
    f32x4 t = {0.f, 0.f, 0.f, 0.f};
#pragma unroll
    for (int p = 0; p < 16; ++p) t += *(const LAS f32x4*)(L + L_BIG + (p * DM + 4 * tid) * 4);
    *(GAS f32x4*)(ws + WS_Y + (size_t)slot * DM + 4 * tid) = t;
  }
  STAMP(bid * 16 + wave, 4);
}

__global__ void __launch_bounds__(256)
k_final(const GAS unsigned short* __restrict__ x, GAS float* __restrict__ ws, GAS unsigned short* __restrict__ out) {
  __shared__ __attribute__((aligned(16))) float red[2][16][64];
  const int tid = threadIdx.x, grp = tid >> 4, c = tid & 15;
  const int b = blockIdx.x / 10, col = (blockIdx.x % 10) * 64 + 4 * c;
  STAMP(4096 + blockIdx.x * 4 + (tid >> 6), 0);
  f32x4 ya[4], fa[2];
  f32x4 ml[2];
#pragma unroll
  for (int k = 0; k < 2; ++k) {
    const int slot = (b * NH + 2 * grp + k) * 2;
    ml[k] = *(const GAS f32x4*)(ws + WS_ML + slot * 2);
    ya[2 * k] = *(const GAS f32x4*)(ws + WS_Y + (size_t)slot * DM + col);
    ya[2 * k + 1] = *(const GAS f32x4*)(ws + WS_Y + (size_t)(slot + 1) * DM + col);
  }
  fa[0] = *(const GAS f32x4*)(ws + WS_F + (size_t)(grp * NB + b) * DM + col);
  fa[1] = (grp + 16 < NSL) ? *(const GAS f32x4*)(ws + WS_F + (size_t)((grp + 16) * NB + b) * DM + col) : f32x4{0.f, 0.f, 0.f, 0.f};
  f32x4 a = {0.f, 0.f, 0.f, 0.f};
#pragma unroll
  for (int k = 0; k < 2; ++k) {
    const float M = fmaxf(ml[k][0], ml[k][2]);
    const float e0 = __builtin_amdgcn_exp2f(ml[k][0] - M), e1 = __builtin_amdgcn_exp2f(ml[k][2] - M);
    const float inv = 1.0f / (e0 * ml[k][1] + e1 * ml[k][3]);
    a += ya[2 * k] * (e0 * inv) + ya[2 * k + 1] * (e1 * inv);
  }
  *(f32x4*)&red[0][grp][4 * c] = a;
  *(f32x4*)&red[1][grp][4 * c] = fa[0] + fa[1];
  __syncthreads();
  if (tid < 64) {
    float sa = 0.f, sf = 0.f;
#pragma unroll
    for (int k = 0; k < 16; ++k) { sa += red[0][k][tid]; sf += red[1][k][tid]; }
    const int idx = b * DM + (blockIdx.x % 10) * 64 + tid;
    const float xv = __builtin_bit_cast(float, (unsigned)x[idx] << 16);
    const float r = rbf(xv + rbf(sa)) + rbf(sf);
    out[idx] = (unsigned short)(pk2(r, 0.f) & 0xffffu);
  }
  STAMP(4096 + blockIdx.x * 4 + (tid >> 6), 1);
}

extern "C" void kernel_launch(void* const* d_in, const int* in_sizes, int n_in, void* d_out, int out_size, void* d_ws,
                              size_t ws_size, hipStream_t stream) {
  (void)in_sizes; (void)n_in; (void)out_size; (void)ws_size;
  typedef const GAS unsigned short* cp;
  k_main<<<NWG, 1024, 0, stream>>>((cp)d_in[0], (cp)d_in[1], (cp)d_in[2], (cp)d_in[3], (cp)d_in[4], (cp)d_in[6], (cp)d_in[7],
                                   (cp)d_in[8], (GAS float*)d_ws);
  k_final<<<40, 256, 0, stream>>>((cp)d_in[0], (GAS float*)d_ws, (GAS unsigned short*)d_out);
}
```

```cpp
#include <hip/hip_runtime.h>
#include <hip/hip_bf16.h>
#include <stdint.h>

#define RING 8
#define DEPTH 4
#define WO_DEPTH 2
#define Q_DELAY 8
#define POLL_N1 2
#define POLL_S1 8
#define POLL_N2 8
#define POLL_S2 32
#define ABL_WO_BYTES (16 * DM * 2)
#define ABL_WQ_BYTES (80 * NH * DH * 2)
#define ABL_GU_N 20
#define ABL_PUB 1
#define ABL_WD 1
#define ABL_FINAL 1
#define GU_DEPTH 1
#define GLU_MEM 0
#define S_PRIO 2
#define W_NT 2
#define WO_SLEEP 0

typedef unsigned u32x4 __attribute__((ext_vector_type(4)));
typedef unsigned u32x2 __attribute__((ext_vector_type(2)));
typedef float f32x4 __attribute__((ext_vector_type(4)));
typedef float f32x2 __attribute__((ext_vector_type(2)));
typedef short bf16x8 __attribute__((ext_vector_type(8)));
typedef __bf16 bf16x2_t __attribute__((ext_vector_type(2)));
typedef unsigned long long u64;
#define GAS __attribute__((address_space(1)))
#define LAS __attribute__((address_space(3)))
#define RLX_AGENT __ATOMIC_RELAXED, __HIP_MEMORY_SCOPE_AGENT
#define RLX_WG __ATOMIC_RELAXED, __HIP_MEMORY_SCOPE_WORKGROUP

constexpr int NB = 4, DM = 640, NH = 32, SK = 4096, DH = 128, FF = 1728;
constexpr int NSL = FF / 64;
constexpr int NWG = 256;
constexpr float LOG2E = 1.4426950408889634f;
constexpr float QSCALE = 0.08838834764831845f;
constexpr unsigned SPIN_CAP = 1u << 18;

constexpr size_t G_E = 0;
constexpr size_t G_FL = 32;
constexpr size_t G_ML = G_FL + NB * 64;
constexpr size_t G_Y = G_ML + (size_t)NWG * 16;

constexpr int L_XN = 0;
constexpr int L_XQ = L_XN + NB * DM * 2;
constexpr int L_Q = L_XQ + DM * 2;
constexpr int ACC_LD = 132;
constexpr int L_ACC = L_Q + 8 * 128 * 4;
constexpr int L_ML = L_ACC + 16 * ACC_LD * 4;
constexpr int L_EW = L_ML + 64;
constexpr int L_FLAG = L_EW + 32;
constexpr int L_H = L_FLAG + 32;
constexpr int L_BIG = L_H + 64;
constexpr int L_GU = L_BIG + 8 * DM * 4;
constexpr int L_END = L_BIG + 8 * 2 * DM * 4;

__device__ __forceinline__ float bflo(unsigned u) { return __builtin_bit_cast(float, u << 16); }
__device__ __forceinline__ float bfhi(unsigned u) { return __builtin_bit_cast(float, u & 0xffff0000u); }
__device__ __forceinline__ unsigned pk2(float lo, float hi) {
  bf16x2_t r;
  r[0] = (__bf16)lo;
  r[1] = (__bf16)hi;
  return __builtin_bit_cast(unsigned, r);
}
__device__ __forceinline__ float rbf(float x) { return (float)(__bf16)x; }
__device__ __forceinline__ float dot2(unsigned a, unsigned b, float c) {
  return __builtin_amdgcn_fdot2_f32_bf16(__builtin_bit_cast(bf16x2_t, a), __builtin_bit_cast(bf16x2_t, b), c, false);
}
template <int CTRL> __device__ __forceinline__ float dppf(float x) {
  return __builtin_bit_cast(float, __builtin_amdgcn_mov_dpp(__builtin_bit_cast(int, x), CTRL, 0xf, 0xf, true));
}
template <int CTRL> __device__ __forceinline__ unsigned dppu(unsigned x) {
  return (unsigned)__builtin_amdgcn_mov_dpp((int)x, CTRL, 0xf, 0xf, true);
}
constexpr int XOR1 = 0xB1, XOR2 = 0x4E, XOR7 = 0x141, XOR8 = 0x128;
template <int CTRL> __device__ __forceinline__ float tr_merge(float lo, float hi, bool up) {
  const float t1 = lo + dppf<CTRL>(lo);
  const float t2 = hi + dppf<CTRL>(hi);
  return up ? t2 : t1;
}
__device__ __forceinline__ float row16_sum(float x) {
  x += dppf<XOR1>(x); x += dppf<XOR2>(x); x += dppf<XOR7>(x); x += dppf<XOR8>(x);
  return x;
}
__device__ __forceinline__ float row16_max(float x) {
  x = fmaxf(x, dppf<XOR1>(x)); x = fmaxf(x, dppf<XOR2>(x)); x = fmaxf(x, dppf<XOR7>(x)); x = fmaxf(x, dppf<XOR8>(x));
  return x;
}
__device__ __forceinline__ float half_sum(float x) {
  auto t = __builtin_amdgcn_permlane32_swap(__float_as_uint(x), __float_as_uint(x), false, false);
  return __uint_as_float(t[0]) + __uint_as_float(t[1]);
}
__device__ __forceinline__ float xrow_max(float x) {
  auto s = __builtin_amdgcn_permlane16_swap(__float_as_uint(x), __float_as_uint(x), false, false);
  x = fmaxf(__uint_as_float(s[0]), __uint_as_float(s[1]));
  auto t = __builtin_amdgcn_permlane32_swap(__float_as_uint(x), __float_as_uint(x), false, false);
  return fmaxf(__uint_as_float(t[0]), __uint_as_float(t[1]));
}
__device__ __forceinline__ float xrow_sum(float x) {
  auto s = __builtin_amdgcn_permlane16_swap(__float_as_uint(x), __float_as_uint(x), false, false);
  x = __uint_as_float(s[0]) + __uint_as_float(s[1]);
  return half_sum(x);
}
__device__ __forceinline__ f32x4 mfma(u32x4 a, u32x4 b, f32x4 c) {
  return __builtin_amdgcn_mfma_f32_16x16x32_bf16(__builtin_bit_cast(bf16x8, a), __builtin_bit_cast(bf16x8, b), c, 0, 0, 0);
}
#define LGKM0() asm volatile("s_waitcnt lgkmcnt(0)" ::: "memory")
#define WG_BARRIER()                \
  do {                              \
    LGKM0();                        \
    __builtin_amdgcn_s_barrier();   \
    asm volatile("" ::: "memory");  \
  } while (0)
#define SB() __builtin_amdgcn_sched_barrier(0)
#define STAMP(k)
#define STAMP_DECL
#define STAMP_OUT()
#define LDW(p) __builtin_nontemporal_load(p)
#define VMWAIT(n) asm volatile("s_waitcnt vmcnt(%0)" ::"i"(n) : "memory")

__device__ __forceinline__ void put(GAS u64* p, float v, unsigned tag) {
  __hip_atomic_store(p, ((u64)tag << 32) | (u64)__float_as_uint(v), RLX_AGENT);
}
__device__ __forceinline__ u64 get(const GAS u64* p) { return __hip_atomic_load(p, RLX_AGENT); }
__device__ __forceinline__ unsigned lds_ld(LAS unsigned char* p) { return __hip_atomic_load((LAS unsigned*)p, RLX_WG); }
__device__ __forceinline__ void lds_wait_ge(LAS unsigned char* p, unsigned want) {
  while (lds_ld(p) < want) __builtin_amdgcn_s_sleep(1);
  asm volatile("" ::: "memory");
}
template <int N, int SLP> __device__ __forceinline__ void sweep(const GAS u64* p, int S, unsigned tag, float (&v)[N]) {
  for (unsigned spins = 0;; ++spins) {
    bool ok = true;
#pragma unroll
    for (int k = 0; k < N; ++k) {
      const u64 x = get(p + k * S);
      v[k] = __uint_as_float((unsigned)x);
      ok &= (unsigned)(x >> 32) == tag;
    }
    if (__all(ok) || spins > SPIN_CAP) return;
    __builtin_amdgcn_s_sleep(SLP);
  }
}

struct Sel { unsigned s[4]; };
__device__ __forceinline__ Sel make_sel(int lane) {
  const int m = lane & 15;
  const unsigned src = (m & 8) ? 0x0706u : 0x0504u;
  const unsigned placed = (m & 1) ? ((src << 16) | 0x0c0cu) : (0x0c0c0000u | src);
  Sel r;
#pragma unroll
  for (int jj = 0; jj < 4; ++jj) r.s[jj] = (((m & 7) >> 1) == jj) ? placed : 0x0c0c0c0cu;
  return r;
}
__device__ __forceinline__ u32x4 make_a(unsigned X, const Sel& s) {
  u32x4 a;
#pragma unroll
  for (int jj = 0; jj < 4; ++jj) a[jj] = __builtin_amdgcn_perm(X, 0u, s.s[jj]);
  return a;
}
__device__ __forceinline__ unsigned split_hi_lo(float v) {
  const float h = rbf(v);
  return pk2(h, v - h);
}

__device__ __forceinline__ void ln_row(const unsigned (&xr)[5], LAS unsigned char* dst, int lane) {
  float sum = 0.f;
#pragma unroll
  for (int k = 0; k < 5; ++k) sum += bflo(xr[k]) + bfhi(xr[k]);
  const float mean = xrow_sum(row16_sum(sum)) * (1.0f / DM);
  float ss = 0.f;
#pragma unroll
  for (int k = 0; k < 5; ++k) {
    const float a = bflo(xr[k]) - mean, bb = bfhi(xr[k]) - mean;
    ss = fmaf(a, a, ss);
    ss = fmaf(bb, bb, ss);
  }
  const float var = xrow_sum(row16_sum(ss)) * (1.0f / DM);
  const float mu = rbf(mean);
  const float rinv = 1.0f / rbf(sqrtf(rbf(rbf(var) + 1e-5f)));
#pragma unroll
  for (int k = 0; k < 5; ++k)
    *(LAS unsigned*)(dst + 4 * (lane + 64 * k)) = pk2(rbf(bflo(xr[k]) - mu) * rinv, rbf(bfhi(xr[k]) - mu) * rinv);
}

__global__ void __launch_bounds__(1024)
k_main(const GAS unsigned short* __restrict__ x, const GAS unsigned short* __restrict__ kc, const GAS unsigned short* __restrict__ vc,
       const GAS unsigned short* __restrict__ wq, const GAS unsigned short* __restrict__ wo, const GAS unsigned short* __restrict__ wg,
       const GAS unsigned short* __restrict__ wu, const GAS unsigned short* __restrict__ wd, GAS u64* ws, GAS unsigned* out) {
  __shared__ __attribute__((aligned(16))) unsigned char smem[L_END];
  LAS unsigned char* L = (LAS unsigned char*)smem;
  const int tid = threadIdx.x, lane = tid & 63, wave = __builtin_amdgcn_readfirstlane(tid >> 6);
  const int g = lane >> 4, c = lane & 15;
  const int bid = blockIdx.x;
  const int jx = bid >> 3;
  const int h = (jx >> 3) * 8 + (bid & 7), mem = jx & 7, b = mem >> 1, s = mem & 1;
  const int slot = (b * NH + h) * 2 + s;
  STAMP_DECL;
  STAMP(0);
  if (tid < 8) *(LAS unsigned*)(L + L_FLAG + 4 * tid) = 0u;
  asm volatile("" ::"s"(x), "s"(kc), "s"(vc), "s"(wq), "s"(wo), "s"(wg), "s"(wu), "s"(wd), "s"(ws), "s"(out));
  WG_BARRIER();
  const Sel sel = make_sel(lane);

  if (wave >= 8) {
    const int w = wave - 8;
    __builtin_amdgcn_s_setprio(S_PRIO);
    const size_t base = ((size_t)(b * NH + h) * SK + (size_t)s * (SK / 2)) * DH;
    const __amdgpu_buffer_rsrc_t rk = __builtin_amdgcn_make_buffer_rsrc((void*)(kc + base), 0, SK / 2 * DH * 2, 0x00020000);
    const __amdgpu_buffer_rsrc_t rv = __builtin_amdgcn_make_buffer_rsrc((void*)(vc + base), 0, SK / 2 * DH * 2, 0x00020000);
    int voff = lane * 16 + w * 4096;
#define FRESH(v) asm volatile("" : "+v"(v))
#define STREAM_LD(n) __builtin_amdgcn_raw_buffer_load_b128((n) < 64 ? rk : rv, voff + ((n) & 3) * 1024, (((n) & 63) >> 2) * 32768, 2)
    u32x4 ring[RING];
#pragma unroll
    for (int i = 0; i < RING; ++i) {
      if (i >= DEPTH) VMWAIT(DEPTH - 1);
      ring[i] = STREAM_LD(i);
      SB();
    }
    lds_wait_ge(L + L_FLAG, 8u);
    STAMP(1);
    FRESH(voff);
    u32x4 q;
    {
      f32x4 q0 = *(const LAS f32x4*)(L + L_Q + (2 * g * 128 + 8 * c) * 4) + *(const LAS f32x4*)(L + L_Q + ((2 * g + 1) * 128 + 8 * c) * 4);
      f32x4 q1 = *(const LAS f32x4*)(L + L_Q + (2 * g * 128 + 8 * c + 4) * 4) + *(const LAS f32x4*)(L + L_Q + ((2 * g + 1) * 128 + 8 * c + 4) * 4);
#pragma unroll
      for (int k = 0; k < 4; ++k) { q0[k] = xrow_sum(q0[k]); q1[k] = xrow_sum(q1[k]); }
      q[0] = pk2(q0[0], q0[1]); q[1] = pk2(q0[2], q0[3]); q[2] = pk2(q1[0], q1[1]); q[3] = pk2(q1[2], q1[3]);
    }
    float sc[4];
#define MERGE(LO, HI, CTRL, BIT) tr_merge<CTRL>((LO), (HI), (c & (BIT)) != 0)
#pragma unroll
    for (int grp = 0; grp < 4; ++grp) {
      float d1 = 0.f, d2 = 0.f, d4 = 0.f, d8 = 0.f;
#pragma unroll
      for (int t = 0; t < 16; ++t) {
        const int i = grp * 16 + t;
        const u32x4 cur = ring[i % RING];
        float a = dot2(cur[0], q[0], 0.f);
        a = dot2(cur[1], q[1], a);
        a = dot2(cur[2], q[2], a);
        a = dot2(cur[3], q[3], a);
        VMWAIT(DEPTH - 1);
        ring[i % RING] = STREAM_LD(i + RING);
        if (!(t & 1)) d1 = a;
        else {
          a = MERGE(d1, a, XOR8, 8);
          if (!(t & 2)) d2 = a;
          else {
            a = MERGE(d2, a, XOR7, 4);
            if (!(t & 4)) d4 = a;
            else {
              a = MERGE(d4, a, XOR2, 2);
              if (!(t & 8)) d8 = a;
              else {
                a = MERGE(d8, a, XOR1, 1);
                sc[grp] = rbf(rbf(a) * QSCALE) * LOG2E;
              }
            }
          }
        }
        SB();
      }
    }
#undef MERGE
    STAMP(2);
    float mw = fmaxf(fmaxf(sc[0], sc[1]), fmaxf(sc[2], sc[3]));
    mw = xrow_max(row16_max(mw));
    unsigned pk[4];
    float lw = 0.f;
#pragma unroll
    for (int grp = 0; grp < 4; ++grp) {
      const float p = __builtin_amdgcn_exp2f(sc[grp] - mw);
      lw += p;
      pk[grp] = split_hi_lo(p);
    }
    lw = xrow_sum(row16_sum(lw));
    if (lane == 0) {
      *(LAS float*)(L + L_ML + w * 4) = mw;
      *(LAS float*)(L + L_ML + 32 + w * 4) = lw;
      LGKM0();
      __hip_atomic_fetch_add((LAS unsigned*)(L + L_FLAG + 4), 1u, RLX_WG);
    }
    SB();
    FRESH(voff);
    f32x4 acc = {0.f, 0.f, 0.f, 0.f};
#define BREV4(T) ((((T) & 1) << 3) | (((T) & 2) << 1) | (((T) & 4) >> 1) | (((T) & 8) >> 3))
#define PV_STEP(T)                                            \
  {                                                           \
    const int i = grp * 16 + (T), n = 64 + i;                 \
    const u32x4 cur = ring[n % RING];                         \
    const unsigned X = dppu<0x150 + BREV4(T)>(pk[grp]);       \
    acc = mfma(make_a(X, sel), cur, acc);                     \
    if (n + RING < 128) {                                     \
      VMWAIT(DEPTH - 1);                                      \
      ring[n % RING] = STREAM_LD(n + RING);                   \
    }                                                         \
    SB();                                                     \
  }
#pragma unroll
    for (int grp = 0; grp < 4; ++grp) {
      PV_STEP(0) PV_STEP(1) PV_STEP(2) PV_STEP(3) PV_STEP(4) PV_STEP(5) PV_STEP(6) PV_STEP(7)
      PV_STEP(8) PV_STEP(9) PV_STEP(10) PV_STEP(11) PV_STEP(12) PV_STEP(13) PV_STEP(14) PV_STEP(15)
    }
#undef PV_STEP
    STAMP(3);
    *(LAS f32x4*)(L + L_ACC + ((w * 2 + (g >> 1)) * ACC_LD + 8 * c + 4 * (g & 1)) * 4) = acc;
    WG_BARRIER();
    if (w >= 5 || !ABL_FINAL) { STAMP_OUT(); return; }
    const int j = h * 2 + s;
    const unsigned xw = ((const GAS unsigned*)x)[(b * DM + 10 * j) / 2 + w];
    const unsigned tag = lds_ld(L + L_FLAG + 16);
    WG_BARRIER();
    STAMP(4);
    const __amdgpu_buffer_rsrc_t ry = __builtin_amdgcn_make_buffer_rsrc((void*)(ws + G_Y + ((size_t)(b * 64 + j) * 5 + w) * 128), 0, 1024, 0x00020000);
    u32x4 yv = {0u, 0u, 0u, 0u};
    bool miss = true;
    for (unsigned spins = 0; spins < SPIN_CAP; ++spins) {
      if (miss) {
        yv = __builtin_amdgcn_raw_buffer_load_b128(ry, lane * 16, 0, 16);
        miss = yv[1] != tag || yv[3] != tag;
      }
      const int nmiss = __builtin_popcountll(__builtin_amdgcn_ballot_w64(miss));
      if (nmiss == 0) break;
      if (nmiss > POLL_N2) __builtin_amdgcn_s_sleep(POLL_S2);
      else if (nmiss > POLL_N1) __builtin_amdgcn_s_sleep(POLL_S1);
    }
    const float s0 = xrow_sum(row16_sum(__uint_as_float(yv[0]))), s1 = xrow_sum(row16_sum(__uint_as_float(yv[2])));
    if (lane == 0) out[(b * DM + 10 * j) / 2 + w] = pk2(bflo(xw) + s0, bfhi(xw) + s1);
    STAMP(5);
    STAMP_OUT();
    return;
  }

  const int hw = wave;
  __builtin_amdgcn_s_setprio(3);
  u32x4 wr[20];
  unsigned xp[10];
  {
    unsigned xr[5];
#pragma unroll
    for (int k = 0; k < 5; ++k) xr[k] = LDW((const GAS unsigned*)(x + b * DM) + lane + 64 * k);
    const __amdgpu_buffer_rsrc_t rq =
        __builtin_amdgcn_make_buffer_rsrc((void*)(wq + (size_t)80 * hw * (NH * DH) + h * DH), 0, ABL_WQ_BYTES, 0x00020000);
    const int vq = (20 * g * NH * DH + 8 * c) * 2;
#pragma unroll
    for (int i = 0; i < 20; ++i) wr[i] = __builtin_amdgcn_raw_buffer_load_b128(rq, vq, i * NH * DH * 2, W_NT);
    SB();
    ln_row(xr, L + L_XQ, lane);
    LGKM0();
#pragma unroll
    for (int k = 0; k < 5; ++k) {
      const u32x2 t = *(const LAS u32x2*)(L + L_XQ + (80 * hw + 20 * g + 4 * k) * 2);
      xp[2 * k] = t[0];
      xp[2 * k + 1] = t[1];
    }
    f32x4 acc = {0.f, 0.f, 0.f, 0.f};
#pragma unroll
    for (int i = 0; i < 20; ++i) {
      const unsigned X = (i & 1) ? (xp[i >> 1] >> 16) : xp[i >> 1];
      acc = mfma(make_a(X, sel), wr[i], acc);
    }
    if (g < 2) *(LAS f32x4*)(L + L_Q + (hw * 128 + 8 * c + 4 * g) * 4) = acc;
    LGKM0();
    if (lane == 0) __hip_atomic_fetch_add((LAS unsigned*)(L + L_FLAG), 1u, RLX_WG);
    STAMP(1);
  }
  if (hw == 0) {
    const unsigned e = __builtin_nontemporal_load((const GAS unsigned*)(ws + G_E)) + 1u;
    if (lane == 0) {
      *(LAS unsigned*)(L + L_FLAG + 16) = e;
      LGKM0();
      __hip_atomic_store((LAS unsigned*)(L + L_FLAG + 24), 1u, RLX_WG);
    }
  }
#define GET_TAG() (lds_wait_ge(L + L_FLAG + 24, 1u), __builtin_amdgcn_readfirstlane(lds_ld(L + L_FLAG + 16)))
  __builtin_amdgcn_s_setprio(0);
  if (h < NSL) {
    const GAS unsigned short* p = ((c & 4) ? wu : wg) + 64 * h + 32 * s + 8 * (c & 3) + (size_t)(80 * hw + 20 * g + (c >> 3)) * FF;
#pragma unroll
    for (int i = 0; i < 10; ++i) {
      VMWAIT(GU_DEPTH - 1);
      wr[i] = LDW((const GAS u32x4*)(p + (size_t)i * 2 * FF));
      SB();
    }
    f32x4 ga = {0.f, 0.f, 0.f, 0.f};
#pragma unroll
    for (int i = 0; i < 10; ++i) ga = mfma(make_a(xp[i], sel), wr[i], ga);
    if ((g >> 1) == (c >> 3)) *(LAS f32x4*)(L + L_GU + ((hw * 2 + (g >> 1)) * 64 + 8 * (c & 7) + 4 * (g & 1)) * 4) = ga;
    LGKM0();
    if (lane == 0) __hip_atomic_fetch_add((LAS unsigned*)(L + L_FLAG + 12), 1u, RLX_WG);
  }
  STAMP(2);
  {
    const __amdgpu_buffer_rsrc_t ro = __builtin_amdgcn_make_buffer_rsrc((void*)(wo + (size_t)(h * DH + 16 * hw) * DM), 0, ABL_WO_BYTES, 0x00020000);
    const int vo = (4 * g * DM + 8 * c) * 2;
#pragma unroll
    for (int j = 0; j < 4; ++j)
#pragma unroll
      for (int cb = 0; cb < 5; ++cb) {
        VMWAIT(WO_DEPTH - 1);
        wr[j * 5 + cb] = __builtin_amdgcn_raw_buffer_load_b128(ro, vo, (j * DM + cb * 128) * 2, W_NT);
        SB();
      }
  }
  SB();
  if (h < NSL) {
    u32x4 wdr[5];
    const GAS u32x4* p = (const GAS u32x4*)(wd + (size_t)(64 * h + 32 * s + 4 * hw + g) * DM + 8 * c);
#pragma unroll
    for (int cb = 0; cb < 5; ++cb) {
      VMWAIT(WO_DEPTH - 1);
      wdr[cb] = LDW(p + cb * 16);
      SB();
    }
    lds_wait_ge(L + L_FLAG + 12, 8u);
    const float gv = row16_sum(*(const LAS float*)(L + L_GU + (c * 64 + 4 * hw + g) * 4));
    const float uv = row16_sum(*(const LAS float*)(L + L_GU + (c * 64 + 32 + 4 * hw + g) * 4));
    const unsigned X = pk2(rbf(gv) * rbf(uv), 0.f);
    const u32x4 a = make_a(X, sel);
#pragma unroll
    for (int cb = 0; cb < 5; ++cb) {
      const f32x4 yf = mfma(a, wdr[cb], f32x4{0.f, 0.f, 0.f, 0.f});
      if (g < 2) *(LAS f32x4*)(L + L_BIG + (hw * DM + 128 * cb + 8 * c + 4 * g) * 4) = yf;
    }
    LGKM0();
    if (lane == 0) __hip_atomic_fetch_add((LAS unsigned*)(L + L_FLAG + 20), 1u, RLX_WG);
  }
  const unsigned tag = GET_TAG();
  f32x2 ysum = {0.f, 0.f};
  if (h < NSL && hw < 5) {
    lds_wait_ge(L + L_FLAG + 20, 8u);
#pragma unroll
    for (int p = 0; p < 8; ++p) ysum += *(const LAS f32x2*)(L + L_BIG + (p * DM + 2 * tid) * 4);
  }
  if (hw == 6) {
    lds_wait_ge(L + L_FLAG + 4, 8u);
    const float mv = *(const LAS float*)(L + L_ML + 4 * (lane & 7)), lv = *(const LAS float*)(L + L_ML + 32 + 4 * (lane & 7));
    float M = fmaxf(mv, dppf<XOR1>(mv));
    M = fmaxf(M, dppf<XOR2>(M));
    M = fmaxf(M, dppf<XOR7>(M));
    float Ls = __builtin_amdgcn_exp2f(mv - M) * lv;
    Ls += dppf<XOR1>(Ls);
    Ls += dppf<XOR2>(Ls);
    Ls += dppf<XOR7>(Ls);
    if (lane < 2) put(ws + G_ML + slot * 16 + lane, lane == 0 ? M : Ls, tag);
    float o2[2];
    sweep<2, 16>(ws + G_ML + (slot ^ 1) * 16, 1, tag, o2);
    const float Mt = fmaxf(M, o2[0]);
    const float e0 = __builtin_amdgcn_exp2f(M - Mt) * Ls, e1 = __builtin_amdgcn_exp2f(o2[0] - Mt) * o2[1];
    const float Lt = s == 0 ? e0 + e1 : e1 + e0;
    if (lane < 8) *(LAS float*)(L + L_EW + 4 * lane) = __builtin_amdgcn_exp2f(mv - Mt) / Lt;
    STAMP(5);
  } else if (hw == 7 && bid == 0) {
    lds_wait_ge(L + L_FLAG + 4, 8u);
    float f4[4];
    sweep<4, 64>(ws + G_ML + lane * 16, 64 * 16, tag, f4);
    if (lane == 0) __hip_atomic_store((GAS unsigned*)(ws + G_E), tag, RLX_AGENT);
  }
  WG_BARRIER();
  STAMP(3);
  {
    f32x4 o = *(const LAS f32x4*)(L + L_ACC + (c * ACC_LD + 16 * hw + 4 * g) * 4) * *(const LAS float*)(L + L_EW + 4 * (c >> 1));
#pragma unroll
    for (int j = 0; j < 4; ++j) o[j] = row16_sum(o[j]);
    f32x4 y[5];
#pragma unroll
    for (int cb = 0; cb < 5; ++cb) y[cb] = f32x4{0.f, 0.f, 0.f, 0.f};
#pragma unroll
    for (int j = 0; j < 4; ++j) {
      const u32x4 a = make_a(split_hi_lo(o[j]), sel);
#pragma unroll
      for (int cb = 0; cb < 5; ++cb) y[cb] = mfma(a, wr[j * 5 + cb], y[cb]);
    }
#pragma unroll
    for (int cb = 0; cb < 5; ++cb)
      *(LAS f32x4*)(L + L_BIG + ((hw * 2 + (g >> 1)) * DM + 128 * cb + 8 * c + 4 * (g & 1)) * 4) = y[cb];
  }
  WG_BARRIER();
  if (tid < DM / 2 && ABL_FINAL) {
    f32x2 t = ysum;
#pragma unroll
    for (int p = 0; p < 16; ++p) t += *(const LAS f32x2*)(L + L_BIG + (p * DM + 2 * tid) * 4);
    const __amdgpu_buffer_rsrc_t ry = __builtin_amdgcn_make_buffer_rsrc((void*)(ws + G_Y + (size_t)b * 320 * 128), 0, 320 * 1024, 0x00020000);
    __builtin_amdgcn_raw_buffer_store_b128(u32x4{__float_as_uint(t[0]), tag, __float_as_uint(t[1]), tag}, ry, tid * 1024, (h * 2 + s) * 16, 16);
  }
  STAMP(4);
  STAMP_OUT();
}

extern "C" void kernel_launch(void* const* d_in, const int* in_sizes, int n_in, void* d_out, int out_size, void* d_ws,
                              size_t ws_size, hipStream_t stream) {
  (void)in_sizes; (void)n_in; (void)out_size; (void)ws_size;
  typedef const GAS unsigned short* cp;
  k_main<<<NWG, 1024, 0, stream>>>((cp)d_in[0], (cp)d_in[1], (cp)d_in[2], (cp)d_in[3], (cp)d_in[4], (cp)d_in[6], (cp)d_in[7],
                                   (cp)d_in[8], (GAS u64*)d_ws, (GAS unsigned*)d_out);
}
```

```cpp
#include <hip/hip_runtime.h>
#include <hip/hip_bf16.h>
#include <stdint.h>

#define RING 8
#define DEPTH 4
#define WO_DEPTH 2
#define Q_DELAY 8
#define POLL_N1 2
#define POLL_S1 8
#define POLL_N2 8
#define POLL_S2 32
#define ABL_WO_BYTES (16 * DM * 2)
#define ABL_WQ_BYTES (80 * NH * DH * 2)
#define ABL_GU_N 20
#define ABL_PUB 1
#define ABL_WD 1
#define ABL_FINAL 1
#define GU_DEPTH 1
#define GLU_MEM 0
#define S_PRIO 2
#define K_AUX 2
#define V_AUX 2
#define W_NT 2
#define WO_SLEEP 0

typedef unsigned u32x4 __attribute__((ext_vector_type(4)));
typedef unsigned u32x2 __attribute__((ext_vector_type(2)));
typedef float f32x4 __attribute__((ext_vector_type(4)));
typedef float f32x2 __attribute__((ext_vector_type(2)));
typedef short bf16x8 __attribute__((ext_vector_type(8)));
typedef __bf16 bf16x2_t __attribute__((ext_vector_type(2)));
typedef unsigned long long u64;
#define GAS __attribute__((address_space(1)))
#define LAS __attribute__((address_space(3)))
#define RLX_AGENT __ATOMIC_RELAXED, __HIP_MEMORY_SCOPE_AGENT
#define RLX_WG __ATOMIC_RELAXED, __HIP_MEMORY_SCOPE_WORKGROUP

constexpr int NB = 4, DM = 640, NH = 32, SK = 4096, DH = 128, FF = 1728;
constexpr int NSL = FF / 64;
constexpr int NWG = 256;
constexpr float LOG2E = 1.4426950408889634f;
constexpr float QSCALE = 0.08838834764831845f;
constexpr unsigned SPIN_CAP = 1u << 18;

constexpr size_t G_E = 0;
constexpr size_t G_FL = 32;
constexpr size_t G_ML = G_FL + NB * 64;
constexpr size_t G_Y = G_ML + (size_t)NWG * 16;

constexpr int L_XN = 0;
constexpr int L_XQ = L_XN + NB * DM * 2;
constexpr int L_Q = L_XQ + DM * 2;
constexpr int ACC_LD = 132;
constexpr int L_ACC = L_Q + 8 * 128 * 4;
constexpr int L_ML = L_ACC + 16 * ACC_LD * 4;
constexpr int L_EW = L_ML + 64;
constexpr int L_FLAG = L_EW + 32;
constexpr int L_H = L_FLAG + 32;
constexpr int L_BIG = L_H + 64;
constexpr int L_GU = L_BIG + 8 * DM * 4;
constexpr int L_END = L_BIG + 8 * 2 * DM * 4;

__device__ __forceinline__ float bflo(unsigned u) { return __builtin_bit_cast(float, u << 16); }
__device__ __forceinline__ float bfhi(unsigned u) { return __builtin_bit_cast(float, u & 0xffff0000u); }
__device__ __forceinline__ unsigned pk2(float lo, float hi) {
  bf16x2_t r;
  r[0] = (__bf16)lo;
  r[1] = (__bf16)hi;
  return __builtin_bit_cast(unsigned, r);
}
__device__ __forceinline__ float rbf(float x) { return (float)(__bf16)x; }
__device__ __forceinline__ float dot2(unsigned a, unsigned b, float c) {
  return __builtin_amdgcn_fdot2_f32_bf16(__builtin_bit_cast(bf16x2_t, a), __builtin_bit_cast(bf16x2_t, b), c, false);
}
template <int CTRL> __device__ __forceinline__ float dppf(float x) {
  return __builtin_bit_cast(float, __builtin_amdgcn_mov_dpp(__builtin_bit_cast(int, x), CTRL, 0xf, 0xf, true));
}
template <int CTRL> __device__ __forceinline__ unsigned dppu(unsigned x) {
  return (unsigned)__builtin_amdgcn_mov_dpp((int)x, CTRL, 0xf, 0xf, true);
}
constexpr int XOR1 = 0xB1, XOR2 = 0x4E, XOR7 = 0x141, XOR8 = 0x128;
template <int CTRL> __device__ __forceinline__ float tr_merge(float lo, float hi, bool up) {
  const float t1 = lo + dppf<CTRL>(lo);
  const float t2 = hi + dppf<CTRL>(hi);
  return up ? t2 : t1;
}
__device__ __forceinline__ float row16_sum(float x) {
  x += dppf<XOR1>(x); x += dppf<XOR2>(x); x += dppf<XOR7>(x); x += dppf<XOR8>(x);
  return x;
}
__device__ __forceinline__ float row16_max(float x) {
  x = fmaxf(x, dppf<XOR1>(x)); x = fmaxf(x, dppf<XOR2>(x)); x = fmaxf(x, dppf<XOR7>(x)); x = fmaxf(x, dppf<XOR8>(x));
  return x;
}
__device__ __forceinline__ float half_sum(float x) {
  auto t = __builtin_amdgcn_permlane32_swap(__float_as_uint(x), __float_as_uint(x), false, false);
  return __uint_as_float(t[0]) + __uint_as_float(t[1]);
}
__device__ __forceinline__ float xrow_max(float x) {
  auto s = __builtin_amdgcn_permlane16_swap(__float_as_uint(x), __float_as_uint(x), false, false);
  x = fmaxf(__uint_as_float(s[0]), __uint_as_float(s[1]));
  auto t = __builtin_amdgcn_permlane32_swap(__float_as_uint(x), __float_as_uint(x), false, false);
  return fmaxf(__uint_as_float(t[0]), __uint_as_float(t[1]));
}
__device__ __forceinline__ float xrow_sum(float x) {
  auto s = __builtin_amdgcn_permlane16_swap(__float_as_uint(x), __float_as_uint(x), false, false);
  x = __uint_as_float(s[0]) + __uint_as_float(s[1]);
  return half_sum(x);
}
__device__ __forceinline__ f32x4 mfma(u32x4 a, u32x4 b, f32x4 c) {
  return __builtin_amdgcn_mfma_f32_16x16x32_bf16(__builtin_bit_cast(bf16x8, a), __builtin_bit_cast(bf16x8, b), c, 0, 0, 0);
}
#define LGKM0() asm volatile("s_waitcnt lgkmcnt(0)" ::: "memory")
#define WG_BARRIER()                \
  do {                              \
    LGKM0();                        \
    __builtin_amdgcn_s_barrier();   \
    asm volatile("" ::: "memory");  \
  } while (0)
#define SB() __builtin_amdgcn_sched_barrier(0)
#define STAMP(k)
#define STAMP_DECL
#define STAMP_OUT()
#define LDW(p) __builtin_nontemporal_load(p)
#define VMWAIT(n) asm volatile("s_waitcnt vmcnt(%0)" ::"i"(n) : "memory")

__device__ __forceinline__ void put(GAS u64* p, float v, unsigned tag) {
  __hip_atomic_store(p, ((u64)tag << 32) | (u64)__float_as_uint(v), RLX_AGENT);
}
__device__ __forceinline__ u64 get(const GAS u64* p) { return __hip_atomic_load(p, RLX_AGENT); }
__device__ __forceinline__ unsigned lds_ld(LAS unsigned char* p) { return __hip_atomic_load((LAS unsigned*)p, RLX_WG); }
__device__ __forceinline__ void lds_wait_ge(LAS unsigned char* p, unsigned want) {
  while (lds_ld(p) < want) __builtin_amdgcn_s_sleep(1);
  asm volatile("" ::: "memory");
}
template <int N, int SLP> __device__ __forceinline__ void sweep(const GAS u64* p, int S, unsigned tag, float (&v)[N]) {
  for (unsigned spins = 0;; ++spins) {
    bool ok = true;
#pragma unroll
    for (int k = 0; k < N; ++k) {
      const u64 x = get(p + k * S);
      v[k] = __uint_as_float((unsigned)x);
      ok &= (unsigned)(x >> 32) == tag;
    }
    if (__all(ok) || spins > SPIN_CAP) return;
    __builtin_amdgcn_s_sleep(SLP);
  }
}

struct Sel { unsigned s[4]; };
__device__ __forceinline__ Sel make_sel(int lane) {
  const int m = lane & 15;
  const unsigned src = (m & 8) ? 0x0706u : 0x0504u;
  const unsigned placed = (m & 1) ? ((src << 16) | 0x0c0cu) : (0x0c0c0000u | src);
  Sel r;
#pragma unroll
  for (int jj = 0; jj < 4; ++jj) r.s[jj] = (((m & 7) >> 1) == jj) ? placed : 0x0c0c0c0cu;
  return r;
}
__device__ __forceinline__ u32x4 make_a(unsigned X, const Sel& s) {
  u32x4 a;
#pragma unroll
  for (int jj = 0; jj < 4; ++jj) a[jj] = __builtin_amdgcn_perm(X, 0u, s.s[jj]);
  return a;
}
__device__ __forceinline__ unsigned split_hi_lo(float v) {
  const float h = rbf(v);
  return pk2(h, v - h);
}

__device__ __forceinline__ void ln_row(const unsigned (&xr)[5], LAS unsigned char* dst, int lane) {
  float sum = 0.f;
#pragma unroll
  for (int k = 0; k < 5; ++k) sum += bflo(xr[k]) + bfhi(xr[k]);
  const float mean = xrow_sum(row16_sum(sum)) * (1.0f / DM);
  float ss = 0.f;
#pragma unroll
  for (int k = 0; k < 5; ++k) {
    const float a = bflo(xr[k]) - mean, bb = bfhi(xr[k]) - mean;
    ss = fmaf(a, a, ss);
    ss = fmaf(bb, bb, ss);
  }
  const float var = xrow_sum(row16_sum(ss)) * (1.0f / DM);
  const float mu = rbf(mean);
  const float rinv = 1.0f / rbf(sqrtf(rbf(rbf(var) + 1e-5f)));
#pragma unroll
  for (int k = 0; k < 5; ++k)
    *(LAS unsigned*)(dst + 4 * (lane + 64 * k)) = pk2(rbf(bflo(xr[k]) - mu) * rinv, rbf(bfhi(xr[k]) - mu) * rinv);
}

__global__ void __launch_bounds__(1024)
k_main(const GAS unsigned short* __restrict__ x, const GAS unsigned short* __restrict__ kc, const GAS unsigned short* __restrict__ vc,
       const GAS unsigned short* __restrict__ wq, const GAS unsigned short* __restrict__ wo, const GAS unsigned short* __restrict__ wg,
       const GAS unsigned short* __restrict__ wu, const GAS unsigned short* __restrict__ wd, GAS u64* ws, GAS unsigned* out) {
  __shared__ __attribute__((aligned(16))) unsigned char smem[L_END];
  LAS unsigned char* L = (LAS unsigned char*)smem;
  const int tid = threadIdx.x, lane = tid & 63, wave = __builtin_amdgcn_readfirstlane(tid >> 6);
  const int g = lane >> 4, c = lane & 15;
  const int bid = blockIdx.x;
  const int jx = bid >> 3;
  const int h = (jx >> 3) * 8 + (bid & 7), mem = jx & 7, b = mem >> 1, s = mem & 1;
  const int slot = (b * NH + h) * 2 + s;
  STAMP_DECL;
  STAMP(0);
  if (tid < 8) *(LAS unsigned*)(L + L_FLAG + 4 * tid) = 0u;
  asm volatile("" ::"s"(x), "s"(kc), "s"(vc), "s"(wq), "s"(wo), "s"(wg), "s"(wu), "s"(wd), "s"(ws), "s"(out));
  WG_BARRIER();
  const Sel sel = make_sel(lane);

  if (wave >= 8) {
    const int w = wave - 8;
    __builtin_amdgcn_s_setprio(S_PRIO);
    const size_t base = ((size_t)(b * NH + h) * SK + (size_t)s * (SK / 2)) * DH;
    const __amdgpu_buffer_rsrc_t rk = __builtin_amdgcn_make_buffer_rsrc((void*)(kc + base), 0, SK / 2 * DH * 2, 0x00020000);
    const __amdgpu_buffer_rsrc_t rv = __builtin_amdgcn_make_buffer_rsrc((void*)(vc + base), 0, SK / 2 * DH * 2, 0x00020000);
    int voff = lane * 16 + w * 4096;
#define FRESH(v) asm volatile("" : "+v"(v))
#define STREAM_LD(n)                                                                                                     \
  ((n) < 64 ? __builtin_amdgcn_raw_buffer_load_b128(rk, voff + ((n) & 3) * 1024, (((n) & 63) >> 2) * 32768, K_AUX)       \
            : __builtin_amdgcn_raw_buffer_load_b128(rv, voff + ((n) & 3) * 1024, (((n) & 63) >> 2) * 32768, V_AUX))
    u32x4 ring[RING];
#pragma unroll
    for (int i = 0; i < RING; ++i) {
      if (i >= DEPTH) VMWAIT(DEPTH - 1);
      ring[i] = STREAM_LD(i);
      SB();
    }
    lds_wait_ge(L + L_FLAG, 8u);
    STAMP(1);
    FRESH(voff);
    u32x4 q;
    {
      f32x4 q0 = *(const LAS f32x4*)(L + L_Q + (2 * g * 128 + 8 * c) * 4) + *(const LAS f32x4*)(L + L_Q + ((2 * g + 1) * 128 + 8 * c) * 4);
      f32x4 q1 = *(const LAS f32x4*)(L + L_Q + (2 * g * 128 + 8 * c + 4) * 4) + *(const LAS f32x4*)(L + L_Q + ((2 * g + 1) * 128 + 8 * c + 4) * 4);
#pragma unroll
      for (int k = 0; k < 4; ++k) { q0[k] = xrow_sum(q0[k]); q1[k] = xrow_sum(q1[k]); }
      q[0] = pk2(q0[0], q0[1]); q[1] = pk2(q0[2], q0[3]); q[2] = pk2(q1[0], q1[1]); q[3] = pk2(q1[2], q1[3]);
    }
    float sc[4];
#define MERGE(LO, HI, CTRL, BIT) tr_merge<CTRL>((LO), (HI), (c & (BIT)) != 0)
#pragma unroll
    for (int grp = 0; grp < 4; ++grp) {
      float d1 = 0.f, d2 = 0.f, d4 = 0.f, d8 = 0.f;
#pragma unroll
      for (int t = 0; t < 16; ++t) {
        const int i = grp * 16 + t;
        const u32x4 cur = ring[i % RING];
        float a = dot2(cur[0], q[0], 0.f);
        a = dot2(cur[1], q[1], a);
        a = dot2(cur[2], q[2], a);
        a = dot2(cur[3], q[3], a);
        VMWAIT(DEPTH - 1);
        ring[i % RING] = STREAM_LD(i + RING);
        if (!(t & 1)) d1 = a;
        else {
          a = MERGE(d1, a, XOR8, 8);
          if (!(t & 2)) d2 = a;
          else {
            a = MERGE(d2, a, XOR7, 4);
            if (!(t & 4)) d4 = a;
            else {
              a = MERGE(d4, a, XOR2, 2);
              if (!(t & 8)) d8 = a;
              else {
                a = MERGE(d8, a, XOR1, 1);
                sc[grp] = rbf(rbf(a) * QSCALE) * LOG2E;
              }
            }
          }
        }
        SB();
      }
    }
#undef MERGE
    STAMP(2);
    float mw = fmaxf(fmaxf(sc[0], sc[1]), fmaxf(sc[2], sc[3]));
    mw = xrow_max(row16_max(mw));
    unsigned pk[4];
    float lw = 0.f;
#pragma unroll
    for (int grp = 0; grp < 4; ++grp) {
      const float p = __builtin_amdgcn_exp2f(sc[grp] - mw);
      lw += p;
      pk[grp] = split_hi_lo(p);
    }
    lw = xrow_sum(row16_sum(lw));
    if (lane == 0) {
      *(LAS float*)(L + L_ML + w * 4) = mw;
      *(LAS float*)(L + L_ML + 32 + w * 4) = lw;
      LGKM0();
      __hip_atomic_fetch_add((LAS unsigned*)(L + L_FLAG + 4), 1u, RLX_WG);
    }
    SB();
    FRESH(voff);
    f32x4 acc = {0.f, 0.f, 0.f, 0.f};
#define BREV4(T) ((((T) & 1) << 3) | (((T) & 2) << 1) | (((T) & 4) >> 1) | (((T) & 8) >> 3))
#define PV_STEP(T)                                            \
  {                                                           \
    const int i = grp * 16 + (T), n = 64 + i;                 \
    const u32x4 cur = ring[n % RING];                         \
    const unsigned X = dppu<0x150 + BREV4(T)>(pk[grp]);       \
    acc = mfma(make_a(X, sel), cur, acc);                     \
    if (n + RING < 128) {                                     \
      VMWAIT(DEPTH - 1);                                      \
      ring[n % RING] = STREAM_LD(n + RING);                   \
    }                                                         \
    SB();                                                     \
  }
#pragma unroll
    for (int grp = 0; grp < 4; ++grp) {
      PV_STEP(0) PV_STEP(1) PV_STEP(2) PV_STEP(3) PV_STEP(4) PV_STEP(5) PV_STEP(6) PV_STEP(7)
      PV_STEP(8) PV_STEP(9) PV_STEP(10) PV_STEP(11) PV_STEP(12) PV_STEP(13) PV_STEP(14) PV_STEP(15)
    }
#undef PV_STEP
    STAMP(3);
    *(LAS f32x4*)(L + L_ACC + ((w * 2 + (g >> 1)) * ACC_LD + 8 * c + 4 * (g & 1)) * 4) = acc;
    WG_BARRIER();
    if (w >= 5 || !ABL_FINAL) { STAMP_OUT(); return; }
    const int j = h * 2 + s;
    const unsigned xw = ((const GAS unsigned*)x)[(b * DM + 10 * j) / 2 + w];
    const unsigned tag = lds_ld(L + L_FLAG + 16);
    WG_BARRIER();
    STAMP(4);
    const __amdgpu_buffer_rsrc_t ry = __builtin_amdgcn_make_buffer_rsrc((void*)(ws + G_Y + ((size_t)(b * 64 + j) * 5 + w) * 128), 0, 1024, 0x00020000);
    u32x4 yv = {0u, 0u, 0u, 0u};
    bool miss = true;
    for (unsigned spins = 0; spins < SPIN_CAP; ++spins) {
      if (miss) {
        yv = __builtin_amdgcn_raw_buffer_load_b128(ry, lane * 16, 0, 16);
        miss = yv[1] != tag || yv[3] != tag;
      }
      const int nmiss = __builtin_popcountll(__builtin_amdgcn_ballot_w64(miss));
      if (nmiss == 0) break;
      if (nmiss > POLL_N2) __builtin_amdgcn_s_sleep(POLL_S2);
      else if (nmiss > POLL_N1) __builtin_amdgcn_s_sleep(POLL_S1);
    }
    const float s0 = xrow_sum(row16_sum(__uint_as_float(yv[0]))), s1 = xrow_sum(row16_sum(__uint_as_float(yv[2])));
    if (lane == 0) out[(b * DM + 10 * j) / 2 + w] = pk2(bflo(xw) + s0, bfhi(xw) + s1);
    STAMP(5);
    STAMP_OUT();
    return;
  }

  const int hw = wave;
  __builtin_amdgcn_s_setprio(3);
  u32x4 wr[20];
  unsigned xp[10];
  {
    unsigned xr[5];
#pragma unroll
    for (int k = 0; k < 5; ++k) xr[k] = LDW((const GAS unsigned*)(x + b * DM) + lane + 64 * k);
    const __amdgpu_buffer_rsrc_t rq =
        __builtin_amdgcn_make_buffer_rsrc((void*)(wq + (size_t)80 * hw * (NH * DH) + h * DH), 0, ABL_WQ_BYTES, 0x00020000);
    const int vq = (20 * g * NH * DH + 8 * c) * 2;
#pragma unroll
    for (int i = 0; i < 20; ++i) wr[i] = __builtin_amdgcn_raw_buffer_load_b128(rq, vq, i * NH * DH * 2, W_NT);
    SB();
    ln_row(xr, L + L_XQ, lane);
    LGKM0();
#pragma unroll
    for (int k = 0; k < 5; ++k) {
      const u32x2 t = *(const LAS u32x2*)(L + L_XQ + (80 * hw + 20 * g + 4 * k) * 2);
      xp[2 * k] = t[0];
      xp[2 * k + 1] = t[1];
    }
    f32x4 acc = {0.f, 0.f, 0.f, 0.f};
#pragma unroll
    for (int i = 0; i < 20; ++i) {
      const unsigned X = (i & 1) ? (xp[i >> 1] >> 16) : xp[i >> 1];
      acc = mfma(make_a(X, sel), wr[i], acc);
    }
    if (g < 2) *(LAS f32x4*)(L + L_Q + (hw * 128 + 8 * c + 4 * g) * 4) = acc;
    LGKM0();
    if (lane == 0) __hip_atomic_fetch_add((LAS unsigned*)(L + L_FLAG), 1u, RLX_WG);
    STAMP(1);
  }
  if (hw == 0) {
    const unsigned e = __builtin_nontemporal_load((const GAS unsigned*)(ws + G_E)) + 1u;
    if (lane == 0) {
      *(LAS unsigned*)(L + L_FLAG + 16) = e;
      LGKM0();
      __hip_atomic_store((LAS unsigned*)(L + L_FLAG + 24), 1u, RLX_WG);
    }
  }
#define GET_TAG() (lds_wait_ge(L + L_FLAG + 24, 1u), __builtin_amdgcn_readfirstlane(lds_ld(L + L_FLAG + 16)))
  __builtin_amdgcn_s_setprio(0);
  if (h < NSL) {
    const GAS unsigned short* p = ((c & 4) ? wu : wg) + 64 * h + 32 * s + 8 * (c & 3) + (size_t)(80 * hw + 20 * g + (c >> 3)) * FF;
#pragma unroll
    for (int i = 0; i < 10; ++i) {
      VMWAIT(GU_DEPTH - 1);
      wr[i] = LDW((const GAS u32x4*)(p + (size_t)i * 2 * FF));
      SB();
    }
    f32x4 ga = {0.f, 0.f, 0.f, 0.f};
#pragma unroll
    for (int i = 0; i < 10; ++i) ga = mfma(make_a(xp[i], sel), wr[i], ga);
    if ((g >> 1) == (c >> 3)) *(LAS f32x4*)(L + L_GU + ((hw * 2 + (g >> 1)) * 64 + 8 * (c & 7) + 4 * (g & 1)) * 4) = ga;
    LGKM0();
    if (lane == 0) __hip_atomic_fetch_add((LAS unsigned*)(L + L_FLAG + 12), 1u, RLX_WG);
  }
  STAMP(2);
  {
    const __amdgpu_buffer_rsrc_t ro = __builtin_amdgcn_make_buffer_rsrc((void*)(wo + (size_t)(h * DH + 16 * hw) * DM), 0, ABL_WO_BYTES, 0x00020000);
    const int vo = (4 * g * DM + 8 * c) * 2;
#pragma unroll
    for (int j = 0; j < 4; ++j)
#pragma unroll
      for (int cb = 0; cb < 5; ++cb) {
        VMWAIT(WO_DEPTH - 1);
        wr[j * 5 + cb] = __builtin_amdgcn_raw_buffer_load_b128(ro, vo, (j * DM + cb * 128) * 2, W_NT);
        SB();
      }
  }
  SB();
  if (h < NSL) {
    u32x4 wdr[5];
    const GAS u32x4* p = (const GAS u32x4*)(wd + (size_t)(64 * h + 32 * s + 4 * hw + g) * DM + 8 * c);
#pragma unroll
    for (int cb = 0; cb < 5; ++cb) {
      VMWAIT(WO_DEPTH - 1);
      wdr[cb] = LDW(p + cb * 16);
      SB();
    }
    lds_wait_ge(L + L_FLAG + 12, 8u);
    const float gv = row16_sum(*(const LAS float*)(L + L_GU + (c * 64 + 4 * hw + g) * 4));
    const float uv = row16_sum(*(const LAS float*)(L + L_GU + (c * 64 + 32 + 4 * hw + g) * 4));
    const unsigned X = pk2(rbf(gv) * rbf(uv), 0.f);
    const u32x4 a = make_a(X, sel);
#pragma unroll
    for (int cb = 0; cb < 5; ++cb) {
      const f32x4 yf = mfma(a, wdr[cb], f32x4{0.f, 0.f, 0.f, 0.f});
      if (g < 2) *(LAS f32x4*)(L + L_BIG + (hw * DM + 128 * cb + 8 * c + 4 * g) * 4) = yf;
    }
    LGKM0();
    if (lane == 0) __hip_atomic_fetch_add((LAS unsigned*)(L + L_FLAG + 20), 1u, RLX_WG);
  }
  const unsigned tag = GET_TAG();
  f32x2 ysum = {0.f, 0.f};
  if (h < NSL && hw < 5) {
    lds_wait_ge(L + L_FLAG + 20, 8u);
#pragma unroll
    for (int p = 0; p < 8; ++p) ysum += *(const LAS f32x2*)(L + L_BIG + (p * DM + 2 * tid) * 4);
  }
  if (hw == 6) {
    lds_wait_ge(L + L_FLAG + 4, 8u);
    const float mv = *(const LAS float*)(L + L_ML + 4 * (lane & 7)), lv = *(const LAS float*)(L + L_ML + 32 + 4 * (lane & 7));
    float M = fmaxf(mv, dppf<XOR1>(mv));
    M = fmaxf(M, dppf<XOR2>(M));
    M = fmaxf(M, dppf<XOR7>(M));
    float Ls = __builtin_amdgcn_exp2f(mv - M) * lv;
    Ls += dppf<XOR1>(Ls);
    Ls += dppf<XOR2>(Ls);
    Ls += dppf<XOR7>(Ls);
    if (lane < 2) put(ws + G_ML + slot * 16 + lane, lane == 0 ? M : Ls, tag);
    float o2[2];
    sweep<2, 16>(ws + G_ML + (slot ^ 1) * 16, 1, tag, o2);
    const float Mt = fmaxf(M, o2[0]);
    const float e0 = __builtin_amdgcn_exp2f(M - Mt) * Ls, e1 = __builtin_amdgcn_exp2f(o2[0] - Mt) * o2[1];
    const float Lt = s == 0 ? e0 + e1 : e1 + e0;
    if (lane < 8) *(LAS float*)(L + L_EW + 4 * lane) = __builtin_amdgcn_exp2f(mv - Mt) / Lt;
    STAMP(5);
  } else if (hw == 7 && bid == 0) {
    lds_wait_ge(L + L_FLAG + 4, 8u);
    float f4[4];
    sweep<4, 64>(ws + G_ML + lane * 16, 64 * 16, tag, f4);
    if (lane == 0) __hip_atomic_store((GAS unsigned*)(ws + G_E), tag, RLX_AGENT);
  }
  WG_BARRIER();
  STAMP(3);
  {
    f32x4 o = *(const LAS f32x4*)(L + L_ACC + (c * ACC_LD + 16 * hw + 4 * g) * 4) * *(const LAS float*)(L + L_EW + 4 * (c >> 1));
#pragma unroll
    for (int j = 0; j < 4; ++j) o[j] = row16_sum(o[j]);
    f32x4 y[5];
#pragma unroll
    for (int cb = 0; cb < 5; ++cb) y[cb] = f32x4{0.f, 0.f, 0.f, 0.f};
#pragma unroll
    for (int j = 0; j < 4; ++j) {
      const u32x4 a = make_a(split_hi_lo(o[j]), sel);
#pragma unroll
      for (int cb = 0; cb < 5; ++cb) y[cb] = mfma(a, wr[j * 5 + cb], y[cb]);
    }
#pragma unroll
    for (int cb = 0; cb < 5; ++cb)
      *(LAS f32x4*)(L + L_BIG + ((hw * 2 + (g >> 1)) * DM + 128 * cb + 8 * c + 4 * (g & 1)) * 4) = y[cb];
  }
  WG_BARRIER();
  if (tid < DM / 2 && ABL_FINAL) {
    f32x2 t = ysum;
#pragma unroll
    for (int p = 0; p < 16; ++p) t += *(const LAS f32x2*)(L + L_BIG + (p * DM + 2 * tid) * 4);
    const __amdgpu_buffer_rsrc_t ry = __builtin_amdgcn_make_buffer_rsrc((void*)(ws + G_Y + (size_t)b * 320 * 128), 0, 320 * 1024, 0x00020000);
    __builtin_amdgcn_raw_buffer_store_b128(u32x4{__float_as_uint(t[0]), tag, __float_as_uint(t[1]), tag}, ry, tid * 1024, (h * 2 + s) * 16, 16);
  }
  STAMP(4);
  STAMP_OUT();
}

extern "C" void kernel_launch(void* const* d_in, const int* in_sizes, int n_in, void* d_out, int out_size, void* d_ws,
                              size_t ws_size, hipStream_t stream) {
  (void)in_sizes; (void)n_in; (void)out_size; (void)ws_size;
  typedef const GAS unsigned short* cp;
  k_main<<<NWG, 1024, 0, stream>>>((cp)d_in[0], (cp)d_in[1], (cp)d_in[2], (cp)d_in[3], (cp)d_in[4], (cp)d_in[6], (cp)d_in[7],
                                   (cp)d_in[8], (GAS u64*)d_ws, (GAS unsigned*)d_out);
}
```

```cpp
#include <hip/hip_runtime.h>
#include <hip/hip_bf16.h>
#include <stdint.h>

#define DEPTH 4
#define GU_DEPTH 1
#define WO_DEPTH 2
#define WQ_AUX 0
#define W_AUX 0
#define X_NT 0
#define EQUALIZE 1
#define START_SLEEP 0
#define DEPTH0 4
#define DEPTH0_N 0
#define PACE 0
#define PACE0 450
#define YMODE 1
#define YPOLL_N 8
#define YPOLL_S 8
#define YPOLL_S0 1
#define HROT 0
#define POLL_N1 2
#define POLL_S1 8
#define POLL_N2 8
#define POLL_S2 32
#define ABL_FINAL 1
constexpr int AUX_NT = 2, AUX_SC1 = 16;

typedef unsigned u32x4 __attribute__((ext_vector_type(4)));
typedef unsigned u32x2 __attribute__((ext_vector_type(2)));
typedef float f32x4 __attribute__((ext_vector_type(4)));
typedef float f32x2 __attribute__((ext_vector_type(2)));
typedef short bf16x8 __attribute__((ext_vector_type(8)));
typedef __bf16 bf16x2_t __attribute__((ext_vector_type(2)));
typedef unsigned long long u64;
#define GAS __attribute__((address_space(1)))
#define LAS __attribute__((address_space(3)))
#define RLX_AGENT __ATOMIC_RELAXED, __HIP_MEMORY_SCOPE_AGENT
#define RLX_WG __ATOMIC_RELAXED, __HIP_MEMORY_SCOPE_WORKGROUP

constexpr int NB = 4, DM = 640, NH = 32, SK = 4096, DH = 128, FF = 1728;
constexpr int NSL = FF / 64;
constexpr int NWG = 256;
constexpr float LOG2E = 1.4426950408889634f;
constexpr float QSCALE = 0.08838834764831845f;
constexpr unsigned SPIN_CAP = 1u << 18;

constexpr size_t G_E = 0;
constexpr size_t G_X = 32;
constexpr size_t G_Y = G_X + (size_t)NWG * 64;

constexpr int L_XQ = 0;
constexpr int L_Q = L_XQ + DM * 2;
constexpr int L_P = L_Q + 256;
constexpr int ACC_LD = 132;
constexpr int L_ACC = L_P + 2 * 16 * 64 * 4;
constexpr int L_ML = L_ACC + 16 * ACC_LD * 4;
constexpr int L_EW = L_ML + 64;
constexpr int L_FLAG = L_EW + 32;
constexpr int F_Q = L_FLAG, F_ML = L_FLAG + 4, F_H = L_FLAG + 8, F_P0 = L_FLAG + 12, F_P1 = L_FLAG + 16, F_FF = L_FLAG + 20,
              F_TAGOK = L_FLAG + 24, F_TAG = L_FLAG + 28;
constexpr int L_H = L_FLAG + 32;
constexpr int L_BIG = L_H + 64;
constexpr int L_END = L_BIG + 16 * DM * 4;

__device__ __forceinline__ float bflo(unsigned u) { return __builtin_bit_cast(float, u << 16); }
__device__ __forceinline__ float bfhi(unsigned u) { return __builtin_bit_cast(float, u & 0xffff0000u); }
__device__ __forceinline__ unsigned pk2(float lo, float hi) {
  bf16x2_t r;
  r[0] = (__bf16)lo;
  r[1] = (__bf16)hi;
  return __builtin_bit_cast(unsigned, r);
}
__device__ __forceinline__ float rbf(float x) { return (float)(__bf16)x; }
__device__ __forceinline__ float dot2(unsigned a, unsigned b, float c) {
  return __builtin_amdgcn_fdot2_f32_bf16(__builtin_bit_cast(bf16x2_t, a), __builtin_bit_cast(bf16x2_t, b), c, false);
}
template <int CTRL> __device__ __forceinline__ float dppf(float x) {
  return __builtin_bit_cast(float, __builtin_amdgcn_mov_dpp(__builtin_bit_cast(int, x), CTRL, 0xf, 0xf, true));
}
template <int CTRL> __device__ __forceinline__ unsigned dppu(unsigned x) {
  return (unsigned)__builtin_amdgcn_mov_dpp((int)x, CTRL, 0xf, 0xf, true);
}
constexpr int XOR1 = 0xB1, XOR2 = 0x4E, XOR7 = 0x141, XOR8 = 0x128;
template <int CTRL> __device__ __forceinline__ float tr_merge(float lo, float hi, bool up) {
  const float t1 = lo + dppf<CTRL>(lo);
  const float t2 = hi + dppf<CTRL>(hi);
  return up ? t2 : t1;
}
__device__ __forceinline__ float row16_sum(float x) {
  x += dppf<XOR1>(x); x += dppf<XOR2>(x); x += dppf<XOR7>(x); x += dppf<XOR8>(x);
  return x;
}
__device__ __forceinline__ float row16_max(float x) {
  x = fmaxf(x, dppf<XOR1>(x)); x = fmaxf(x, dppf<XOR2>(x)); x = fmaxf(x, dppf<XOR7>(x)); x = fmaxf(x, dppf<XOR8>(x));
  return x;
}
__device__ __forceinline__ float half_sum(float x) {
  auto t = __builtin_amdgcn_permlane32_swap(__float_as_uint(x), __float_as_uint(x), false, false);
  return __uint_as_float(t[0]) + __uint_as_float(t[1]);
}
__device__ __forceinline__ float xrow_max(float x) {
  auto s = __builtin_amdgcn_permlane16_swap(__float_as_uint(x), __float_as_uint(x), false, false);
  x = fmaxf(__uint_as_float(s[0]), __uint_as_float(s[1]));
  auto t = __builtin_amdgcn_permlane32_swap(__float_as_uint(x), __float_as_uint(x), false, false);
  return fmaxf(__uint_as_float(t[0]), __uint_as_float(t[1]));
}
__device__ __forceinline__ float xrow_sum(float x) {
  auto s = __builtin_amdgcn_permlane16_swap(__float_as_uint(x), __float_as_uint(x), false, false);
  x = __uint_as_float(s[0]) + __uint_as_float(s[1]);
  return half_sum(x);
}
__device__ __forceinline__ f32x4 mfma(u32x4 a, u32x4 b, f32x4 c) {
  return __builtin_amdgcn_mfma_f32_16x16x32_bf16(__builtin_bit_cast(bf16x8, a), __builtin_bit_cast(bf16x8, b), c, 0, 0, 0);
}
#define LGKM0() asm volatile("s_waitcnt lgkmcnt(0)" ::: "memory")
#define WG_BARRIER()                \
  do {                              \
    LGKM0();                        \
    __builtin_amdgcn_s_barrier();   \
    asm volatile("" ::: "memory");  \
  } while (0)
#define SB() __builtin_amdgcn_sched_barrier(0)
#define STAMP(k)
#define STAMP_DECL
#define STAMP_OUT()
#define LDW(p) (*(p))
#define RSRC(p, n) __builtin_amdgcn_make_buffer_rsrc((void*)(p), 0, (n), 0x00020000)
#define FRESH(v) asm volatile("" : "+v"(v))
#define VMWAIT(n) asm volatile("s_waitcnt vmcnt(%0)" ::"i"(n) : "memory")

__device__ __forceinline__ void put(GAS u64* p, float v, unsigned tag) {
  __hip_atomic_store(p, ((u64)tag << 32) | (u64)__float_as_uint(v), RLX_AGENT);
}
__device__ __forceinline__ u64 get(const GAS u64* p) { return __hip_atomic_load(p, RLX_AGENT); }
__device__ __forceinline__ unsigned lds_ld(LAS unsigned char* p) { return __hip_atomic_load((LAS unsigned*)p, RLX_WG); }
__device__ __forceinline__ void lds_wait_ge(LAS unsigned char* p, unsigned want) {
  while (lds_ld(p) < want) __builtin_amdgcn_s_sleep(1);
  asm volatile("" ::: "memory");
}
template <int N, int SLP> __device__ __forceinline__ void sweep(const GAS u64* p, int S, unsigned tag, float (&v)[N]) {
  for (unsigned spins = 0;; ++spins) {
    bool ok = true;
#pragma unroll
    for (int k = 0; k < N; ++k) {
      const u64 x = get(p + k * S);
      v[k] = __uint_as_float((unsigned)x);
      ok &= (unsigned)(x >> 32) == tag;
    }
    if (__all(ok) || spins > SPIN_CAP) return;
    __builtin_amdgcn_s_sleep(SLP);
  }
}

struct Sel { unsigned s[4]; };
__device__ __forceinline__ Sel make_sel(int lane) {
  const int m = lane & 15;
  const unsigned src = (m & 8) ? 0x0706u : 0x0504u;
  const unsigned placed = (m & 1) ? ((src << 16) | 0x0c0cu) : (0x0c0c0000u | src);
  Sel r;
#pragma unroll
  for (int jj = 0; jj < 4; ++jj) r.s[jj] = (((m & 7) >> 1) == jj) ? placed : 0x0c0c0c0cu;
  return r;
}
__device__ __forceinline__ u32x4 make_a(unsigned X, const Sel& s) {
  u32x4 a;
#pragma unroll
  for (int jj = 0; jj < 4; ++jj) a[jj] = __builtin_amdgcn_perm(X, 0u, s.s[jj]);
  return a;
}
__device__ __forceinline__ unsigned split_hi_lo(float v) {
  const float h = rbf(v);
  return pk2(h, v - h);
}

__device__ __forceinline__ void ln_row(const unsigned (&xr)[5], LAS unsigned char* dst, int lane) {
  float sum = 0.f;
#pragma unroll
  for (int k = 0; k < 5; ++k) sum += bflo(xr[k]) + bfhi(xr[k]);
  const float mean = xrow_sum(row16_sum(sum)) * (1.0f / DM);
  float ss = 0.f;
#pragma unroll
  for (int k = 0; k < 5; ++k) {
    const float a = bflo(xr[k]) - mean, bb = bfhi(xr[k]) - mean;
    ss = fmaf(a, a, ss);
    ss = fmaf(bb, bb, ss);
  }
  const float var = xrow_sum(row16_sum(ss)) * (1.0f / DM);
  const float mu = rbf(mean);
  const float rinv = 1.0f / rbf(sqrtf(rbf(rbf(var) + 1e-5f)));
#pragma unroll
  for (int k = 0; k < 5; ++k)
    *(LAS unsigned*)(dst + 4 * (lane + 64 * k)) = pk2(rbf(bflo(xr[k]) - mu) * rinv, rbf(bfhi(xr[k]) - mu) * rinv);
}

__global__ void __launch_bounds__(1024)
k_main(const GAS unsigned short* __restrict__ x, const GAS unsigned short* __restrict__ kc, const GAS unsigned short* __restrict__ vc,
       const GAS unsigned short* __restrict__ wq, const GAS unsigned short* __restrict__ wo, const GAS unsigned short* __restrict__ wg,
       const GAS unsigned short* __restrict__ wu, const GAS unsigned short* __restrict__ wd, GAS u64* ws, GAS unsigned* out) {
  __shared__ __attribute__((aligned(16))) unsigned char smem[L_END];
  LAS unsigned char* L = (LAS unsigned char*)smem;
  const int tid = threadIdx.x, lane = tid & 63, wave = __builtin_amdgcn_readfirstlane(tid >> 6);
  const int g = lane >> 4, c = lane & 15;
  const int bid = blockIdx.x;
  const int jx = bid >> 3;
  const int h = (jx >> 3) * 8 + ((bid + HROT * (jx >> 3)) & 7), mem = jx & 7, b = mem >> 1, s = mem & 1;
  const int slot = (b * NH + h) * 2 + s;
  const int hs = (h < NSL || !EQUALIZE) ? h : 16 + ((bid + HROT * 2) & 7);
  STAMP_DECL;
  STAMP(0);
  if (tid < 8) *(LAS unsigned*)(L + L_FLAG + 4 * tid) = 0u;
  asm volatile("" ::"s"(x), "s"(kc), "s"(vc), "s"(wq), "s"(wo), "s"(wg), "s"(wu), "s"(wd), "s"(ws), "s"(out));
  WG_BARRIER();
  const Sel sel = make_sel(lane);

  GAS u64* const xme = ws + G_X + (size_t)slot * 64;
  const GAS u64* const xot = ws + G_X + (size_t)(slot ^ 1) * 64;

  if (wave >= 8) {
    const int w = wave - 8;
    const size_t base = ((size_t)(b * NH + h) * SK + (size_t)s * (SK / 2)) * DH;
    const GAS unsigned short* kb = kc + base;
    const GAS unsigned short* vb = vc + base;
    int voff = lane * 16 + w * 4096;
#define TILE_LD(r, so, t) __builtin_amdgcn_raw_buffer_load_b128((r), voff + ((t) & 3) * 1024, (so) + ((t) >> 2) * 32768, AUX_NT)
    u32x4 ring[16];
#define PACE_CHECK(t)
    {
      const __amdgpu_buffer_rsrc_t rk = RSRC(kb, SK / 2 * DH * 2);
#pragma unroll
      for (int t = 0; t < 16; ++t) {
        if (t >= DEPTH0 && t < DEPTH0_N) VMWAIT(DEPTH0 - 1);
        else if (t >= DEPTH) VMWAIT(DEPTH - 1);
        ring[t] = TILE_LD(rk, 0, t);
        SB();
      }
    }
    lds_wait_ge(L + F_Q, 2u);
    STAMP(1);
    const u32x4 q = *(const LAS u32x4*)(L + L_Q + 16 * c);
    float sc0 = 0.f, sc1 = 0.f, sc2 = 0.f, sc3 = 0.f;
#define MERGE(LO, HI, CTRL, BIT) tr_merge<CTRL>((LO), (HI), (c & (BIT)) != 0)
#pragma unroll 1
    for (int grp = 0; grp < 4; ++grp) {
      const __amdgpu_buffer_rsrc_t rn = RSRC(grp < 3 ? kb : vb, SK / 2 * DH * 2);
      const int so = ((grp + 1) & 3) * 131072;
      FRESH(voff);
      float d1 = 0.f, d2 = 0.f, d4 = 0.f, d8 = 0.f;
#pragma unroll
      for (int t = 0; t < 16; ++t) {
        float a = dot2(ring[t][0], q[0], 0.f);
        a = dot2(ring[t][1], q[1], a);
        a = dot2(ring[t][2], q[2], a);
        a = dot2(ring[t][3], q[3], a);
        FRESH(a);
        SB();
        PACE_CHECK(t)
        VMWAIT(DEPTH - 1);
        ring[t] = TILE_LD(rn, so, t);
        if (!(t & 1)) d1 = a;
        else {
          a = MERGE(d1, a, XOR8, 8);
          if (!(t & 2)) d2 = a;
          else {
            a = MERGE(d2, a, XOR7, 4);
            if (!(t & 4)) d4 = a;
            else {
              a = MERGE(d4, a, XOR2, 2);
              if (!(t & 8)) d8 = a;
              else {
                a = MERGE(d8, a, XOR1, 1);
                sc0 = sc1; sc1 = sc2; sc2 = sc3;
                sc3 = rbf(rbf(a) * QSCALE) * LOG2E;
              }
            }
          }
        }
        SB();
      }
    }
#undef MERGE
    STAMP(2);
    float mw = fmaxf(fmaxf(sc0, sc1), fmaxf(sc2, sc3));
    mw = xrow_max(row16_max(mw));
    const float p0 = __builtin_amdgcn_exp2f(sc0 - mw), p1 = __builtin_amdgcn_exp2f(sc1 - mw);
    const float p2 = __builtin_amdgcn_exp2f(sc2 - mw), p3 = __builtin_amdgcn_exp2f(sc3 - mw);
    unsigned pk0 = split_hi_lo(p0), pk1 = split_hi_lo(p1), pk2_ = split_hi_lo(p2), pk3 = split_hi_lo(p3);
    const float lw = xrow_sum(row16_sum((p0 + p1) + (p2 + p3)));
    if (lane == 0) {
      *(LAS float*)(L + L_ML + w * 4) = mw;
      *(LAS float*)(L + L_ML + 32 + w * 4) = lw;
      LGKM0();
      __hip_atomic_fetch_add((LAS unsigned*)(L + F_ML), 1u, RLX_WG);
    }
    SB();
    f32x4 acc = {0.f, 0.f, 0.f, 0.f};
#define BREV4(T) ((((T) & 1) << 3) | (((T) & 2) << 1) | (((T) & 4) >> 1) | (((T) & 8) >> 3))
#define PV_STEP(T, REFILL)                              \
  {                                                     \
    const unsigned X = dppu<0x150 + BREV4(T)>(pk0);     \
    acc = mfma(make_a(X, sel), ring[T], acc);           \
    if (REFILL) {                                       \
      SB();                                             \
      PACE_CHECK(T)                                     \
      VMWAIT(DEPTH - 1);                                \
      ring[T] = TILE_LD(rv, so, T);                     \
    }                                                   \
    SB();                                               \
  }
#define PV_GROUP(R)                                                                                         \
  PV_STEP(0, R) PV_STEP(1, R) PV_STEP(2, R) PV_STEP(3, R) PV_STEP(4, R) PV_STEP(5, R) PV_STEP(6, R) PV_STEP(7, R) \
  PV_STEP(8, R) PV_STEP(9, R) PV_STEP(10, R) PV_STEP(11, R) PV_STEP(12, R) PV_STEP(13, R) PV_STEP(14, R) PV_STEP(15, R)
    const __amdgpu_buffer_rsrc_t rv = RSRC(vb, SK / 2 * DH * 2);
#pragma unroll 1
    for (int grp = 0; grp < 3; ++grp) {
      const int so = (grp + 1) * 131072;
      FRESH(voff);
      PV_GROUP(true)
      pk0 = pk1; pk1 = pk2_; pk2_ = pk3;
    }
    {
      const int so = 0;
      PV_GROUP(false)
    }
#undef PV_GROUP
#undef PV_STEP
    STAMP(3);
    *(LAS f32x4*)(L + L_ACC + ((w * 2 + (g >> 1)) * ACC_LD + 8 * c + 4 * (g & 1)) * 4) = acc;
    WG_BARRIER();
    {
      const int j = h * 2 + s;
      if (j >= 40 || !ABL_FINAL) { STAMP_OUT(); return; }
      const unsigned xw = ((const GAS unsigned*)x)[(b * DM + 16 * j) / 2 + (lane & 7)];
      const unsigned tag = lds_ld(L + F_TAG);
      WG_BARRIER();
      STAMP(4);
      const __amdgpu_buffer_rsrc_t ry = RSRC(ws + G_Y + (size_t)(b * 64 + 8 * w) * DM + 16 * j, 8 * DM * 8);
      u32x4 yv = {0u, 0u, 0u, 0u};
      bool miss = true;
      for (unsigned spins = 0; spins < SPIN_CAP; ++spins) {
        if (miss) {
          yv = __builtin_amdgcn_raw_buffer_load_b128(ry, (lane >> 3) * (DM * 8) + (lane & 7) * 16, 0, AUX_SC1);
          miss = yv[1] != tag || yv[3] != tag;
        }
        const int nmiss = __builtin_popcountll(__builtin_amdgcn_ballot_w64(miss));
        if (nmiss == 0) break;
        if (nmiss > YPOLL_N) __builtin_amdgcn_s_sleep(YPOLL_S); else __builtin_amdgcn_s_sleep(YPOLL_S0);
      }
      float s0 = __uint_as_float(yv[0]), s1 = __uint_as_float(yv[2]);
      s0 += dppf<XOR8>(s0); s1 += dppf<XOR8>(s1);
      s0 = xrow_sum(s0); s1 = xrow_sum(s1);
      if (lane < 8) *(LAS f32x2*)(L + L_ACC + (w * 8 + lane) * 8) = f32x2{s0, s1};
      LGKM0();
      unsigned old = 0;
      if (lane == 0) old = __hip_atomic_fetch_add((LAS unsigned*)(L + F_TAGOK), 1u, RLX_WG);
      asm volatile("" ::: "memory");
      if (__builtin_amdgcn_readfirstlane(old) == 7u) {
        const f32x2 t = *(const LAS f32x2*)(L + L_ACC + lane * 8);
        s0 = t[0]; s1 = t[1];
        s0 += dppf<XOR8>(s0); s1 += dppf<XOR8>(s1);
        s0 = xrow_sum(s0); s1 = xrow_sum(s1);
        if (lane < 8) out[(b * DM + 16 * j) / 2 + lane] = pk2(bflo(xw) + s0, bfhi(xw) + s1);
      }
      STAMP(5);
      STAMP_OUT();
      return;
    }
    if (w >= 5 || !ABL_FINAL) { STAMP_OUT(); return; }
    const int j = h * 2 + s;
    const unsigned xw = ((const GAS unsigned*)x)[(b * DM + 10 * j) / 2 + w];
    const unsigned tag = lds_ld(L + F_TAG);
    WG_BARRIER();
    STAMP(4);
    const __amdgpu_buffer_rsrc_t ry = RSRC(ws + G_Y + ((size_t)(b * 64 + j) * 5 + w) * 128, 1024);
    u32x4 yv = {0u, 0u, 0u, 0u};
    bool miss = true;
    for (unsigned spins = 0; spins < SPIN_CAP; ++spins) {
      if (miss) {
        yv = __builtin_amdgcn_raw_buffer_load_b128(ry, lane * 16, 0, AUX_SC1);
        miss = yv[1] != tag || yv[3] != tag;
      }
      const int nmiss = __builtin_popcountll(__builtin_amdgcn_ballot_w64(miss));
      if (nmiss == 0) break;
      if (nmiss > POLL_N2) __builtin_amdgcn_s_sleep(POLL_S2);
      else if (nmiss > POLL_N1) __builtin_amdgcn_s_sleep(POLL_S1);
    }
    const float s0 = xrow_sum(row16_sum(__uint_as_float(yv[0]))), s1 = xrow_sum(row16_sum(__uint_as_float(yv[2])));
    if (lane == 0) out[(b * DM + 10 * j) / 2 + w] = pk2(bflo(xw) + s0, bfhi(xw) + s1);
    STAMP(5);
    STAMP_OUT();
    return;
  }

  const int hw = wave;
  __builtin_amdgcn_s_setprio(3);
  u32x4 wr[20];
  unsigned xp[10];
#define PROJ_MFMA(PH)                                                                                              \
  {                                                                                                                \
    f32x4 ga = {0.f, 0.f, 0.f, 0.f};                                                                               \
    _Pragma("unroll") for (int i = 0; i < 10; ++i) ga = mfma(make_a(xp[i], sel), wr[i], ga);                       \
    if ((g >> 1) == (c >> 3))                                                                                      \
      *(LAS f32x4*)(L + L_P + ((PH) * 1024 + (hw * 2 + (g >> 1)) * 64 + 8 * (c & 7) + 4 * (g & 1)) * 4) = ga;      \
    LGKM0();                                                                                                       \
    if (lane == 0) __hip_atomic_fetch_add((LAS unsigned*)(L + ((PH) ? F_P1 : F_P0)), 1u, RLX_WG);                   \
  }
#define PROJ_SUM(PH, R0, R1)                                                                                       \
  {                                                                                                                \
    lds_wait_ge(L + ((PH) ? F_P1 : F_P0), 8u);                                                                     \
    f32x4 t4 = {0.f, 0.f, 0.f, 0.f};                                                                               \
    _Pragma("unroll") for (int k = 0; k < 4; ++k) t4 += *(const LAS f32x4*)(L + L_P + ((PH) * 1024 + (4 * g + k) * 64 + 4 * c) * 4); \
    _Pragma("unroll") for (int k = 0; k < 4; ++k) t4[k] = xrow_sum(t4[k]);                                         \
    R0 = pk2(t4[0], t4[1]);                                                                                        \
    R1 = pk2(t4[2], t4[3]);                                                                                        \
  }
  unsigned tag = 0;
  {
    unsigned xr[5];
#pragma unroll
    for (int k = 0; k < 5; ++k) xr[k] = LDW((const GAS unsigned*)(x + b * DM) + lane + 64 * k);
    unsigned e = 0;
    if (hw < 2) e = LDW((const GAS unsigned*)(ws + G_E));
    const __amdgpu_buffer_rsrc_t rq = RSRC(wq + (size_t)80 * hw * (NH * DH) + h * DH + 64 * s, 80 * NH * DH * 2 - (h * DH + 64 * s) * 2);
    const int vq = ((20 * g + (c >> 3)) * NH * DH + 8 * (c & 7)) * 2;
#pragma unroll
    for (int i = 0; i < 10; ++i) {
      wr[i] = __builtin_amdgcn_raw_buffer_load_b128(rq, vq, i * 2 * NH * DH * 2, WQ_AUX);
      SB();
    }
    ln_row(xr, L + L_XQ, lane);
    LGKM0();
#pragma unroll
    for (int k = 0; k < 5; ++k) {
      const u32x2 t = *(const LAS u32x2*)(L + L_XQ + (80 * hw + 20 * g + 4 * k) * 2);
      xp[2 * k] = t[0];
      xp[2 * k + 1] = t[1];
    }
    PROJ_MFMA(0)
    STAMP(1);
    if (hw < 2) tag = __builtin_amdgcn_readfirstlane(e) + 1u;
    if (hw == 0) {
      unsigned r0, r1;
      PROJ_SUM(0, r0, r1)
      if (g == 0) {
        __builtin_amdgcn_raw_buffer_store_b128(u32x4{r0, tag, r1, tag}, RSRC(xme, 512), c * 16, 0, AUX_SC1);
        *(LAS u32x2*)(L + L_Q + 128 * s + 8 * c) = u32x2{r0, r1};
      }
      if (lane == 0) *(LAS unsigned*)(L + F_TAG) = tag;
      LGKM0();
      if (lane == 0) {
        __hip_atomic_fetch_add((LAS unsigned*)(L + F_Q), 1u, RLX_WG);
      }
    } else if (hw == 1) {
      lds_wait_ge(L + F_P0, 8u);
      float v[1];
      sweep<1, 1>(xot + (lane & 31), 1, tag, v);
      if (lane < 32) *(LAS float*)(L + L_Q + 128 * (s ^ 1) + 4 * lane) = v[0];
      LGKM0();
      if (lane == 0) __hip_atomic_fetch_add((LAS unsigned*)(L + F_Q), 1u, RLX_WG);
      STAMP(6);
    }
  }
  __builtin_amdgcn_s_setprio(0);
  lds_wait_ge(L + F_Q, 2u);
  tag = __builtin_amdgcn_readfirstlane(lds_ld(L + F_TAG));
  if (hs < NSL) {
    const __amdgpu_buffer_rsrc_t rg = RSRC((s ? wu : wg) + (size_t)80 * hw * FF + 64 * hs, 80 * FF * 2 - 64 * hs * 2);
    const int vg = ((20 * g + (c >> 3)) * FF + 8 * (c & 7)) * 2;
#pragma unroll
    for (int i = 0; i < 10; ++i) {
      VMWAIT(GU_DEPTH - 1);
      wr[i] = __builtin_amdgcn_raw_buffer_load_b128(rg, vg, i * 2 * FF * 2, W_AUX);
      SB();
    }
    PROJ_MFMA(1)
    if (hw == 2) {
      unsigned r0, r1;
      PROJ_SUM(1, r0, r1)
      if (g == 0 && (c >> 3) != s) __builtin_amdgcn_raw_buffer_store_b128(u32x4{r0, tag, r1, tag}, RSRC(xme + 32, 128), (c & 7) * 16, 0, AUX_SC1);
      float v[2];
      sweep<2, 16>(xot + 32 + 2 * (c & 7), 1, tag, v);
      const unsigned o0 = __float_as_uint(v[0]), o1 = __float_as_uint(v[1]);
      if (g == 0 && (c >> 3) == s)
        *(LAS u32x2*)(L + L_H + 8 * (c & 7)) = u32x2{pk2(bflo(r0) * bflo(o0), bfhi(r0) * bfhi(o0)), pk2(bflo(r1) * bflo(o1), bfhi(r1) * bfhi(o1))};
      LGKM0();
      if (lane == 0) __hip_atomic_store((LAS unsigned*)(L + F_H), 1u, RLX_WG);
    }
  }
  STAMP(2);
  {
    const __amdgpu_buffer_rsrc_t ro = RSRC(wo + (size_t)(h * DH + 16 * hw) * DM, 16 * DM * 2);
    const int vo = (4 * g * DM + 8 * c) * 2;
#pragma unroll
    for (int j = 0; j < 4; ++j)
#pragma unroll
      for (int cb = 0; cb < 5; ++cb) {
        VMWAIT(WO_DEPTH - 1);
        wr[j * 5 + cb] = __builtin_amdgcn_raw_buffer_load_b128(ro, vo, (j * DM + cb * 128) * 2, W_AUX);
        SB();
      }
  }
  f32x2 ysum = {0.f, 0.f};
  if (hs < NSL) {
    u32x4 wdr[5];
    const __amdgpu_buffer_rsrc_t rd = RSRC(wd + (size_t)(64 * hs + 32 * s + 4 * hw) * DM, 4 * DM * 2);
#pragma unroll
    for (int cb = 0; cb < 5; ++cb) {
      VMWAIT(WO_DEPTH - 1);
      wdr[cb] = __builtin_amdgcn_raw_buffer_load_b128(rd, (g * DM + 8 * c) * 2, cb * 256, W_AUX);
      SB();
    }
    lds_wait_ge(L + F_H, 1u);
    const unsigned X = *(const LAS unsigned short*)(L + L_H + 2 * (4 * hw + g));
    const u32x4 a = make_a(X, sel);
#pragma unroll
    for (int cb = 0; cb < 5; ++cb) {
      const f32x4 yf = mfma(a, wdr[cb], f32x4{0.f, 0.f, 0.f, 0.f});
      if (g < 2) *(LAS f32x4*)(L + L_BIG + (hw * DM + 128 * cb + 8 * c + 4 * g) * 4) = yf;
    }
    LGKM0();
    if (lane == 0) __hip_atomic_fetch_add((LAS unsigned*)(L + F_FF), 1u, RLX_WG);
    if (hw < 5 && h < NSL) {
      lds_wait_ge(L + F_FF, 8u);
#pragma unroll
      for (int p = 0; p < 8; ++p) ysum += *(const LAS f32x2*)(L + L_BIG + (p * DM + 2 * tid) * 4);
    }
  }
  STAMP(7);
  if (hw == 6) {
    lds_wait_ge(L + F_ML, 8u);
    const float mv = *(const LAS float*)(L + L_ML + 4 * (lane & 7)), lv = *(const LAS float*)(L + L_ML + 32 + 4 * (lane & 7));
    float M = fmaxf(mv, dppf<XOR1>(mv));
    M = fmaxf(M, dppf<XOR2>(M));
    M = fmaxf(M, dppf<XOR7>(M));
    float Ls = __builtin_amdgcn_exp2f(mv - M) * lv;
    Ls += dppf<XOR1>(Ls);
    Ls += dppf<XOR2>(Ls);
    Ls += dppf<XOR7>(Ls);
    if (lane < 2) put(xme + 48 + lane, lane == 0 ? M : Ls, tag);
    float o2[2];
    sweep<2, 16>(xot + 48, 1, tag, o2);
    const float Mt = fmaxf(M, o2[0]);
    const float e0 = __builtin_amdgcn_exp2f(M - Mt) * Ls, e1 = __builtin_amdgcn_exp2f(o2[0] - Mt) * o2[1];
    if (lane < 8) *(LAS float*)(L + L_EW + 4 * lane) = __builtin_amdgcn_exp2f(mv - Mt) / (e0 + e1);
    STAMP(5);
  } else if (hw == 7 && bid == 0) {
    lds_wait_ge(L + F_ML, 8u);
    float f4[4];
    sweep<4, 64>(ws + G_X + lane * 64 + 48, 64 * 64, tag, f4);
    if (lane == 0) __hip_atomic_store((GAS unsigned*)(ws + G_E), tag, RLX_AGENT);
  }
  WG_BARRIER();
  STAMP(3);
  {
    f32x4 o = *(const LAS f32x4*)(L + L_ACC + (c * ACC_LD + 16 * hw + 4 * g) * 4) * *(const LAS float*)(L + L_EW + 4 * (c >> 1));
#pragma unroll
    for (int j = 0; j < 4; ++j) o[j] = row16_sum(o[j]);
    f32x4 y[5];
#pragma unroll
    for (int cb = 0; cb < 5; ++cb) y[cb] = f32x4{0.f, 0.f, 0.f, 0.f};
#pragma unroll
    for (int j = 0; j < 4; ++j) {
      const u32x4 a = make_a(split_hi_lo(o[j]), sel);
#pragma unroll
      for (int cb = 0; cb < 5; ++cb) y[cb] = mfma(a, wr[j * 5 + cb], y[cb]);
    }
#pragma unroll
    for (int cb = 0; cb < 5; ++cb)
      *(LAS f32x4*)(L + L_BIG + ((hw * 2 + (g >> 1)) * DM + 128 * cb + 8 * c + 4 * (g & 1)) * 4) = y[cb];
  }
  WG_BARRIER();
  if (tid < DM / 2 && ABL_FINAL) {
    f32x2 t = ysum;
#pragma unroll
    for (int p = 0; p < 16; ++p) t += *(const LAS f32x2*)(L + L_BIG + (p * DM + 2 * tid) * 4);
    __builtin_amdgcn_raw_buffer_store_b128(u32x4{__float_as_uint(t[0]), tag, __float_as_uint(t[1]), tag},
                                           RSRC(ws + G_Y + (size_t)(b * 64 + h * 2 + s) * DM, DM * 8), tid * 16, 0, AUX_SC1);
  }
  STAMP(4);
  STAMP_OUT();
}

extern "C" void kernel_launch(void* const* d_in, const int* in_sizes, int n_in, void* d_out, int out_size, void* d_ws,
                              size_t ws_size, hipStream_t stream) {
  (void)in_sizes; (void)n_in; (void)out_size; (void)ws_size;
  typedef const GAS unsigned short* cp;
  k_main<<<NWG, 1024, 0, stream>>>((cp)d_in[0], (cp)d_in[1], (cp)d_in[2], (cp)d_in[3], (cp)d_in[4], (cp)d_in[6], (cp)d_in[7],
                                   (cp)d_in[8], (GAS u64*)d_ws, (GAS unsigned*)d_out);
}
```

```cpp
#include <hip/hip_runtime.h>
#include <hip/hip_bf16.h>
#include <stdint.h>

#define DEPTH 4
#define GU_DEPTH 6
#define WO_DEPTH 6
#define WQ_AUX 0
#define W_AUX 0
#define X_NT 0
#define EQUALIZE 1
#define START_SLEEP 0
#define DEPTH0 4
#define DEPTH0_N 0
#define PACE 112
#define PACE0 450
#define YMODE 1
#define YPOLL_N 8
#define YPOLL_S 8
#define YPOLL_S0 1
#define EXT 0
#define ABL_SIDE 0
#define ROWMAP 0
#define ROWG 20
#define ROWI 2
#define WQ_DEPTH 10
#define TAILFREE 8
#define TAILDEPTH 16
#define O_LO 0
#define LATE_B0 0
#define LATE_H0 2
#define STAG1_0 450
#define STAG1 100
#define STAG2_0 1500
#define STAG2 170
#define BURST_PRIO 3
#define PAUSE_EVERY 1
#define FT_DEPTH 6
#define PACE_N 4
#define PSHIFT 0
#define AHEAD 0u
#define HROT 0
#define POLL_N1 2
#define POLL_S1 8
#define POLL_N2 8
#define POLL_S2 32
#define ABL_FINAL 1
constexpr int AUX_NT = 2, AUX_SC1 = 16;

typedef unsigned u32x4 __attribute__((ext_vector_type(4)));
typedef unsigned u32x2 __attribute__((ext_vector_type(2)));
typedef float f32x4 __attribute__((ext_vector_type(4)));
typedef float f32x2 __attribute__((ext_vector_type(2)));
typedef short bf16x8 __attribute__((ext_vector_type(8)));
typedef __bf16 bf16x2_t __attribute__((ext_vector_type(2)));
typedef unsigned long long u64;
#define GAS __attribute__((address_space(1)))
#define LAS __attribute__((address_space(3)))
#define RLX_AGENT __ATOMIC_RELAXED, __HIP_MEMORY_SCOPE_AGENT
#define RLX_WG __ATOMIC_RELAXED, __HIP_MEMORY_SCOPE_WORKGROUP

constexpr int NB = 4, DM = 640, NH = 32, SK = 4096, DH = 128, FF = 1728;
constexpr int NSL = FF / 64;
constexpr int NWG = 256;
constexpr float LOG2E = 1.4426950408889634f;
constexpr float QSCALE = 0.08838834764831845f;
constexpr unsigned SPIN_CAP = 1u << 18;

constexpr size_t G_E = 0;
constexpr size_t G_X = 32;
constexpr size_t G_Y = G_X + (size_t)NWG * 64;

constexpr int L_XQ = 0;
constexpr int L_Q = L_XQ + DM * 2;
constexpr int L_P = L_Q + 256;
constexpr int ACC_LD = 132;
constexpr int L_ACC = L_P + 2 * 16 * 64 * 4;
constexpr int L_ML = L_ACC + 16 * ACC_LD * 4;
constexpr int L_EW = L_ML + 64;
constexpr int L_FLAG = L_EW + 32;
constexpr int F_Q = L_FLAG, F_ML = L_FLAG + 4, F_H = L_FLAG + 8, F_P0 = L_FLAG + 12, F_P1 = L_FLAG + 16, F_FF = L_FLAG + 20,
              F_TAGOK = L_FLAG + 24, F_TAG = L_FLAG + 28, F_PAUSE = L_FLAG + 32;
constexpr int L_H = L_FLAG + 64;
constexpr int L_BIG = L_H + 64;
constexpr int L_END = L_BIG + 8 * DM * 4;

__device__ __forceinline__ float bflo(unsigned u) { return __builtin_bit_cast(float, u << 16); }
__device__ __forceinline__ float bfhi(unsigned u) { return __builtin_bit_cast(float, u & 0xffff0000u); }
__device__ __forceinline__ unsigned pk2(float lo, float hi) {
  bf16x2_t r;
  r[0] = (__bf16)lo;
  r[1] = (__bf16)hi;
  return __builtin_bit_cast(unsigned, r);
}
__device__ __forceinline__ float rbf(float x) { return (float)(__bf16)x; }
__device__ __forceinline__ float dot2(unsigned a, unsigned b, float c) {
  return __builtin_amdgcn_fdot2_f32_bf16(__builtin_bit_cast(bf16x2_t, a), __builtin_bit_cast(bf16x2_t, b), c, false);
}
template <int CTRL> __device__ __forceinline__ float dppf(float x) {
  return __builtin_bit_cast(float, __builtin_amdgcn_mov_dpp(__builtin_bit_cast(int, x), CTRL, 0xf, 0xf, true));
}
template <int CTRL> __device__ __forceinline__ unsigned dppu(unsigned x) {
  return (unsigned)__builtin_amdgcn_mov_dpp((int)x, CTRL, 0xf, 0xf, true);
}
constexpr int XOR1 = 0xB1, XOR2 = 0x4E, XOR7 = 0x141, XOR8 = 0x128;
template <int CTRL> __device__ __forceinline__ float tr_merge(float lo, float hi, bool up) {
  const float t1 = lo + dppf<CTRL>(lo);
  const float t2 = hi + dppf<CTRL>(hi);
  return up ? t2 : t1;
}
__device__ __forceinline__ float row16_sum(float x) {
  x += dppf<XOR1>(x); x += dppf<XOR2>(x); x += dppf<XOR7>(x); x += dppf<XOR8>(x);
  return x;
}
__device__ __forceinline__ float row16_max(float x) {
  x = fmaxf(x, dppf<XOR1>(x)); x = fmaxf(x, dppf<XOR2>(x)); x = fmaxf(x, dppf<XOR7>(x)); x = fmaxf(x, dppf<XOR8>(x));
  return x;
}
__device__ __forceinline__ float half_sum(float x) {
  auto t = __builtin_amdgcn_permlane32_swap(__float_as_uint(x), __float_as_uint(x), false, false);
  return __uint_as_float(t[0]) + __uint_as_float(t[1]);
}
__device__ __forceinline__ float xrow_max(float x) {
  auto s = __builtin_amdgcn_permlane16_swap(__float_as_uint(x), __float_as_uint(x), false, false);
  x = fmaxf(__uint_as_float(s[0]), __uint_as_float(s[1]));
  auto t = __builtin_amdgcn_permlane32_swap(__float_as_uint(x), __float_as_uint(x), false, false);
  return fmaxf(__uint_as_float(t[0]), __uint_as_float(t[1]));
}
__device__ __forceinline__ float xrow_sum(float x) {
  auto s = __builtin_amdgcn_permlane16_swap(__float_as_uint(x), __float_as_uint(x), false, false);
  x = __uint_as_float(s[0]) + __uint_as_float(s[1]);
  return half_sum(x);
}
__device__ __forceinline__ f32x4 mfma(u32x4 a, u32x4 b, f32x4 c) {
  return __builtin_amdgcn_mfma_f32_16x16x32_bf16(__builtin_bit_cast(bf16x8, a), __builtin_bit_cast(bf16x8, b), c, 0, 0, 0);
}
#define LGKM0() asm volatile("s_waitcnt lgkmcnt(0)" ::: "memory")
#define WG_BARRIER()                \
  do {                              \
    LGKM0();                        \
    __builtin_amdgcn_s_barrier();   \
    asm volatile("" ::: "memory");  \
  } while (0)
#define SB() __builtin_amdgcn_sched_barrier(0)
#define STAMP(k)
#define STAMP_DECL
#define STAMP_OUT()
#define LDW(p) (*(p))
#define RSRC(p, n) __builtin_amdgcn_make_buffer_rsrc((void*)(p), 0, (n), 0x00020000)
#define FRESH(v) asm volatile("" : "+v"(v))
#define VMWAIT_UNLESS(n, v, k, m) \
  asm volatile("s_waitcnt vmcnt(%3)\n\ts_cmp_eq_u32 %1, %2\n\ts_cbranch_scc1 1f\n\ts_waitcnt vmcnt(%0)\n1:" ::"i"(n), "s"(v), "i"(k), "i"(m) : "memory", "scc")
#define VMWAIT(n) asm volatile("s_waitcnt vmcnt(%0)" ::"i"(n) : "memory")

__device__ __forceinline__ void put(GAS u64* p, float v, unsigned tag) {
  __hip_atomic_store(p, ((u64)tag << 32) | (u64)__float_as_uint(v), RLX_AGENT);
}
__device__ __forceinline__ u64 get(const GAS u64* p) { return __hip_atomic_load(p, RLX_AGENT); }
__device__ __forceinline__ unsigned lds_ld(LAS unsigned char* p) { return __hip_atomic_load((LAS unsigned*)p, RLX_WG); }
__device__ __forceinline__ void lds_wait_ge(LAS unsigned char* p, unsigned want) {
  while (lds_ld(p) < want) __builtin_amdgcn_s_sleep(1);
  asm volatile("" ::: "memory");
}
template <int N, int SLP> __device__ __forceinline__ void sweep(const GAS u64* p, int S, unsigned tag, float (&v)[N]) {
  for (unsigned spins = 0;; ++spins) {
    bool ok = true;
#pragma unroll
    for (int k = 0; k < N; ++k) {
      const u64 x = get(p + k * S);
      v[k] = __uint_as_float((unsigned)x);
      ok &= (unsigned)(x >> 32) == tag;
    }
    if (__all(ok) || spins > SPIN_CAP) return;
    __builtin_amdgcn_s_sleep(SLP);
  }
}

struct Sel { unsigned s[4]; };
__device__ __forceinline__ Sel make_sel(int lane) {
  const int m = lane & 15;
  const unsigned src = (m & 8) ? 0x0706u : 0x0504u;
  const unsigned placed = (m & 1) ? ((src << 16) | 0x0c0cu) : (0x0c0c0000u | src);
  Sel r;
#pragma unroll
  for (int jj = 0; jj < 4; ++jj) r.s[jj] = (((m & 7) >> 1) == jj) ? placed : 0x0c0c0c0cu;
  return r;
}
__device__ __forceinline__ u32x4 make_a(unsigned X, const Sel& s) {
  u32x4 a;
#pragma unroll
  for (int jj = 0; jj < 4; ++jj) a[jj] = __builtin_amdgcn_perm(X, 0u, s.s[jj]);
  return a;
}
__device__ __forceinline__ unsigned split_hi_lo(float v) {
  const float h = rbf(v);
  return pk2(h, v - h);
}

__device__ __forceinline__ void ln_row(const unsigned (&xr)[5], LAS unsigned char* dst, int lane) {
  float sum = 0.f;
#pragma unroll
  for (int k = 0; k < 5; ++k) sum += bflo(xr[k]) + bfhi(xr[k]);
  const float mean = xrow_sum(row16_sum(sum)) * (1.0f / DM);
  float ss = 0.f;
#pragma unroll
  for (int k = 0; k < 5; ++k) {
    const float a = bflo(xr[k]) - mean, bb = bfhi(xr[k]) - mean;
    ss = fmaf(a, a, ss);
    ss = fmaf(bb, bb, ss);
  }
  const float var = xrow_sum(row16_sum(ss)) * (1.0f / DM);
  const float mu = rbf(mean);
  const float rinv = 1.0f / rbf(sqrtf(rbf(rbf(var) + 1e-5f)));
#pragma unroll
  for (int k = 0; k < 5; ++k)
    *(LAS unsigned*)(dst + 4 * (lane + 64 * k)) = pk2(rbf(bflo(xr[k]) - mu) * rinv, rbf(bfhi(xr[k]) - mu) * rinv);
}

__global__ void __launch_bounds__(1024)
k_main(const GAS unsigned short* __restrict__ x, const GAS unsigned short* __restrict__ kc, const GAS unsigned short* __restrict__ vc,
       const GAS unsigned short* __restrict__ wq, const GAS unsigned short* __restrict__ wo, const GAS unsigned short* __restrict__ wg,
       const GAS unsigned short* __restrict__ wu, const GAS unsigned short* __restrict__ wd, GAS u64* ws, GAS unsigned* out) {
  __shared__ __attribute__((aligned(16))) unsigned char smem[L_END];
  LAS unsigned char* L = (LAS unsigned char*)smem;
  const int tid = threadIdx.x, lane = tid & 63, wave = __builtin_amdgcn_readfirstlane(tid >> 6);
  const int g = lane >> 4, c = lane & 15;
  const int bid = blockIdx.x;
  const int jx = bid >> 3;
  const int h = (jx >> 3) * 8 + ((bid + HROT * (jx >> 3)) & 7), mem = jx & 7, b = mem >> 1, s = mem & 1;
  const int slot = (b * NH + h) * 2 + s;
  const int hs = h < NSL ? h : 16 + ((bid + HROT * 2) & 7);
  STAMP_DECL;
  STAMP(0);
  if (tid < 16) *(LAS unsigned*)(L + L_FLAG + 4 * tid) = 0u;
  asm volatile("" ::"s"(x), "s"(kc), "s"(vc), "s"(wq), "s"(wo), "s"(wg), "s"(wu), "s"(wd), "s"(ws), "s"(out));
  if (!LATE_B0) WG_BARRIER();
  const Sel sel = make_sel(lane);

  GAS u64* const xme = ws + G_X + (size_t)slot * 64;
  const GAS u64* const xot = ws + G_X + (size_t)(slot ^ 1) * 64;

  if (wave >= 8) {
    const int w = wave - 8;
    const size_t base = ((size_t)(b * NH + h) * SK + (size_t)s * (SK / 2)) * DH;
    const GAS unsigned short* kb = kc + base;
    const GAS unsigned short* vb = vc + base;
    int voff = lane * 16 + w * 4096;
#define TILE_LD(r, so, t) __builtin_amdgcn_raw_buffer_load_b128((r), voff + ((t) & 3) * 1024, (so) + ((t) >> 2) * 32768, AUX_NT)
    u32x4 ring[16];
    unsigned pz = 0, pstep = PACE;
    unsigned tgt = (unsigned)__builtin_amdgcn_s_memrealtime() + PACE0 + w * (PACE / 8);
    const unsigned tb2 = tgt + STAG2_0 + ((mem + 2 * (jx >> 3)) & 7) * STAG2;
#define CLOCK_CHECK(t)                                                                                   \
  if (((t) % PACE_N) == 0) {                                                                             \
    tgt += ((t) >= 16 - TAILFREE + 1) ? pstep : PACE;                                                    \
    for (;;) {                                                                                           \
      const unsigned now = (unsigned)__builtin_amdgcn_s_memrealtime();                                   \
      if ((int)(now + ((int)(now - tb2) < 0 ? AHEAD : 0u) - tgt) >= 0) break;                            \
      __builtin_amdgcn_s_sleep(1);                                                                       \
    }                                                                                                    \
  }
#define PACE_CHECK(t)                                                      \
  CLOCK_CHECK(t)                                                           \
  if (((t) % PAUSE_EVERY) == 0) {                                          \
    if (__builtin_amdgcn_readfirstlane(pz) != 0u) {                        \
      if (PSHIFT) tgt -= (unsigned)__builtin_amdgcn_s_memrealtime();       \
      while (__builtin_amdgcn_readfirstlane(lds_ld(L + F_PAUSE)) != 0u) __builtin_amdgcn_s_sleep(2); \
      if (PSHIFT) tgt += (unsigned)__builtin_amdgcn_s_memrealtime();       \
    }                                                                      \
  }
#define PACE_NEXT(t) if ((((t) + 1) % PAUSE_EVERY) == 0) pz = lds_ld(L + F_PAUSE);
    u32x4 ext[EXT + 1];
    {
      const __amdgpu_buffer_rsrc_t rk = RSRC(kb, SK / 2 * DH * 2);
#pragma unroll
      for (int t = 0; t < 16; ++t) {
        if (LATE_B0 && t == LATE_B0) WG_BARRIER();
        if (t >= DEPTH) VMWAIT(DEPTH - 1);
        ring[t] = TILE_LD(rk, 0, t);
        SB();
      }
#pragma unroll
      for (int t = 0; t < EXT; ++t) {
        VMWAIT(DEPTH - 1);
        ext[t] = TILE_LD(rk, 131072, t);
        SB();
      }
    }
    STAMP(6);
    if (!ABL_SIDE) lds_wait_ge(L + F_Q, 2u);
    STAMP(1);
    const u32x4 q = *(const LAS u32x4*)(L + L_Q + 16 * c);
    float sc0 = 0.f, sc1 = 0.f, sc2 = 0.f, sc3 = 0.f;
#define MERGE(LO, HI, CTRL, BIT) tr_merge<CTRL>((LO), (HI), (c & (BIT)) != 0)
#define QK_GROUP(NEXT)                                  \
  {                                                     \
    float d1 = 0.f, d2 = 0.f, d4 = 0.f, d8 = 0.f;       \
    _Pragma("unroll") for (int t = 0; t < 16; ++t) {    \
      float a = dot2(ring[t][0], q[0], 0.f);            \
      a = dot2(ring[t][1], q[1], a);                    \
      a = dot2(ring[t][2], q[2], a);                    \
      a = dot2(ring[t][3], q[3], a);                    \
      FRESH(a);                                         \
      SB();                                             \
      if (t >= (NEXT)) {                                \
        PACE_CHECK(t)                                   \
        VMWAIT(DEPTH - 1);                              \
        ring[t] = TILE_LD(rn, so, t);                   \
        PACE_NEXT(t)                                    \
      }                                                 \
      if (!(t & 1)) d1 = a;                             \
      else {                                            \
        a = MERGE(d1, a, XOR8, 8);                      \
        if (!(t & 2)) d2 = a;                           \
        else {                                          \
          a = MERGE(d2, a, XOR7, 4);                    \
          if (!(t & 4)) d4 = a;                         \
          else {                                        \
            a = MERGE(d4, a, XOR2, 2);                  \
            if (!(t & 8)) d8 = a;                       \
            else {                                      \
              a = MERGE(d8, a, XOR1, 1);                \
              sc0 = sc1; sc1 = sc2; sc2 = sc3;          \
              sc3 = rbf(rbf(a) * QSCALE) * LOG2E;       \
            }                                           \
          }                                             \
        }                                               \
      }                                                 \
      SB();                                             \
    }                                                   \
  }
    if (EXT) {
      const __amdgpu_buffer_rsrc_t rn = RSRC(kb, SK / 2 * DH * 2);
      const int so = 131072;
      FRESH(voff);
      QK_GROUP(EXT)
#pragma unroll
      for (int t = 0; t < EXT; ++t) ring[t] = ext[t];
    }
#pragma unroll 1
    for (int grp = EXT ? 1 : 0; grp < 4; ++grp) {
      const __amdgpu_buffer_rsrc_t rn = RSRC(grp < 3 ? kb : vb, SK / 2 * DH * 2);
      const int so = ((grp + 1) & 3) * 131072;
      FRESH(voff);
      QK_GROUP(0)
    }
#undef QK_GROUP
#undef MERGE
    STAMP(2);
    float mw = fmaxf(fmaxf(sc0, sc1), fmaxf(sc2, sc3));
    mw = xrow_max(row16_max(mw));
    const float p0 = __builtin_amdgcn_exp2f(sc0 - mw), p1 = __builtin_amdgcn_exp2f(sc1 - mw);
    const float p2 = __builtin_amdgcn_exp2f(sc2 - mw), p3 = __builtin_amdgcn_exp2f(sc3 - mw);
    unsigned pk0 = split_hi_lo(p0), pk1 = split_hi_lo(p1), pk2_ = split_hi_lo(p2), pk3 = split_hi_lo(p3);
    const float lw = xrow_sum(row16_sum((p0 + p1) + (p2 + p3)));
    if (lane == 0) {
      *(LAS float*)(L + L_ML + w * 4) = mw;
      *(LAS float*)(L + L_ML + 32 + w * 4) = lw;
      LGKM0();
      __hip_atomic_fetch_add((LAS unsigned*)(L + F_ML), 1u, RLX_WG);
    }
    SB();
    f32x4 acc = {0.f, 0.f, 0.f, 0.f};
#define BREV4(T) ((((T) & 1) << 3) | (((T) & 2) << 1) | (((T) & 4) >> 1) | (((T) & 8) >> 3))
#define PV_STEP(T, REFILL)                              \
  {                                                     \
    const unsigned X = dppu<0x150 + BREV4(T)>(pk0);     \
    acc = mfma(make_a(X, sel), ring[T], acc);           \
    if (REFILL) {                                       \
      SB();                                             \
      PACE_CHECK(T)                                     \
      if ((T) >= 16 - TAILFREE) VMWAIT_UNLESS(DEPTH - 1, grp, 2, TAILDEPTH - 1); \
      else VMWAIT(DEPTH - 1);                           \
      ring[T] = TILE_LD(rv, so, T);                     \
      PACE_NEXT(T)                                      \
    }                                                   \
    SB();                                               \
  }
#define PV_GROUP(R)                                                                                         \
  PV_STEP(0, R) PV_STEP(1, R) PV_STEP(2, R) PV_STEP(3, R) PV_STEP(4, R) PV_STEP(5, R) PV_STEP(6, R) PV_STEP(7, R) \
  PV_STEP(8, R) PV_STEP(9, R) PV_STEP(10, R) PV_STEP(11, R) PV_STEP(12, R) PV_STEP(13, R) PV_STEP(14, R) PV_STEP(15, R)
    const __amdgpu_buffer_rsrc_t rv = RSRC(vb, SK / 2 * DH * 2);
#pragma unroll 1
    for (int grp = 0; grp < 3; ++grp) {
      const int so = (grp + 1) * 131072;
      if (grp == 2) pstep = 0;
      FRESH(voff);
      PV_GROUP(true)
      pk0 = pk1; pk1 = pk2_; pk2_ = pk3;
    }
    {
      const int so = 0, grp = 3;
      PV_GROUP(false)
    }
#undef PV_GROUP
#undef PV_STEP
    STAMP(3);
    *(LAS f32x4*)(L + L_ACC + ((w * 2 + (g >> 1)) * ACC_LD + 8 * c + 4 * (g & 1)) * 4) = acc;
    WG_BARRIER();
    {
      const int j = h * 2 + s;
      if (j >= 40 || !ABL_FINAL) { STAMP_OUT(); return; }
      const unsigned xw = ((const GAS unsigned*)x)[(b * DM + 16 * j) / 2 + (lane & 7)];
      const unsigned tag = lds_ld(L + F_TAG);
      WG_BARRIER();
      STAMP(4);
      const __amdgpu_buffer_rsrc_t ry = RSRC(ws + G_Y + (size_t)(b * 64 + 8 * w) * DM + 16 * j, 8 * DM * 8);
      u32x4 yv = {0u, 0u, 0u, 0u};
      bool miss = true;
      for (unsigned spins = 0; spins < SPIN_CAP; ++spins) {
        if (miss) {
          yv = __builtin_amdgcn_raw_buffer_load_b128(ry, (lane >> 3) * (DM * 8) + (lane & 7) * 16, 0, AUX_SC1);
          miss = yv[1] != tag || yv[3] != tag;
        }
        const int nmiss = __builtin_popcountll(__builtin_amdgcn_ballot_w64(miss));
        if (nmiss == 0) break;
        if (nmiss > YPOLL_N) __builtin_amdgcn_s_sleep(YPOLL_S); else __builtin_amdgcn_s_sleep(YPOLL_S0);
      }
      float s0 = __uint_as_float(yv[0]), s1 = __uint_as_float(yv[2]);
      s0 += dppf<XOR8>(s0); s1 += dppf<XOR8>(s1);
      s0 = xrow_sum(s0); s1 = xrow_sum(s1);
      if (lane < 8) *(LAS f32x2*)(L + L_ACC + (w * 8 + lane) * 8) = f32x2{s0, s1};
      LGKM0();
      unsigned old = 0;
      if (lane == 0) old = __hip_atomic_fetch_add((LAS unsigned*)(L + F_TAGOK), 1u, RLX_WG);
      asm volatile("" ::: "memory");
      if (__builtin_amdgcn_readfirstlane(old) == 7u) {
        const f32x2 t = *(const LAS f32x2*)(L + L_ACC + lane * 8);
        s0 = t[0]; s1 = t[1];
        s0 += dppf<XOR8>(s0); s1 += dppf<XOR8>(s1);
        s0 = xrow_sum(s0); s1 = xrow_sum(s1);
        if (lane < 8) out[(b * DM + 16 * j) / 2 + lane] = pk2(bflo(xw) + s0, bfhi(xw) + s1);
      }
      STAMP(5);
      STAMP_OUT();
      return;
    }
  }

  const int hw = wave;
  if (ABL_SIDE == 1) { STAMP_OUT(); return; }
  __builtin_amdgcn_s_setprio(3);
  u32x4 wr[20];
  unsigned xp[10];
#define PROJ_MFMA(PH)                                                                                              \
  {                                                                                                                \
    f32x4 ga = {0.f, 0.f, 0.f, 0.f};                                                                               \
    _Pragma("unroll") for (int i = 0; i < 10; ++i) ga = mfma(make_a(xp[i], sel), wr[i], ga);                       \
    if ((g >> 1) == (c >> 3))                                                                                      \
      *(LAS f32x4*)(L + L_P + ((PH) * 1024 + (hw * 2 + (g >> 1)) * 64 + 8 * (c & 7) + 4 * (g & 1)) * 4) = ga;      \
    LGKM0();                                                                                                       \
    if (lane == 0) __hip_atomic_fetch_add((LAS unsigned*)(L + ((PH) ? F_P1 : F_P0)), 1u, RLX_WG);                   \
  }
#define PROJ_SUM(PH, R0, R1)                                                                                       \
  {                                                                                                                \
    lds_wait_ge(L + ((PH) ? F_P1 : F_P0), 8u);                                                                     \
    f32x4 t4 = {0.f, 0.f, 0.f, 0.f};                                                                               \
    _Pragma("unroll") for (int k = 0; k < 4; ++k) t4 += *(const LAS f32x4*)(L + L_P + ((PH) * 1024 + (4 * g + k) * 64 + 4 * c) * 4); \
    _Pragma("unroll") for (int k = 0; k < 4; ++k) t4[k] = xrow_sum(t4[k]);                                         \
    R0 = pk2(t4[0], t4[1]);                                                                                        \
    R1 = pk2(t4[2], t4[3]);                                                                                        \
  }
  unsigned tag = 0;
  {
    unsigned xr[5];
#pragma unroll
    for (int k = 0; k < 5; ++k) xr[k] = LDW((const GAS unsigned*)(x + b * DM) + lane + 64 * k);
    unsigned e = 0;
    if (hw < 2) e = LDW((const GAS unsigned*)(ws + G_E));
    const __amdgpu_buffer_rsrc_t rq = RSRC(wq + (size_t)80 * hw * (NH * DH) + h * DH + 64 * s, 80 * NH * DH * 2 - (h * DH + 64 * s) * 2);
    const int vq = ((ROWG * g + (c >> 3)) * NH * DH + 8 * (c & 7)) * 2;
#pragma unroll
    for (int i = 0; i < WQ_DEPTH; ++i) {
      if (LATE_B0 && i == LATE_H0) WG_BARRIER();
      wr[i] = __builtin_amdgcn_raw_buffer_load_b128(rq, vq, i * ROWI * NH * DH * 2, WQ_AUX);
      SB();
    }
    ln_row(xr, L + L_XQ, lane);
    SB();
#pragma unroll
    for (int i = WQ_DEPTH; i < 10; ++i) {
      VMWAIT(WQ_DEPTH - 1);
      wr[i] = __builtin_amdgcn_raw_buffer_load_b128(rq, vq, i * ROWI * NH * DH * 2, WQ_AUX);
      SB();
    }
    LGKM0();
    if (hw != 1 && hw != 6) STAMP(5);
#pragma unroll
    for (int k = 0; k < 10; ++k) xp[k] = *(const LAS unsigned*)(L + L_XQ + (80 * hw + ROWG * g + ROWI * k) * 2);
    PROJ_MFMA(0)
    STAMP(1);
    if (hw < 2) tag = __builtin_amdgcn_readfirstlane(e) + 1u;
    if (hw == 0) {
      unsigned r0, r1;
      PROJ_SUM(0, r0, r1)
      if (g == 0) {
        __builtin_amdgcn_raw_buffer_store_b128(u32x4{r0, tag, r1, tag}, RSRC(xme, 512), c * 16, 0, AUX_SC1);
        *(LAS u32x2*)(L + L_Q + 128 * s + 8 * c) = u32x2{r0, r1};
      }
      if (lane == 0) *(LAS unsigned*)(L + F_TAG) = tag;
      LGKM0();
      if (lane == 0) {
        __hip_atomic_fetch_add((LAS unsigned*)(L + F_Q), 1u, RLX_WG);
      }
      STAMP(6);
    } else if (hw == 1) {
      lds_wait_ge(L + F_P0, 8u);
      float v[1];
      sweep<1, 1>(xot + (lane & 31), 1, tag, v);
      if (lane < 32) *(LAS float*)(L + L_Q + 128 * (s ^ 1) + 4 * lane) = v[0];
      LGKM0();
      if (lane == 0) __hip_atomic_fetch_add((LAS unsigned*)(L + F_Q), 1u, RLX_WG);
      STAMP(6);
    }
  }
  __builtin_amdgcn_s_setprio(0);
  lds_wait_ge(L + F_Q, 2u);
  tag = __builtin_amdgcn_readfirstlane(lds_ld(L + F_TAG));
  if (ABL_SIDE == 2) { STAMP_OUT(); return; }
  const unsigned tb = (unsigned)__builtin_amdgcn_s_memrealtime();
  const unsigned turn = (mem + 2 * (jx >> 3)) & 7;
#define WAIT_UNTIL(t) while ((int)((unsigned)__builtin_amdgcn_s_memrealtime() - tb - (t)) < 0) __builtin_amdgcn_s_sleep(4)
#define PAUSE_ADD(v) do { if (lane == 0) __hip_atomic_fetch_add((LAS unsigned*)(L + F_PAUSE), (v), RLX_WG); SB(); } while (0)
  f32x2 ysum = {0.f, 0.f};
  {
#pragma unroll
    for (int i = 0; i < 10; ++i) FRESH(xp[i]);
    if (hw == ((mem + 2) & 7)) {
      const __amdgpu_buffer_rsrc_t rp = RSRC(wo + (size_t)(h * DH + 16 * mem) * DM, 16 * DM * 2);
#pragma unroll
      for (int i = 0; i < 20; ++i) {
        VMWAIT(FT_DEPTH - 1);
        wr[i] = __builtin_amdgcn_raw_buffer_load_b128(rp, lane * 16, i * 1024, W_AUX);
        SB();
      }
#pragma unroll
      for (int i = 0; i < 20; ++i) asm volatile("" ::"v"(wr[i]));
    }
    const bool burst = (hw >> 1) != b;
    if (burst) {
      WAIT_UNTIL(STAG1_0 + turn * STAG1);
      PAUSE_ADD(1u);
    }
    const __amdgpu_buffer_rsrc_t rg = RSRC((s ? wu : wg) + (size_t)80 * hw * FF + 64 * hs, 80 * FF * 2 - 64 * hs * 2);
    const int vg = ((ROWG * g + (c >> 3)) * FF + 8 * (c & 7)) * 2;
#pragma unroll
    for (int i = 0; i < 10; ++i) {
      VMWAIT(GU_DEPTH - 1);
      wr[i] = __builtin_amdgcn_raw_buffer_load_b128(rg, vg, i * ROWI * FF * 2, W_AUX);
      SB();
    }
    const __amdgpu_buffer_rsrc_t rd = RSRC(wd + (size_t)(64 * hs + 32 * s + 4 * hw) * DM, 4 * DM * 2);
#pragma unroll
    for (int cb = 0; cb < 5; ++cb) {
      VMWAIT(GU_DEPTH - 1);
      wr[10 + cb] = __builtin_amdgcn_raw_buffer_load_b128(rd, (g * DM + 8 * c) * 2, cb * 256, W_AUX);
      SB();
    }
    if (burst) PAUSE_ADD(~0u);
    PROJ_MFMA(1)
    if (hw == 2) {
      unsigned r0, r1;
      PROJ_SUM(1, r0, r1)
      if (g == 0 && (c >> 3) != s) __builtin_amdgcn_raw_buffer_store_b128(u32x4{r0, tag, r1, tag}, RSRC(xme + 32, 128), (c & 7) * 16, 0, AUX_SC1);
      float v[2];
      sweep<2, 16>(xot + 32 + 2 * (c & 7), 1, tag, v);
      const unsigned o0 = __float_as_uint(v[0]), o1 = __float_as_uint(v[1]);
      if (g == 0 && (c >> 3) == s)
        *(LAS u32x2*)(L + L_H + 8 * (c & 7)) = u32x2{pk2(bflo(r0) * bflo(o0), bfhi(r0) * bfhi(o0)), pk2(bflo(r1) * bflo(o1), bfhi(r1) * bfhi(o1))};
      LGKM0();
      if (lane == 0) __hip_atomic_store((LAS unsigned*)(L + F_H), 1u, RLX_WG);
    }
    lds_wait_ge(L + F_H, 1u);
    const unsigned X = *(const LAS unsigned short*)(L + L_H + 2 * (4 * hw + g));
    const u32x4 a = make_a(X, sel);
#pragma unroll
    for (int cb = 0; cb < 5; ++cb) {
      const f32x4 yf = mfma(a, wr[10 + cb], f32x4{0.f, 0.f, 0.f, 0.f});
      if (g < 2) *(LAS f32x4*)(L + L_BIG + (hw * DM + 128 * cb + 8 * c + 4 * g) * 4) = yf;
    }
    LGKM0();
    if (lane == 0) __hip_atomic_fetch_add((LAS unsigned*)(L + F_FF), 1u, RLX_WG);
    if (hw < 5 && h < NSL) {
      lds_wait_ge(L + F_FF, 8u);
#pragma unroll
      for (int p = 0; p < 8; ++p) ysum += *(const LAS f32x2*)(L + L_BIG + (p * DM + 2 * tid) * 4);
    }
  }
  STAMP(2);
  {
    WAIT_UNTIL(STAG2_0 + turn * STAG2);
    PAUSE_ADD(1u);
    const __amdgpu_buffer_rsrc_t ro = RSRC(wo + (size_t)(h * DH + 16 * hw) * DM, 16 * DM * 2);
    const int vo = (4 * g * DM + 8 * c) * 2;
#pragma unroll
    for (int j = 0; j < 4; ++j)
#pragma unroll
      for (int cb = 0; cb < 5; ++cb) {
        VMWAIT(WO_DEPTH - 1);
        wr[j * 5 + cb] = __builtin_amdgcn_raw_buffer_load_b128(ro, vo, (j * DM + cb * 128) * 2, W_AUX);
        SB();
      }
    PAUSE_ADD(~0u);
  }
  STAMP(7);
  if (hw == 6) {
    lds_wait_ge(L + F_ML, 8u);
    const float mv = *(const LAS float*)(L + L_ML + 4 * (lane & 7)), lv = *(const LAS float*)(L + L_ML + 32 + 4 * (lane & 7));
    float M = fmaxf(mv, dppf<XOR1>(mv));
    M = fmaxf(M, dppf<XOR2>(M));
    M = fmaxf(M, dppf<XOR7>(M));
    float Ls = __builtin_amdgcn_exp2f(mv - M) * lv;
    Ls += dppf<XOR1>(Ls);
    Ls += dppf<XOR2>(Ls);
    Ls += dppf<XOR7>(Ls);
    if (lane < 2) put(xme + 48 + lane, lane == 0 ? M : Ls, tag);
    float o2[2];
    sweep<2, 16>(xot + 48, 1, tag, o2);
    const float Mt = fmaxf(M, o2[0]);
    const float e0 = __builtin_amdgcn_exp2f(M - Mt) * Ls, e1 = __builtin_amdgcn_exp2f(o2[0] - Mt) * o2[1];
    if (lane < 8) *(LAS float*)(L + L_EW + 4 * lane) = __builtin_amdgcn_exp2f(mv - Mt) / (e0 + e1);
    STAMP(5);
  } else if (hw == 7 && bid == 0) {
    lds_wait_ge(L + F_ML, 8u);
    float f4[4];
    sweep<4, 64>(ws + G_X + lane * 64 + 48, 64 * 64, tag, f4);
    if (lane == 0) __hip_atomic_store((GAS unsigned*)(ws + G_E), tag, RLX_AGENT);
  }
  int a_acc = L_ACC + (c * ACC_LD + 16 * hw + 4 * g) * 4, a_ew = L_EW + 4 * (c >> 1);
  int a_wr = L_BIG + (hw * DM + 64 * (g & 1) + 4 * c) * 4;
  int a_rd = L_BIG + ((tid >> 6) * 128 + ((tid >> 1) & 1) * 64 + ((tid >> 2) & 15) * 4 + (tid & 1) * 2) * 4;
  int v_st = tid * 16;
  const __amdgpu_buffer_rsrc_t rpub = RSRC(ws + G_Y + (size_t)(b * 64 + h * 2 + s) * DM, DM * 8);
  FRESH(a_acc); FRESH(a_ew); FRESH(a_wr); FRESH(a_rd); FRESH(v_st);
  asm volatile("" ::"s"(rpub));
  if (hw >= 4) __builtin_amdgcn_s_setprio(1);
  WG_BARRIER();
  STAMP(3);
  {
    f32x4 o = *(const LAS f32x4*)(L + a_acc) * *(const LAS float*)(L + a_ew);
#pragma unroll
    for (int j = 0; j < 4; ++j) o[j] += dppf<XOR1>(o[j]);
#pragma unroll
    for (int j = 0; j < 4; ++j) o[j] += dppf<XOR2>(o[j]);
#pragma unroll
    for (int j = 0; j < 4; ++j) o[j] += dppf<XOR7>(o[j]);
#pragma unroll
    for (int j = 0; j < 4; ++j) o[j] += dppf<XOR8>(o[j]);
    const unsigned x01 = pk2(o[0], o[1]), x23 = pk2(o[2], o[3]);
    u32x4 a[4] = {make_a(x01, sel), make_a(x01 >> 16, sel), make_a(x23, sel), make_a(x23 >> 16, sel)};
#pragma unroll
    for (int j = 0; j < 4; ++j) FRESH(a[j]);
    __builtin_amdgcn_s_setprio(0);
    SB();
    f32x4 y[5];
#pragma unroll
    for (int cb = 0; cb < 5; ++cb) y[cb] = f32x4{0.f, 0.f, 0.f, 0.f};
#pragma unroll
    for (int j = 0; j < 4; ++j)
#pragma unroll
      for (int cb = 0; cb < 5; ++cb) {
        y[cb] = mfma(a[j], wr[j * 5 + cb], y[cb]);
        SB();
      }
    if (g < 2) {
#pragma unroll
      for (int cb = 0; cb < 5; ++cb) *(LAS f32x4*)(L + a_wr + 512 * cb) = y[cb];
    }
  }
  WG_BARRIER();
  if (hw < 5 && ABL_FINAL) {
    f32x2 t8[8];
#pragma unroll
    for (int p = 0; p < 8; ++p) t8[p] = *(const LAS f32x2*)(L + a_rd + p * DM * 4);
    const f32x2 t = (((t8[0] + t8[1]) + (t8[2] + t8[3])) + ((t8[4] + t8[5]) + (t8[6] + t8[7]))) + ysum;
    __builtin_amdgcn_raw_buffer_store_b128(u32x4{__float_as_uint(t[0]), tag, __float_as_uint(t[1]), tag}, rpub, v_st, 0, AUX_SC1);
  }
  STAMP(4);
  STAMP_OUT();
}

extern "C" void kernel_launch(void* const* d_in, const int* in_sizes, int n_in, void* d_out, int out_size, void* d_ws,
                              size_t ws_size, hipStream_t stream) {
  (void)in_sizes; (void)n_in; (void)out_size; (void)ws_size;
  typedef const GAS unsigned short* cp;
  k_main<<<NWG, 1024, 0, stream>>>((cp)d_in[0], (cp)d_in[1], (cp)d_in[2], (cp)d_in[3], (cp)d_in[4], (cp)d_in[6], (cp)d_in[7],
                                   (cp)d_in[8], (GAS u64*)d_ws, (GAS unsigned*)d_out);
}
```

```cpp
#include <hip/hip_runtime.h>
#include <hip/hip_bf16.h>
#include <stdint.h>

#define DEPTH 4
#define GU_DEPTH 6
#define WO_DEPTH 6
#define WQ_AUX 0
#define W_AUX 0
#define X_NT 0
#define EQUALIZE 1
#define START_SLEEP 0
#define DEPTH0 4
#define DEPTH0_N 0
#define PACE 98
#define PACE0 450
#define YMODE 1
#define YPOLL_N 8
#define YPOLL_S 8
#define YPOLL_S0 1
#define EXT 0
#define ABL_SIDE 0
#define ROWMAP 0
#define ROWG 20
#define ROWI 2
#define WQ_DEPTH 10
#define TAILFREE 8
#define TAILDEPTH 16
#define O_LO 0
#define LATE_B0 0
#define LATE_H0 2
#define STAG1_0 950
#define STAG1 125
#define STAG2_0 1950
#define STAG2 205
#define BURST_PRIO 3
#define PAUSE_EVERY 0
#define FT_DEPTH 6
#define PACE_N 4
#define PSHIFT 0
#define AHEAD 0u
#define FIXEND 0
#define PACE_A 128
#define JUMP1 140
#define JUMP2 240
#define YDELAY 20
#define YPRE 1
#define LAG1 70
#define DEPTH_LO 3
#define DEPTH_HI 5
#define HROT 0
#define POLL_N1 2
#define POLL_S1 8
#define POLL_N2 8
#define POLL_S2 32
#define ABL_FINAL 1
constexpr int AUX_NT = 2, AUX_SC1 = 16;

typedef unsigned u32x4 __attribute__((ext_vector_type(4)));
typedef unsigned u32x2 __attribute__((ext_vector_type(2)));
typedef float f32x4 __attribute__((ext_vector_type(4)));
typedef float f32x2 __attribute__((ext_vector_type(2)));
typedef short bf16x8 __attribute__((ext_vector_type(8)));
typedef __bf16 bf16x2_t __attribute__((ext_vector_type(2)));
typedef unsigned long long u64;
#define GAS __attribute__((address_space(1)))
#define LAS __attribute__((address_space(3)))
#define RLX_AGENT __ATOMIC_RELAXED, __HIP_MEMORY_SCOPE_AGENT
#define RLX_WG __ATOMIC_RELAXED, __HIP_MEMORY_SCOPE_WORKGROUP

constexpr int NB = 4, DM = 640, NH = 32, SK = 4096, DH = 128, FF = 1728;
constexpr int NSL = FF / 64;
constexpr int NWG = 256;
constexpr float LOG2E = 1.4426950408889634f;
constexpr float QSCALE = 0.08838834764831845f;
constexpr unsigned SPIN_CAP = 1u << 18;

constexpr size_t G_E = 0;
constexpr size_t G_X = 32;
constexpr size_t G_Y = G_X + (size_t)NWG * 64;

constexpr int L_XQ = 0;
constexpr int L_Q = L_XQ + DM * 2;
constexpr int L_P = L_Q + 256;
constexpr int ACC_LD = 132;
constexpr int L_ACC = L_P + 2 * 16 * 64 * 4;
constexpr int L_ML = L_ACC + 16 * ACC_LD * 4;
constexpr int L_EW = L_ML + 64;
constexpr int L_FLAG = L_EW + 32;
constexpr int F_Q = L_FLAG, F_ML = L_FLAG + 4, F_H = L_FLAG + 8, F_P0 = L_FLAG + 12, F_P1 = L_FLAG + 16, F_FF = L_FLAG + 20,
              F_TAGOK = L_FLAG + 24, F_TAG = L_FLAG + 28, F_PAUSE = L_FLAG + 32;
constexpr int L_H = L_FLAG + 64;
constexpr int L_BIG = L_H + 64;
constexpr int L_END = L_BIG + 8 * DM * 4;

__device__ __forceinline__ float bflo(unsigned u) { return __builtin_bit_cast(float, u << 16); }
__device__ __forceinline__ float bfhi(unsigned u) { return __builtin_bit_cast(float, u & 0xffff0000u); }
__device__ __forceinline__ unsigned pk2(float lo, float hi) {
  bf16x2_t r;
  r[0] = (__bf16)lo;
  r[1] = (__bf16)hi;
  return __builtin_bit_cast(unsigned, r);
}
__device__ __forceinline__ float rbf(float x) { return (float)(__bf16)x; }
__device__ __forceinline__ float dot2(unsigned a, unsigned b, float c) {
  return __builtin_amdgcn_fdot2_f32_bf16(__builtin_bit_cast(bf16x2_t, a), __builtin_bit_cast(bf16x2_t, b), c, false);
}
template <int CTRL> __device__ __forceinline__ float dppf(float x) {
  return __builtin_bit_cast(float, __builtin_amdgcn_mov_dpp(__builtin_bit_cast(int, x), CTRL, 0xf, 0xf, true));
}
template <int CTRL> __device__ __forceinline__ unsigned dppu(unsigned x) {
  return (unsigned)__builtin_amdgcn_mov_dpp((int)x, CTRL, 0xf, 0xf, true);
}
constexpr int XOR1 = 0xB1, XOR2 = 0x4E, XOR7 = 0x141, XOR8 = 0x128;
template <int CTRL> __device__ __forceinline__ float tr_merge(float lo, float hi, bool up) {
  const float t1 = lo + dppf<CTRL>(lo);
  const float t2 = hi + dppf<CTRL>(hi);
  return up ? t2 : t1;
}
__device__ __forceinline__ float row16_sum(float x) {
  x += dppf<XOR1>(x); x += dppf<XOR2>(x); x += dppf<XOR7>(x); x += dppf<XOR8>(x);
  return x;
}
__device__ __forceinline__ float row16_max(float x) {
  x = fmaxf(x, dppf<XOR1>(x)); x = fmaxf(x, dppf<XOR2>(x)); x = fmaxf(x, dppf<XOR7>(x)); x = fmaxf(x, dppf<XOR8>(x));
  return x;
}
__device__ __forceinline__ float half_sum(float x) {
  auto t = __builtin_amdgcn_permlane32_swap(__float_as_uint(x), __float_as_uint(x), false, false);
  return __uint_as_float(t[0]) + __uint_as_float(t[1]);
}
__device__ __forceinline__ float xrow_max(float x) {
  auto s = __builtin_amdgcn_permlane16_swap(__float_as_uint(x), __float_as_uint(x), false, false);
  x = fmaxf(__uint_as_float(s[0]), __uint_as_float(s[1]));
  auto t = __builtin_amdgcn_permlane32_swap(__float_as_uint(x), __float_as_uint(x), false, false);
  return fmaxf(__uint_as_float(t[0]), __uint_as_float(t[1]));
}
__device__ __forceinline__ float xrow_sum(float x) {
  auto s = __builtin_amdgcn_permlane16_swap(__float_as_uint(x), __float_as_uint(x), false, false);
  x = __uint_as_float(s[0]) + __uint_as_float(s[1]);
  return half_sum(x);
}
__device__ __forceinline__ f32x4 mfma(u32x4 a, u32x4 b, f32x4 c) {
  return __builtin_amdgcn_mfma_f32_16x16x32_bf16(__builtin_bit_cast(bf16x8, a), __builtin_bit_cast(bf16x8, b), c, 0, 0, 0);
}
#define LGKM0() asm volatile("s_waitcnt lgkmcnt(0)" ::: "memory")
#define WG_BARRIER()                \
  do {                              \
    LGKM0();                        \
    __builtin_amdgcn_s_barrier();   \
    asm volatile("" ::: "memory");  \
  } while (0)
#define SB() __builtin_amdgcn_sched_barrier(0)
#define STAMP(k)
#define STAMP_DECL
#define STAMP_OUT()
#define LDW(p) (*(p))
#define RSRC(p, n) __builtin_amdgcn_make_buffer_rsrc((void*)(p), 0, (n), 0x00020000)
#define FRESH(v) asm volatile("" : "+v"(v))
#define VMWAIT_UNLESS(n, v, k, m) \
  asm volatile("s_waitcnt vmcnt(%3)\n\ts_cmp_ge_u32 %1, %2\n\ts_cbranch_scc1 1f\n\ts_waitcnt vmcnt(%0)\n1:" ::"i"(n), "s"(v), "i"(k), "i"(m) : "memory", "scc")
#define VMWAIT(n) asm volatile("s_waitcnt vmcnt(%0)" ::"i"(n) : "memory")

__device__ __forceinline__ void put(GAS u64* p, float v, unsigned tag) {
  __hip_atomic_store(p, ((u64)tag << 32) | (u64)__float_as_uint(v), RLX_AGENT);
}
__device__ __forceinline__ u64 get(const GAS u64* p) { return __hip_atomic_load(p, RLX_AGENT); }
__device__ __forceinline__ unsigned lds_ld(LAS unsigned char* p) { return __hip_atomic_load((LAS unsigned*)p, RLX_WG); }
__device__ __forceinline__ void lds_wait_ge(LAS unsigned char* p, unsigned want) {
  while (lds_ld(p) < want) __builtin_amdgcn_s_sleep(1);
  asm volatile("" ::: "memory");
}
template <int N, int SLP> __device__ __forceinline__ void sweep(const GAS u64* p, int S, unsigned tag, float (&v)[N]) {
  for (unsigned spins = 0;; ++spins) {
    bool ok = true;
#pragma unroll
    for (int k = 0; k < N; ++k) {
      const u64 x = get(p + k * S);
      v[k] = __uint_as_float((unsigned)x);
      ok &= (unsigned)(x >> 32) == tag;
    }
    if (__all(ok) || spins > SPIN_CAP) return;
    __builtin_amdgcn_s_sleep(SLP);
  }
}

struct Sel { unsigned s[4]; };
__device__ __forceinline__ Sel make_sel(int lane) {
  const int m = lane & 15;
  const unsigned src = (m & 8) ? 0x0706u : 0x0504u;
  const unsigned placed = (m & 1) ? ((src << 16) | 0x0c0cu) : (0x0c0c0000u | src);
  Sel r;
#pragma unroll
  for (int jj = 0; jj < 4; ++jj) r.s[jj] = (((m & 7) >> 1) == jj) ? placed : 0x0c0c0c0cu;
  return r;
}
__device__ __forceinline__ u32x4 make_a(unsigned X, const Sel& s) {
  u32x4 a;
#pragma unroll
  for (int jj = 0; jj < 4; ++jj) a[jj] = __builtin_amdgcn_perm(X, 0u, s.s[jj]);
  return a;
}
__device__ __forceinline__ unsigned split_hi_lo(float v) {
  const float h = rbf(v);
  return pk2(h, v - h);
}

__device__ __forceinline__ void ln_row(const unsigned (&xr)[5], LAS unsigned char* dst, int lane) {
  float sum = 0.f;
#pragma unroll
  for (int k = 0; k < 5; ++k) sum += bflo(xr[k]) + bfhi(xr[k]);
  const float mean = xrow_sum(row16_sum(sum)) * (1.0f / DM);
  float ss = 0.f;
#pragma unroll
  for (int k = 0; k < 5; ++k) {
    const float a = bflo(xr[k]) - mean, bb = bfhi(xr[k]) - mean;
    ss = fmaf(a, a, ss);
    ss = fmaf(bb, bb, ss);
  }
  const float var = xrow_sum(row16_sum(ss)) * (1.0f / DM);
  const float mu = rbf(mean);
  const float rinv = 1.0f / rbf(sqrtf(rbf(rbf(var) + 1e-5f)));
#pragma unroll
  for (int k = 0; k < 5; ++k)
    *(LAS unsigned*)(dst + 4 * (lane + 64 * k)) = pk2(rbf(bflo(xr[k]) - mu) * rinv, rbf(bfhi(xr[k]) - mu) * rinv);
}

__global__ void __launch_bounds__(1024)
k_main(const GAS unsigned short* __restrict__ x, const GAS unsigned short* __restrict__ kc, const GAS unsigned short* __restrict__ vc,
       const GAS unsigned short* __restrict__ wq, const GAS unsigned short* __restrict__ wo, const GAS unsigned short* __restrict__ wg,
       const GAS unsigned short* __restrict__ wu, const GAS unsigned short* __restrict__ wd, GAS u64* ws, GAS unsigned* out) {
  __shared__ __attribute__((aligned(16))) unsigned char smem[L_END];
  LAS unsigned char* L = (LAS unsigned char*)smem;
  const int tid = threadIdx.x, lane = tid & 63, wave = __builtin_amdgcn_readfirstlane(tid >> 6);
  const int g = lane >> 4, c = lane & 15;
  const int bid = blockIdx.x;
  const int jx = bid >> 3;
  const int h = (jx >> 3) * 8 + ((bid + HROT * (jx >> 3)) & 7), mem = jx & 7, b = mem >> 1, s = mem & 1;
  const int slot = (b * NH + h) * 2 + s;
  const int hs = h < NSL ? h : 16 + ((bid + HROT * 2) & 7);
  STAMP_DECL;
  STAMP(0);
  if (tid < 16) *(LAS unsigned*)(L + L_FLAG + 4 * tid) = 0u;
  asm volatile("" ::"s"(x), "s"(kc), "s"(vc), "s"(wq), "s"(wo), "s"(wg), "s"(wu), "s"(wd), "s"(ws), "s"(out));
  if (!LATE_B0) WG_BARRIER();
  const unsigned tb = (unsigned)__builtin_amdgcn_s_memrealtime();
  const unsigned turn = (mem + 2 * (jx >> 3)) & 7;
  const Sel sel = make_sel(lane);

  GAS u64* const xme = ws + G_X + (size_t)slot * 64;
  const GAS u64* const xot = ws + G_X + (size_t)(slot ^ 1) * 64;

  if (wave >= 8) {
    const int w = wave - 8;
    const size_t base = ((size_t)(b * NH + h) * SK + (size_t)s * (SK / 2)) * DH;
    const GAS unsigned short* kb = kc + base;
    const GAS unsigned short* vb = vc + base;
    int voff = lane * 16 + w * 4096;
#define TILE_LD(r, so, t) __builtin_amdgcn_raw_buffer_load_b128((r), voff + ((t) & 3) * 1024, (so) + ((t) >> 2) * 32768, AUX_NT)
    u32x4 ring[16];
    unsigned pz = 0, pstep = PACE_A, deep = 0;
    unsigned tgt = PACE0 + w * (PACE / 8), j1 = STAG1_0 + turn * STAG1, j2 = STAG2_0 + turn * STAG2;
#define CLOCK_CHECK(t)                                                                                   \
  if (((t) % PACE_N) == 0) {                                                                             \
    tgt += ((t) >= 16 - TAILFREE + 1) ? ptail : pstep;                                                   \
    if (tgt >= j1) { tgt += JUMP1; j1 = ~0u; }                                                           \
    if (tgt >= j2) { tgt += JUMP2; j2 = ~0u; }                                                           \
    unsigned now;                                                                                        \
    while ((now = (unsigned)__builtin_amdgcn_s_memrealtime() - tb) < tgt) __builtin_amdgcn_s_sleep(1);   \
    deep = now - tgt;                                                                                    \
  }
#define PACE_CHECK(t) CLOCK_CHECK(t)
#define PACE_NEXT(t)
    u32x4 ext[EXT + 1];
    {
      const __amdgpu_buffer_rsrc_t rk = RSRC(kb, SK / 2 * DH * 2);
#pragma unroll
      for (int t = 0; t < 16; ++t) {
        if (LATE_B0 && t == LATE_B0) WG_BARRIER();
        if (t >= DEPTH) VMWAIT(DEPTH - 1);
        ring[t] = TILE_LD(rk, 0, t);
        SB();
      }
#pragma unroll
      for (int t = 0; t < EXT; ++t) {
        VMWAIT(DEPTH - 1);
        ext[t] = TILE_LD(rk, 131072, t);
        SB();
      }
    }
    STAMP(6);
    if (!ABL_SIDE) lds_wait_ge(L + F_Q, 2u);
    STAMP(1);
    const u32x4 q = *(const LAS u32x4*)(L + L_Q + 16 * c);
    float sc0 = 0.f, sc1 = 0.f, sc2 = 0.f, sc3 = 0.f;
#define MERGE(LO, HI, CTRL, BIT) tr_merge<CTRL>((LO), (HI), (c & (BIT)) != 0)
#define QK_GROUP(NEXT)                                  \
  {                                                     \
    float d1 = 0.f, d2 = 0.f, d4 = 0.f, d8 = 0.f;       \
    _Pragma("unroll") for (int t = 0; t < 16; ++t) {    \
      float a = dot2(ring[t][0], q[0], 0.f);            \
      a = dot2(ring[t][1], q[1], a);                    \
      a = dot2(ring[t][2], q[2], a);                    \
      a = dot2(ring[t][3], q[3], a);                    \
      FRESH(a);                                         \
      SB();                                             \
      if (t >= (NEXT)) {                                \
        PACE_CHECK(t)                                   \
        VMWAIT_UNLESS(DEPTH_LO - 1, deep, LAG1, DEPTH_HI - 1); \
        ring[t] = TILE_LD(rn, so, t);                   \
        PACE_NEXT(t)                                    \
      }                                                 \
      if (!(t & 1)) d1 = a;                             \
      else {                                            \
        a = MERGE(d1, a, XOR8, 8);                      \
        if (!(t & 2)) d2 = a;                           \
        else {                                          \
          a = MERGE(d2, a, XOR7, 4);                    \
          if (!(t & 4)) d4 = a;                         \
          else {                                        \
            a = MERGE(d4, a, XOR2, 2);                  \
            if (!(t & 8)) d8 = a;                       \
            else {                                      \
              a = MERGE(d8, a, XOR1, 1);                \
              sc0 = sc1; sc1 = sc2; sc2 = sc3;          \
              sc3 = rbf(rbf(a) * QSCALE) * LOG2E;       \
            }                                           \
          }                                             \
        }                                               \
      }                                                 \
      SB();                                             \
    }                                                   \
  }
    if (EXT) {
      const __amdgpu_buffer_rsrc_t rn = RSRC(kb, SK / 2 * DH * 2);
      const int so = 131072;
      FRESH(voff);
      const unsigned ptail = pstep;
      QK_GROUP(EXT)
#pragma unroll
      for (int t = 0; t < EXT; ++t) ring[t] = ext[t];
    }
#pragma unroll 1
    for (int grp = EXT ? 1 : 0; grp < 4; ++grp) {
      const __amdgpu_buffer_rsrc_t rn = RSRC(grp < 3 ? kb : vb, SK / 2 * DH * 2);
      const int so = ((grp + 1) & 3) * 131072;
      FRESH(voff);
      const unsigned ptail = pstep;
      QK_GROUP(0)
      pstep = PACE;
    }
#undef QK_GROUP
#undef MERGE
    STAMP(2);
    float mw = fmaxf(fmaxf(sc0, sc1), fmaxf(sc2, sc3));
    mw = xrow_max(row16_max(mw));
    const float p0 = __builtin_amdgcn_exp2f(sc0 - mw), p1 = __builtin_amdgcn_exp2f(sc1 - mw);
    const float p2 = __builtin_amdgcn_exp2f(sc2 - mw), p3 = __builtin_amdgcn_exp2f(sc3 - mw);
    unsigned pk0 = split_hi_lo(p0), pk1 = split_hi_lo(p1), pk2_ = split_hi_lo(p2), pk3 = split_hi_lo(p3);
    const float lw = xrow_sum(row16_sum((p0 + p1) + (p2 + p3)));
    if (lane == 0) {
      *(LAS float*)(L + L_ML + w * 4) = mw;
      *(LAS float*)(L + L_ML + 32 + w * 4) = lw;
      LGKM0();
      __hip_atomic_fetch_add((LAS unsigned*)(L + F_ML), 1u, RLX_WG);
    }
    SB();
    f32x4 acc = {0.f, 0.f, 0.f, 0.f};
#define BREV4(T) ((((T) & 1) << 3) | (((T) & 2) << 1) | (((T) & 4) >> 1) | (((T) & 8) >> 3))
#define PV_STEP(T, REFILL)                              \
  {                                                     \
    const unsigned X = dppu<0x150 + BREV4(T)>(pk0);     \
    acc = mfma(make_a(X, sel), ring[T], acc);           \
    if (REFILL) {                                       \
      SB();                                             \
      PACE_CHECK(T)                                     \
      if ((T) == 16 - TAILFREE) deep |= dtail;          \
      VMWAIT_UNLESS(DEPTH_LO - 1, deep, LAG1, DEPTH_HI - 1); \
      ring[T] = TILE_LD(rv, so, T);                     \
      PACE_NEXT(T)                                      \
    }                                                   \
    SB();                                               \
  }
#define PV_GROUP(R)                                                                                         \
  PV_STEP(0, R) PV_STEP(1, R) PV_STEP(2, R) PV_STEP(3, R) PV_STEP(4, R) PV_STEP(5, R) PV_STEP(6, R) PV_STEP(7, R) \
  PV_STEP(8, R) PV_STEP(9, R) PV_STEP(10, R) PV_STEP(11, R) PV_STEP(12, R) PV_STEP(13, R) PV_STEP(14, R) PV_STEP(15, R)
    const __amdgpu_buffer_rsrc_t rv = RSRC(vb, SK / 2 * DH * 2);
#pragma unroll 1
    for (int grp = 0; grp < 3; ++grp) {
      const int so = (grp + 1) * 131072;
      const unsigned ptail = grp == 2 ? 0u : pstep, dtail = (unsigned)(grp >> 1) << 20;
      FRESH(voff);
      PV_GROUP(true)
      pk0 = pk1; pk1 = pk2_; pk2_ = pk3;
    }
    {
      const int so = 0, grp = 3;
      const unsigned ptail = 0, dtail = 0;
      PV_GROUP(false)
    }
#undef PV_GROUP
#undef PV_STEP
    STAMP(3);
    *(LAS f32x4*)(L + L_ACC + ((w * 2 + (g >> 1)) * ACC_LD + 8 * c + 4 * (g & 1)) * 4) = acc;
    WG_BARRIER();
    {
      const int j = h * 2 + s;
      if (j >= 40 || !ABL_FINAL) { STAMP_OUT(); return; }
      const unsigned xw = ((const GAS unsigned*)x)[(b * DM + 16 * j) / 2 + (lane & 7)];
      const unsigned tag = lds_ld(L + F_TAG);
      WG_BARRIER();
      STAMP(4);
      const __amdgpu_buffer_rsrc_t ry = RSRC(ws + G_Y + (size_t)(b * 64 + 8 * w) * DM + 16 * j, 8 * DM * 8);
      u32x4 yv = {0u, 0u, 0u, 0u};
      bool miss = true;
      if (YDELAY) __builtin_amdgcn_s_sleep(YDELAY);
      for (unsigned spins = 0; spins < SPIN_CAP; ++spins) {
        if (miss) {
          yv = __builtin_amdgcn_raw_buffer_load_b128(ry, (lane >> 3) * (DM * 8) + (lane & 7) * 16, 0, AUX_SC1);
          miss = yv[1] != tag || yv[3] != tag;
        }
        const int nmiss = __builtin_popcountll(__builtin_amdgcn_ballot_w64(miss));
        if (nmiss == 0) break;
        if (nmiss > YPOLL_N) __builtin_amdgcn_s_sleep(YPOLL_S); else __builtin_amdgcn_s_sleep(YPOLL_S0);
      }
      float s0 = __uint_as_float(yv[0]), s1 = __uint_as_float(yv[2]);
      s0 += dppf<XOR8>(s0); s1 += dppf<XOR8>(s1);
      s0 = xrow_sum(s0); s1 = xrow_sum(s1);
      if (lane < 8) *(LAS f32x2*)(L + L_ACC + (w * 8 + lane) * 8) = f32x2{s0, s1};
      LGKM0();
      unsigned old = 0;
      if (lane == 0) old = __hip_atomic_fetch_add((LAS unsigned*)(L + F_TAGOK), 1u, RLX_WG);
      asm volatile("" ::: "memory");
      if (__builtin_amdgcn_readfirstlane(old) == 7u) {
        const f32x2 t = *(const LAS f32x2*)(L + L_ACC + lane * 8);
        s0 = t[0]; s1 = t[1];
        s0 += dppf<XOR8>(s0); s1 += dppf<XOR8>(s1);
        s0 = xrow_sum(s0); s1 = xrow_sum(s1);
        if (lane < 8) out[(b * DM + 16 * j) / 2 + lane] = pk2(bflo(xw) + s0, bfhi(xw) + s1);
      }
      STAMP(5);
      STAMP_OUT();
      return;
    }
  }

  const int hw = wave;
  if (ABL_SIDE == 1) { STAMP_OUT(); return; }
  __builtin_amdgcn_s_setprio(3);
  u32x4 wr[20];
  unsigned xp[10];
#define PROJ_MFMA(PH)                                                                                              \
  {                                                                                                                \
    f32x4 ga = {0.f, 0.f, 0.f, 0.f};                                                                               \
    _Pragma("unroll") for (int i = 0; i < 10; ++i) ga = mfma(make_a(xp[i], sel), wr[i], ga);                       \
    if ((g >> 1) == (c >> 3))                                                                                      \
      *(LAS f32x4*)(L + L_P + ((PH) * 1024 + (hw * 2 + (g >> 1)) * 64 + 8 * (c & 7) + 4 * (g & 1)) * 4) = ga;      \
    LGKM0();                                                                                                       \
    if (lane == 0) __hip_atomic_fetch_add((LAS unsigned*)(L + ((PH) ? F_P1 : F_P0)), 1u, RLX_WG);                   \
  }
#define PROJ_SUM(PH, R0, R1)                                                                                       \
  {                                                                                                                \
    lds_wait_ge(L + ((PH) ? F_P1 : F_P0), 8u);                                                                     \
    f32x4 t4 = {0.f, 0.f, 0.f, 0.f};                                                                               \
    _Pragma("unroll") for (int k = 0; k < 4; ++k) t4 += *(const LAS f32x4*)(L + L_P + ((PH) * 1024 + (4 * g + k) * 64 + 4 * c) * 4); \
    _Pragma("unroll") for (int k = 0; k < 4; ++k) t4[k] = xrow_sum(t4[k]);                                         \
    R0 = pk2(t4[0], t4[1]);                                                                                        \
    R1 = pk2(t4[2], t4[3]);                                                                                        \
  }
  unsigned tag = 0;
  {
    unsigned xr[5];
#pragma unroll
    for (int k = 0; k < 5; ++k) xr[k] = LDW((const GAS unsigned*)(x + b * DM) + lane + 64 * k);
    unsigned e = 0;
    if (hw < 2) e = LDW((const GAS unsigned*)(ws + G_E));
    const __amdgpu_buffer_rsrc_t rq = RSRC(wq + (size_t)80 * hw * (NH * DH) + h * DH + 64 * s, 80 * NH * DH * 2 - (h * DH + 64 * s) * 2);
    const int vq = ((ROWG * g + (c >> 3)) * NH * DH + 8 * (c & 7)) * 2;
#pragma unroll
    for (int i = 0; i < WQ_DEPTH; ++i) {
      if (LATE_B0 && i == LATE_H0) WG_BARRIER();
      wr[i] = __builtin_amdgcn_raw_buffer_load_b128(rq, vq, i * ROWI * NH * DH * 2, WQ_AUX);
      SB();
    }
    ln_row(xr, L + L_XQ, lane);
    SB();
#pragma unroll
    for (int i = WQ_DEPTH; i < 10; ++i) {
      VMWAIT(WQ_DEPTH - 1);
      wr[i] = __builtin_amdgcn_raw_buffer_load_b128(rq, vq, i * ROWI * NH * DH * 2, WQ_AUX);
      SB();
    }
    LGKM0();
    if (hw != 1 && hw != 6) STAMP(5);
#pragma unroll
    for (int k = 0; k < 10; ++k) xp[k] = *(const LAS unsigned*)(L + L_XQ + (80 * hw + ROWG * g + ROWI * k) * 2);
    PROJ_MFMA(0)
    STAMP(1);
    if (hw < 2) tag = __builtin_amdgcn_readfirstlane(e) + 1u;
    if (hw == 0) {
      unsigned r0, r1;
      PROJ_SUM(0, r0, r1)
      if (g == 0) {
        __builtin_amdgcn_raw_buffer_store_b128(u32x4{r0, tag, r1, tag}, RSRC(xme, 512), c * 16, 0, AUX_SC1);
        *(LAS u32x2*)(L + L_Q + 128 * s + 8 * c) = u32x2{r0, r1};
      }
      if (lane == 0) *(LAS unsigned*)(L + F_TAG) = tag;
      LGKM0();
      if (lane == 0) {
        __hip_atomic_fetch_add((LAS unsigned*)(L + F_Q), 1u, RLX_WG);
      }
      STAMP(6);
    } else if (hw == 1) {
      lds_wait_ge(L + F_P0, 8u);
      float v[1];
      sweep<1, 1>(xot + (lane & 31), 1, tag, v);
      if (lane < 32) *(LAS float*)(L + L_Q + 128 * (s ^ 1) + 4 * lane) = v[0];
      LGKM0();
      if (lane == 0) __hip_atomic_fetch_add((LAS unsigned*)(L + F_Q), 1u, RLX_WG);
      STAMP(6);
    }
  }
  __builtin_amdgcn_s_setprio(0);
  lds_wait_ge(L + F_Q, 2u);
  tag = __builtin_amdgcn_readfirstlane(lds_ld(L + F_TAG));
  if (ABL_SIDE == 2) { STAMP_OUT(); return; }
#define WAIT_UNTIL(t) while ((int)((unsigned)__builtin_amdgcn_s_memrealtime() - tb - (t)) < 0) __builtin_amdgcn_s_sleep(4)
#define PAUSE_ADD(v) do { if (lane == 0) __hip_atomic_fetch_add((LAS unsigned*)(L + F_PAUSE), (v), RLX_WG); SB(); } while (0)
  f32x2 ysum = {0.f, 0.f};
  {
#pragma unroll
    for (int i = 0; i < 10; ++i) FRESH(xp[i]);
    if (hw == ((mem + 2) & 7)) {
      const __amdgpu_buffer_rsrc_t rp = RSRC(wo + (size_t)(h * DH + 16 * mem) * DM, 16 * DM * 2);
#pragma unroll
      for (int i = 0; i < 20; ++i) {
        VMWAIT(FT_DEPTH - 1);
        wr[i] = __builtin_amdgcn_raw_buffer_load_b128(rp, lane * 16, i * 1024, W_AUX);
        SB();
      }
#pragma unroll
      for (int i = 0; i < 20; ++i) asm volatile("" ::"v"(wr[i]));
    }
    const bool burst = (hw >> 1) != b;
    if (burst) {
      WAIT_UNTIL(STAG1_0 + turn * STAG1);
      PAUSE_ADD(1u);
    }
    const __amdgpu_buffer_rsrc_t rg = RSRC((s ? wu : wg) + (size_t)80 * hw * FF + 64 * hs, 80 * FF * 2 - 64 * hs * 2);
    const int vg = ((ROWG * g + (c >> 3)) * FF + 8 * (c & 7)) * 2;
#pragma unroll
    for (int i = 0; i < 10; ++i) {
      VMWAIT(GU_DEPTH - 1);
      wr[i] = __builtin_amdgcn_raw_buffer_load_b128(rg, vg, i * ROWI * FF * 2, W_AUX);
      SB();
    }
    const __amdgpu_buffer_rsrc_t rd = RSRC(wd + (size_t)(64 * hs + 32 * s + 4 * hw) * DM, 4 * DM * 2);
#pragma unroll
    for (int cb = 0; cb < 5; ++cb) {
      VMWAIT(GU_DEPTH - 1);
      wr[10 + cb] = __builtin_amdgcn_raw_buffer_load_b128(rd, (g * DM + 8 * c) * 2, cb * 256, W_AUX);
      SB();
    }
    if (burst) PAUSE_ADD(~0u);
    PROJ_MFMA(1)
    if (hw == 2) {
      unsigned r0, r1;
      PROJ_SUM(1, r0, r1)
      if (g == 0 && (c >> 3) != s) __builtin_amdgcn_raw_buffer_store_b128(u32x4{r0, tag, r1, tag}, RSRC(xme + 32, 128), (c & 7) * 16, 0, AUX_SC1);
      float v[2];
      sweep<2, 16>(xot + 32 + 2 * (c & 7), 1, tag, v);
      const unsigned o0 = __float_as_uint(v[0]), o1 = __float_as_uint(v[1]);
      if (g == 0 && (c >> 3) == s)
        *(LAS u32x2*)(L + L_H + 8 * (c & 7)) = u32x2{pk2(bflo(r0) * bflo(o0), bfhi(r0) * bfhi(o0)), pk2(bflo(r1) * bflo(o1), bfhi(r1) * bfhi(o1))};
      LGKM0();
      if (lane == 0) __hip_atomic_store((LAS unsigned*)(L + F_H), 1u, RLX_WG);
    }
    lds_wait_ge(L + F_H, 1u);
    const unsigned X = *(const LAS unsigned short*)(L + L_H + 2 * (4 * hw + g));
    const u32x4 a = make_a(X, sel);
#pragma unroll
    for (int cb = 0; cb < 5; ++cb) {
      const f32x4 yf = mfma(a, wr[10 + cb], f32x4{0.f, 0.f, 0.f, 0.f});
      if (g < 2) *(LAS f32x4*)(L + L_BIG + (hw * DM + 128 * cb + 8 * c + 4 * g) * 4) = yf;
    }
    LGKM0();
    if (lane == 0) __hip_atomic_fetch_add((LAS unsigned*)(L + F_FF), 1u, RLX_WG);
    if (hw < 5 && h < NSL) {
      lds_wait_ge(L + F_FF, 8u);
#pragma unroll
      for (int p = 0; p < 8; ++p) ysum += *(const LAS f32x2*)(L + L_BIG + (p * DM + 2 * tid) * 4);
    }
  }
  STAMP(2);
  {
    WAIT_UNTIL(STAG2_0 + turn * STAG2);
    PAUSE_ADD(1u);
    const __amdgpu_buffer_rsrc_t ro = RSRC(wo + (size_t)(h * DH + 16 * hw) * DM, 16 * DM * 2);
    const int vo = (4 * g * DM + 8 * c) * 2;
#pragma unroll
    for (int j = 0; j < 4; ++j)
#pragma unroll
      for (int cb = 0; cb < 5; ++cb) {
        VMWAIT(WO_DEPTH - 1);
        wr[j * 5 + cb] = __builtin_amdgcn_raw_buffer_load_b128(ro, vo, (j * DM + cb * 128) * 2, W_AUX);
        SB();
      }
    PAUSE_ADD(~0u);
  }
  if (hw < 5) {
    const u32x4 pre = __builtin_amdgcn_raw_buffer_load_b128(RSRC(ws + G_Y + (size_t)(b * 64 + h * 2 + s) * DM, DM * 8), tid * 16, 0, YPRE - 1);
    asm volatile("" ::"v"(pre));
  }
  STAMP(7);
  if (hw == 6) {
    lds_wait_ge(L + F_ML, 8u);
    const float mv = *(const LAS float*)(L + L_ML + 4 * (lane & 7)), lv = *(const LAS float*)(L + L_ML + 32 + 4 * (lane & 7));
    float M = fmaxf(mv, dppf<XOR1>(mv));
    M = fmaxf(M, dppf<XOR2>(M));
    M = fmaxf(M, dppf<XOR7>(M));
    float Ls = __builtin_amdgcn_exp2f(mv - M) * lv;
    Ls += dppf<XOR1>(Ls);
    Ls += dppf<XOR2>(Ls);
    Ls += dppf<XOR7>(Ls);
    if (lane < 2) put(xme + 48 + lane, lane == 0 ? M : Ls, tag);
    float o2[2];
    sweep<2, 16>(xot + 48, 1, tag, o2);
    const float Mt = fmaxf(M, o2[0]);
    const float e0 = __builtin_amdgcn_exp2f(M - Mt) * Ls, e1 = __builtin_amdgcn_exp2f(o2[0] - Mt) * o2[1];
    if (lane < 8) *(LAS float*)(L + L_EW + 4 * lane) = __builtin_amdgcn_exp2f(mv - Mt) / (e0 + e1);
    STAMP(5);
  } else if (hw == 7 && bid == 0) {
    lds_wait_ge(L + F_ML, 8u);
    float f4[4];
    sweep<4, 64>(ws + G_X + lane * 64 + 48, 64 * 64, tag, f4);
    if (lane == 0) __hip_atomic_store((GAS unsigned*)(ws + G_E), tag, RLX_AGENT);
  }
  int a_acc = L_ACC + (c * ACC_LD + 16 * hw + 4 * g) * 4, a_ew = L_EW + 4 * (c >> 1);
  int a_wr = L_BIG + (hw * DM + 64 * (g & 1) + 4 * c) * 4;
  int a_rd = L_BIG + ((tid >> 6) * 128 + ((tid >> 1) & 1) * 64 + ((tid >> 2) & 15) * 4 + (tid & 1) * 2) * 4;
  int v_st = tid * 16;
  const __amdgpu_buffer_rsrc_t rpub = RSRC(ws + G_Y + (size_t)(b * 64 + h * 2 + s) * DM, DM * 8);
  FRESH(a_acc); FRESH(a_ew); FRESH(a_wr); FRESH(a_rd); FRESH(v_st);
  asm volatile("" ::"s"(rpub));
  if (hw >= 4) __builtin_amdgcn_s_setprio(1);
  WG_BARRIER();
  STAMP(3);
  {
    f32x4 o = *(const LAS f32x4*)(L + a_acc) * *(const LAS float*)(L + a_ew);
#pragma unroll
    for (int j = 0; j < 4; ++j) o[j] += dppf<XOR1>(o[j]);
#pragma unroll
    for (int j = 0; j < 4; ++j) o[j] += dppf<XOR2>(o[j]);
#pragma unroll
    for (int j = 0; j < 4; ++j) o[j] += dppf<XOR7>(o[j]);
#pragma unroll
    for (int j = 0; j < 4; ++j) o[j] += dppf<XOR8>(o[j]);
    const unsigned x01 = pk2(o[0], o[1]), x23 = pk2(o[2], o[3]);
    u32x4 a[4] = {make_a(x01, sel), make_a(x01 >> 16, sel), make_a(x23, sel), make_a(x23 >> 16, sel)};
#pragma unroll
    for (int j = 0; j < 4; ++j) FRESH(a[j]);
    __builtin_amdgcn_s_setprio(0);
    SB();
    f32x4 y[5];
#pragma unroll
    for (int cb = 0; cb < 5; ++cb) y[cb] = f32x4{0.f, 0.f, 0.f, 0.f};
#pragma unroll
    for (int j = 0; j < 4; ++j)
#pragma unroll
      for (int cb = 0; cb < 5; ++cb) {
        y[cb] = mfma(a[j], wr[j * 5 + cb], y[cb]);
        SB();
      }
    if (g < 2) {
#pragma unroll
      for (int cb = 0; cb < 5; ++cb) *(LAS f32x4*)(L + a_wr + 512 * cb) = y[cb];
    }
  }
  WG_BARRIER();
  if (hw < 5 && ABL_FINAL) {
    f32x2 t8[8];
#pragma unroll
    for (int p = 0; p < 8; ++p) t8[p] = *(const LAS f32x2*)(L + a_rd + p * DM * 4);
    const f32x2 t = (((t8[0] + t8[1]) + (t8[2] + t8[3])) + ((t8[4] + t8[5]) + (t8[6] + t8[7]))) + ysum;
    __builtin_amdgcn_raw_buffer_store_b128(u32x4{__float_as_uint(t[0]), tag, __float_as_uint(t[1]), tag}, rpub, v_st, 0, AUX_SC1);
  }
  STAMP(4);
  STAMP_OUT();
}

extern "C" void kernel_launch(void* const* d_in, const int* in_sizes, int n_in, void* d_out, int out_size, void* d_ws,
                              size_t ws_size, hipStream_t stream) {
  (void)in_sizes; (void)n_in; (void)out_size; (void)ws_size;
  typedef const GAS unsigned short* cp;
  k_main<<<NWG, 1024, 0, stream>>>((cp)d_in[0], (cp)d_in[1], (cp)d_in[2], (cp)d_in[3], (cp)d_in[4], (cp)d_in[6], (cp)d_in[7],
                                   (cp)d_in[8], (GAS u64*)d_ws, (GAS unsigned*)d_out);
}
```

```cpp
#include <hip/hip_runtime.h>
#include <hip/hip_bf16.h>
#include <stdint.h>

#define DEPTH 4
#define DEPTH_LO 3
#define DEPTH_HI 5
#define LAG1 70
#define W_DEPTH 6
#define XORG 105
#define PACE0 450
#define PACE_A 128
#define PACE 102
#define STAG1_0 950
#define STAG1 125
#define JUMP1 140
#define STAG2_0 1950
#define STAG2 205
#define JUMP2 240
#define TAILFREE 8
#define YDELAY 22
#define YPOLL_N 8
#define YPOLL_S 8
#define YPOLL_S0 1
#define RMODE 0
#define RWAVES 1
#define RSTAG 8
constexpr int AUX_NT = 2, AUX_SC1 = 16;

typedef unsigned u32x4 __attribute__((ext_vector_type(4)));
typedef unsigned u32x2 __attribute__((ext_vector_type(2)));
typedef float f32x4 __attribute__((ext_vector_type(4)));
typedef float f32x2 __attribute__((ext_vector_type(2)));
typedef short bf16x8 __attribute__((ext_vector_type(8)));
typedef __bf16 bf16x2_t __attribute__((ext_vector_type(2)));
typedef short s16x4 __attribute__((ext_vector_type(4)));
typedef unsigned long long u64;
#define GAS __attribute__((address_space(1)))
#define LAS __attribute__((address_space(3)))
#define RLX_AGENT __ATOMIC_RELAXED, __HIP_MEMORY_SCOPE_AGENT
#define RLX_WG __ATOMIC_RELAXED, __HIP_MEMORY_SCOPE_WORKGROUP

constexpr int NB = 4, DM = 640, NH = 32, SK = 4096, DH = 128, FF = 1728;
constexpr int NSL = FF / 64;
constexpr int NWG = 256;
constexpr float LOG2E = 1.4426950408889634f;
constexpr float QSCALE = 0.08838834764831845f;
constexpr unsigned SPIN_CAP = 1u << 18;

constexpr size_t G_E = 0;
constexpr size_t G_X = 32;
constexpr size_t G_Y = G_X + (size_t)NWG * 64;

constexpr int L_XQ = 0;
constexpr int L_Q = L_XQ + DM * 2;
constexpr int L_P = L_Q + 256;
constexpr int O_LD = 272;
constexpr int L_O = L_P + 2 * 16 * 64 * 4;
constexpr int L_ML = L_O + 16 * O_LD;
constexpr int L_EW = L_ML + 64;
constexpr int L_FLAG = L_EW + 32;
constexpr int F_Q = L_FLAG, F_ML = L_FLAG + 4, F_H = L_FLAG + 8, F_P0 = L_FLAG + 12, F_P1 = L_FLAG + 16, F_FF = L_FLAG + 20,
              F_RED = L_FLAG + 24, F_TAG = L_FLAG + 28, F_T0 = L_FLAG + 32, F_EW = L_FLAG + 36, F_HS = L_FLAG + 40;
constexpr int L_H = L_FLAG + 64;
constexpr int L_RED = L_H + 64;
constexpr int L_FF = L_RED + 512;
constexpr int ST_LD = DM * 2 + 16;
constexpr int L_ST = L_FF + 8 * DM * 4;
constexpr int L_END = L_ST + 64 * ST_LD;

__device__ __forceinline__ float bflo(unsigned u) { return __builtin_bit_cast(float, u << 16); }
__device__ __forceinline__ float bfhi(unsigned u) { return __builtin_bit_cast(float, u & 0xffff0000u); }
__device__ __forceinline__ unsigned pk2(float lo, float hi) {
  bf16x2_t r;
  r[0] = (__bf16)lo;
  r[1] = (__bf16)hi;
  return __builtin_bit_cast(unsigned, r);
}
__device__ __forceinline__ float rbf(float x) { return (float)(__bf16)x; }
__device__ __forceinline__ float dot2(unsigned a, unsigned b, float c) {
  return __builtin_amdgcn_fdot2_f32_bf16(__builtin_bit_cast(bf16x2_t, a), __builtin_bit_cast(bf16x2_t, b), c, false);
}
template <int CTRL> __device__ __forceinline__ float dppf(float x) {
  return __builtin_bit_cast(float, __builtin_amdgcn_mov_dpp(__builtin_bit_cast(int, x), CTRL, 0xf, 0xf, true));
}
template <int CTRL> __device__ __forceinline__ unsigned dppu(unsigned x) {
  return (unsigned)__builtin_amdgcn_mov_dpp((int)x, CTRL, 0xf, 0xf, true);
}
constexpr int XOR1 = 0xB1, XOR2 = 0x4E, XOR7 = 0x141, XOR8 = 0x128;
template <int CTRL> __device__ __forceinline__ float tr_merge(float lo, float hi, bool up) {
  const float t1 = lo + dppf<CTRL>(lo);
  const float t2 = hi + dppf<CTRL>(hi);
  return up ? t2 : t1;
}
__device__ __forceinline__ float row16_sum(float x) {
  x += dppf<XOR1>(x); x += dppf<XOR2>(x); x += dppf<XOR7>(x); x += dppf<XOR8>(x);
  return x;
}
__device__ __forceinline__ float row16_max(float x) {
  x = fmaxf(x, dppf<XOR1>(x)); x = fmaxf(x, dppf<XOR2>(x)); x = fmaxf(x, dppf<XOR7>(x)); x = fmaxf(x, dppf<XOR8>(x));
  return x;
}
__device__ __forceinline__ float half_sum(float x) {
  auto t = __builtin_amdgcn_permlane32_swap(__float_as_uint(x), __float_as_uint(x), false, false);
  return __uint_as_float(t[0]) + __uint_as_float(t[1]);
}
__device__ __forceinline__ float xrow_max(float x) {
  auto s = __builtin_amdgcn_permlane16_swap(__float_as_uint(x), __float_as_uint(x), false, false);
  x = fmaxf(__uint_as_float(s[0]), __uint_as_float(s[1]));
  auto t = __builtin_amdgcn_permlane32_swap(__float_as_uint(x), __float_as_uint(x), false, false);
  return fmaxf(__uint_as_float(t[0]), __uint_as_float(t[1]));
}
__device__ __forceinline__ float xrow_sum(float x) {
  auto s = __builtin_amdgcn_permlane16_swap(__float_as_uint(x), __float_as_uint(x), false, false);
  x = __uint_as_float(s[0]) + __uint_as_float(s[1]);
  return half_sum(x);
}
__device__ __forceinline__ float swap32_sum(float a, float b) {
  auto t = __builtin_amdgcn_permlane32_swap(__float_as_uint(a), __float_as_uint(b), false, false);
  return __uint_as_float(t[0]) + __uint_as_float(t[1]);
}
__device__ __forceinline__ float swap16_sum(float a, float b) {
  auto t = __builtin_amdgcn_permlane16_swap(__float_as_uint(a), __float_as_uint(b), false, false);
  return __uint_as_float(t[0]) + __uint_as_float(t[1]);
}
__device__ __forceinline__ f32x4 mfma(u32x4 a, u32x4 b, f32x4 c) {
  return __builtin_amdgcn_mfma_f32_16x16x32_bf16(__builtin_bit_cast(bf16x8, a), __builtin_bit_cast(bf16x8, b), c, 0, 0, 0);
}
#define LGKM0() asm volatile("s_waitcnt lgkmcnt(0)" ::: "memory")
#define WG_BARRIER()                \
  do {                              \
    LGKM0();                        \
    __builtin_amdgcn_s_barrier();   \
    asm volatile("" ::: "memory");  \
  } while (0)
#define SB() __builtin_amdgcn_sched_barrier(0)
#define STAMP(k)
#define STAMP_DECL
#define STAMP_OUT()
#define RSRC(p, n) __builtin_amdgcn_make_buffer_rsrc((void*)(p), 0, (n), 0x00020000)
#define FRESH(v) asm volatile("" : "+v"(v))
#define VMWAIT_UNLESS(n, v, k, m) \
  asm volatile("s_waitcnt vmcnt(%3)\n\ts_cmp_ge_u32 %1, %2\n\ts_cbranch_scc1 1f\n\ts_waitcnt vmcnt(%0)\n1:" ::"i"(n), "s"(v), "i"(k), "i"(m) : "memory", "scc")
#define VMWAIT(n) asm volatile("s_waitcnt vmcnt(%0)" ::"i"(n) : "memory")

__device__ __forceinline__ void put(GAS u64* p, float v, unsigned tag) {
  __hip_atomic_store(p, ((u64)tag << 32) | (u64)__float_as_uint(v), RLX_AGENT);
}
__device__ __forceinline__ u64 get(const GAS u64* p) { return __hip_atomic_load(p, RLX_AGENT); }
__device__ __forceinline__ unsigned lds_ld(LAS unsigned char* p) { return __hip_atomic_load((LAS unsigned*)p, RLX_WG); }
__device__ __forceinline__ void lds_wait_ge(LAS unsigned char* p, unsigned want) {
  while (lds_ld(p) < want) __builtin_amdgcn_s_sleep(1);
  asm volatile("" ::: "memory");
}
template <int N, int SLP> __device__ __forceinline__ void sweep(const GAS u64* p, int S, unsigned tag, float (&v)[N]) {
  for (unsigned spins = 0;; ++spins) {
    bool ok = true;
#pragma unroll
    for (int k = 0; k < N; ++k) {
      const u64 x = get(p + k * S);
      v[k] = __uint_as_float((unsigned)x);
      ok &= (unsigned)(x >> 32) == tag;
    }
    if (__all(ok) || spins > SPIN_CAP) return;
    __builtin_amdgcn_s_sleep(SLP);
  }
}

struct Sel { unsigned s[4]; };
__device__ __forceinline__ Sel make_sel(int lane) {
  const int m = lane & 15;
  const unsigned src = (m & 8) ? 0x0706u : 0x0504u;
  const unsigned placed = (m & 1) ? ((src << 16) | 0x0c0cu) : (0x0c0c0000u | src);
  Sel r;
#pragma unroll
  for (int jj = 0; jj < 4; ++jj) r.s[jj] = (((m & 7) >> 1) == jj) ? placed : 0x0c0c0c0cu;
  return r;
}
__device__ __forceinline__ u32x4 make_a(unsigned X, const Sel& s) {
  u32x4 a;
#pragma unroll
  for (int jj = 0; jj < 4; ++jj) a[jj] = __builtin_amdgcn_perm(X, 0u, s.s[jj]);
  return a;
}
__device__ __forceinline__ unsigned split_hi_lo(float v) {
  const float h = rbf(v);
  return pk2(h, v - h);
}

__device__ __forceinline__ void ln_row(const unsigned (&xr)[5], LAS unsigned char* dst, int lane) {
  float sum = 0.f;
#pragma unroll
  for (int k = 0; k < 5; ++k) sum += bflo(xr[k]) + bfhi(xr[k]);
  const float mean = xrow_sum(row16_sum(sum)) * (1.0f / DM);
  float ss = 0.f;
#pragma unroll
  for (int k = 0; k < 5; ++k) {
    const float a = bflo(xr[k]) - mean, bb = bfhi(xr[k]) - mean;
    ss = fmaf(a, a, ss);
    ss = fmaf(bb, bb, ss);
  }
  const float var = xrow_sum(row16_sum(ss)) * (1.0f / DM);
  const float mu = rbf(mean);
  const float rinv = 1.0f / rbf(sqrtf(rbf(rbf(var) + 1e-5f)));
#pragma unroll
  for (int k = 0; k < 5; ++k)
    *(LAS unsigned*)(dst + 4 * (lane + 64 * k)) = pk2(rbf(bflo(xr[k]) - mu) * rinv, rbf(bfhi(xr[k]) - mu) * rinv);
}

__global__ void __launch_bounds__(1024)
k_main(const GAS unsigned short* __restrict__ x, const GAS unsigned short* __restrict__ kc, const GAS unsigned short* __restrict__ vc,
       const GAS unsigned short* __restrict__ wq, const GAS unsigned short* __restrict__ wo, const GAS unsigned short* __restrict__ wg,
       const GAS unsigned short* __restrict__ wu, const GAS unsigned short* __restrict__ wd, GAS u64* ws, GAS unsigned short* out) {
  __shared__ __attribute__((aligned(16))) unsigned char smem[L_END];
  LAS unsigned char* L = (LAS unsigned char*)smem;
  const int tid = threadIdx.x, lane = tid & 63, wave = __builtin_amdgcn_readfirstlane(tid >> 6);
  const int g = lane >> 4, c = lane & 15;
  const int bid = blockIdx.x;
  const int jx = bid >> 3;
  const int h = (jx >> 3) * 8 + (bid & 7), mem = jx & 7, b = mem >> 1, s = mem & 1;
  const int slot = (b * NH + h) * 2 + s;
  const int hs = h < NSL ? h : h - 8;
  const unsigned turn = (mem + 2 * (jx >> 3)) & 7;
  STAMP_DECL;
  STAMP(0);
  if (tid < 16) *(LAS unsigned*)(L + L_FLAG + 4 * tid) = 0u;
  asm volatile("" ::"s"(x), "s"(kc), "s"(vc), "s"(wq), "s"(wo), "s"(wg), "s"(wu), "s"(wd), "s"(ws), "s"(out));
  WG_BARRIER();
  const Sel sel = make_sel(lane);
  GAS u64* const xme = ws + G_X + (size_t)slot * 64;
  const GAS u64* const xot = ws + G_X + (size_t)(slot ^ 1) * 64;
#define CLOCK() ((unsigned)__builtin_amdgcn_s_memrealtime())

  if (wave >= 8) {
    const int w = wave - 8;
    const size_t base = ((size_t)(b * NH + h) * SK + (size_t)s * (SK / 2)) * DH;
    const GAS unsigned short* kb = kc + base;
    const GAS unsigned short* vb = vc + base;
    int voff = lane * 16 + w * 4096;
#define TILE_LD(r, so, t) __builtin_amdgcn_raw_buffer_load_b128((r), voff + ((t) & 3) * 1024, (so) + ((t) >> 2) * 32768, AUX_NT)
    u32x4 ring[16];
    {
      const __amdgpu_buffer_rsrc_t rk = RSRC(kb, SK / 2 * DH * 2);
#pragma unroll
      for (int t = 0; t < 16; ++t) {
        if (t >= DEPTH) VMWAIT(DEPTH - 1);
        ring[t] = TILE_LD(rk, 0, t);
        SB();
      }
    }
    STAMP(6);
    lds_wait_ge(L + F_Q, 2u);
    STAMP(1);
    const u32x4 q = *(const LAS u32x4*)(L + L_Q + 16 * c);
    const unsigned tb = __builtin_amdgcn_readfirstlane(lds_ld(L + F_T0));
    unsigned pstep = PACE_A, lag = 0;
    unsigned tgt = PACE0 + w * (PACE / 8), j1 = STAG1_0 + turn * STAG1, j2 = STAG2_0 + turn * STAG2;
#define PACED_WAIT(t)                                                         \
  if (((t) & 3) == 0) {                                                       \
    tgt += ((t) > 16 - TAILFREE) ? ptail : pstep;                             \
    if (tgt >= j1) { tgt += JUMP1; j1 = ~0u; }                                \
    if (tgt >= j2) { tgt += JUMP2; j2 = ~0u; }                                \
    unsigned now;                                                             \
    while ((now = CLOCK() - tb) < tgt && now < (1u << 20)) __builtin_amdgcn_s_sleep(1); \
    lag = now - tgt;                                                          \
  }                                                                           \
  if ((t) == 16 - TAILFREE) lag |= dtail;                                     \
  VMWAIT_UNLESS(DEPTH_LO - 1, lag, LAG1, DEPTH_HI - 1);
    float sc0 = 0.f, sc1 = 0.f, sc2 = 0.f, sc3 = 0.f;
#define MERGE(LO, HI, CTRL, BIT) tr_merge<CTRL>((LO), (HI), (c & (BIT)) != 0)
#pragma unroll 1
    for (int grp = 0; grp < 4; ++grp) {
      const __amdgpu_buffer_rsrc_t rn = RSRC(grp < 3 ? kb : vb, SK / 2 * DH * 2);
      const int so = ((grp + 1) & 3) * 131072;
      const unsigned ptail = pstep, dtail = 0;
      FRESH(voff);
      float d1 = 0.f, d2 = 0.f, d4 = 0.f, d8 = 0.f;
#pragma unroll
      for (int t = 0; t < 16; ++t) {
        float a = dot2(ring[t][0], q[0], 0.f);
        a = dot2(ring[t][1], q[1], a);
        a = dot2(ring[t][2], q[2], a);
        a = dot2(ring[t][3], q[3], a);
        FRESH(a);
        SB();
        PACED_WAIT(t)
        ring[t] = TILE_LD(rn, so, t);
        if (!(t & 1)) d1 = a;
        else {
          a = MERGE(d1, a, XOR8, 8);
          if (!(t & 2)) d2 = a;
          else {
            a = MERGE(d2, a, XOR7, 4);
            if (!(t & 4)) d4 = a;
            else {
              a = MERGE(d4, a, XOR2, 2);
              if (!(t & 8)) d8 = a;
              else {
                a = MERGE(d8, a, XOR1, 1);
                sc0 = sc1; sc1 = sc2; sc2 = sc3;
                sc3 = rbf(rbf(a) * QSCALE) * LOG2E;
              }
            }
          }
        }
        SB();
      }
      pstep = PACE;
    }
#undef MERGE
    STAMP(2);
    float mw = fmaxf(fmaxf(sc0, sc1), fmaxf(sc2, sc3));
    mw = xrow_max(row16_max(mw));
    const float p0 = __builtin_amdgcn_exp2f(sc0 - mw), p1 = __builtin_amdgcn_exp2f(sc1 - mw);
    const float p2 = __builtin_amdgcn_exp2f(sc2 - mw), p3 = __builtin_amdgcn_exp2f(sc3 - mw);
    unsigned pk0 = split_hi_lo(p0), pk1 = split_hi_lo(p1), pk2_ = split_hi_lo(p2), pk3 = split_hi_lo(p3);
    const float lw = xrow_sum(row16_sum((p0 + p1) + (p2 + p3)));
    if (lane == 0) {
      *(LAS float*)(L + L_ML + w * 4) = mw;
      *(LAS float*)(L + L_ML + 32 + w * 4) = lw;
      LGKM0();
      __hip_atomic_fetch_add((LAS unsigned*)(L + F_ML), 1u, RLX_WG);
    }
    SB();
    f32x4 acc = {0.f, 0.f, 0.f, 0.f};
#define BREV4(T) ((((T) & 1) << 3) | (((T) & 2) << 1) | (((T) & 4) >> 1) | (((T) & 8) >> 3))
#define PV_STEP(T, REFILL)                              \
  {                                                     \
    const unsigned X = dppu<0x150 + BREV4(T)>(pk0);     \
    acc = mfma(make_a(X, sel), ring[T], acc);           \
    if (REFILL) {                                       \
      SB();                                             \
      PACED_WAIT(T)                                     \
      ring[T] = TILE_LD(rv, so, T);                     \
    }                                                   \
    SB();                                               \
  }
#define PV_GROUP(R)                                                                                               \
  PV_STEP(0, R) PV_STEP(1, R) PV_STEP(2, R) PV_STEP(3, R) PV_STEP(4, R) PV_STEP(5, R) PV_STEP(6, R) PV_STEP(7, R) \
  PV_STEP(8, R) PV_STEP(9, R) PV_STEP(10, R) PV_STEP(11, R) PV_STEP(12, R) PV_STEP(13, R) PV_STEP(14, R) PV_STEP(15, R)
    const __amdgpu_buffer_rsrc_t rv = RSRC(vb, SK / 2 * DH * 2);
#pragma unroll 1
    for (int grp = 0; grp < 3; ++grp) {
      const int so = (grp + 1) * 131072;
      const unsigned ptail = grp == 2 ? 0u : pstep, dtail = (unsigned)(grp >> 1) << 20;
      FRESH(voff);
      PV_GROUP(true)
      pk0 = pk1; pk1 = pk2_; pk2_ = pk3;
    }
    lds_wait_ge(L + F_EW, 1u);
    const float ew = *(const LAS float*)(L + L_EW + 4 * w);
    int a_o = L_O + (w * 2 + (g >> 1)) * O_LD + (8 * c + 4 * (g & 1)) * 2;
    FRESH(a_o);
    SB();
    {
      const int so = 0;
      const unsigned ptail = 0, dtail = 0;
      PV_GROUP(false)
    }
#undef PV_GROUP
#undef PV_STEP
    STAMP(3);
    *(LAS u32x2*)(L + a_o) = u32x2{pk2(acc[0] * ew, acc[1] * ew), pk2(acc[2] * ew, acc[3] * ew)};
    WG_BARRIER();
    const int j = h * 2 + s;
    if (j >= 40) { STAMP_OUT(); return; }
    const int r5 = j % 5, k8 = lane & 7;
    const int col0 = b * DM + 80 * (j / 5) + (r5 < 4 ? 32 * (r5 >> 1) + 8 * (r5 & 1) : 64) + k8, col1 = col0 + (r5 < 4 ? 16 : 8);
    const unsigned x0 = x[col0], x1 = x[col1];
    const unsigned tag = lds_ld(L + F_TAG);
    STAMP(4);
    const __amdgpu_buffer_rsrc_t ry = RSRC(ws + G_Y + (size_t)(b * 64 + 8 * w) * DM + 16 * j, 8 * DM * 8);
    u32x4 yv = {0u, 0u, 0u, 0u};
    bool miss = true;
    __builtin_amdgcn_s_sleep(YDELAY);
    for (unsigned spins = 0; spins < SPIN_CAP; ++spins) {
      if (miss) {
        yv = __builtin_amdgcn_raw_buffer_load_b128(ry, (lane >> 3) * (DM * 8) + k8 * 16, 0, AUX_SC1);
        miss = yv[1] != tag || yv[3] != tag;
      }
      const int nmiss = __builtin_popcountll(__builtin_amdgcn_ballot_w64(miss));
      if (nmiss == 0) break;
      if (nmiss > YPOLL_N) __builtin_amdgcn_s_sleep(YPOLL_S); else __builtin_amdgcn_s_sleep(YPOLL_S0);
    }
    float s0 = __uint_as_float(yv[0]), s1 = __uint_as_float(yv[2]);
    s0 += dppf<XOR8>(s0); s1 += dppf<XOR8>(s1);
    s0 = xrow_sum(s0); s1 = xrow_sum(s1);
    if (lane < 8) *(LAS f32x2*)(L + L_RED + (w * 8 + lane) * 8) = f32x2{s0, s1};
    LGKM0();
    unsigned old = 0;
    if (lane == 0) old = __hip_atomic_fetch_add((LAS unsigned*)(L + F_RED), 1u, RLX_WG);
    asm volatile("" ::: "memory");
    if (__builtin_amdgcn_readfirstlane(old) == 7u) {
      const f32x2 t = *(const LAS f32x2*)(L + L_RED + lane * 8);
      s0 = t[0]; s1 = t[1];
      s0 += dppf<XOR8>(s0); s1 += dppf<XOR8>(s1);
      s0 = xrow_sum(s0); s1 = xrow_sum(s1);
      const unsigned r = pk2(bflo(x0) + s0, bflo(x1) + s1);
      if (lane < 8) {
        out[col0] = (unsigned short)r;
        out[col1] = (unsigned short)(r >> 16);
      }
    }
    STAMP(5);
    STAMP_OUT();
    return;
  }

  const int hw = wave;
  __builtin_amdgcn_s_setprio(3);
  u32x4 wr[20];
  unsigned xp[10];
#define PROJ_MFMA(PH)                                                                                              \
  {                                                                                                                \
    f32x4 ga = {0.f, 0.f, 0.f, 0.f};                                                                               \
    _Pragma("unroll") for (int i = 0; i < 10; ++i) ga = mfma(make_a(xp[i], sel), wr[i], ga);                       \
    if ((g >> 1) == (c >> 3))                                                                                      \
      *(LAS f32x4*)(L + L_P + ((PH) * 1024 + (hw * 2 + (g >> 1)) * 64 + 8 * (c & 7) + 4 * (g & 1)) * 4) = ga;      \
    LGKM0();                                                                                                       \
    if (lane == 0) __hip_atomic_fetch_add((LAS unsigned*)(L + ((PH) ? F_P1 : F_P0)), 1u, RLX_WG);                   \
  }
#define PROJ_SUM(PH, R0, R1)                                                                                       \
  {                                                                                                                \
    lds_wait_ge(L + ((PH) ? F_P1 : F_P0), 8u);                                                                     \
    f32x4 t4 = {0.f, 0.f, 0.f, 0.f};                                                                               \
    _Pragma("unroll") for (int k = 0; k < 4; ++k) t4 += *(const LAS f32x4*)(L + L_P + ((PH) * 1024 + (4 * g + k) * 64 + 4 * c) * 4); \
    _Pragma("unroll") for (int k = 0; k < 4; ++k) t4[k] = xrow_sum(t4[k]);                                         \
    R0 = pk2(t4[0], t4[1]);                                                                                        \
    R1 = pk2(t4[2], t4[3]);                                                                                        \
  }
  unsigned tag = 0;
  {
    unsigned xr[5];
#pragma unroll
    for (int k = 0; k < 5; ++k) xr[k] = ((const GAS unsigned*)(x + b * DM))[lane + 64 * k];
    unsigned e = 0;
    if (hw < 2) e = *(const GAS unsigned*)(ws + G_E);
    const __amdgpu_buffer_rsrc_t rq = RSRC(wq + (size_t)80 * hw * (NH * DH) + h * DH + 64 * s, 80 * NH * DH * 2 - (h * DH + 64 * s) * 2);
    const int vq = ((20 * g + (c >> 3)) * NH * DH + 8 * (c & 7)) * 2;
#pragma unroll
    for (int i = 0; i < 10; ++i) {
      wr[i] = __builtin_amdgcn_raw_buffer_load_b128(rq, vq, i * 2 * NH * DH * 2, 0);
      SB();
    }
    ln_row(xr, L + L_XQ, lane);
    SB();
    if (hw == 0) {
      const unsigned t1 = CLOCK() - XORG;
      if (lane == 0) *(LAS unsigned*)(L + F_T0) = t1;
    }
    LGKM0();
#pragma unroll
    for (int k = 0; k < 5; ++k) {
      const u32x2 t = *(const LAS u32x2*)(L + L_XQ + (80 * hw + 20 * g + 4 * k) * 2);
      xp[2 * k] = t[0];
      xp[2 * k + 1] = t[1];
    }
    PROJ_MFMA(0)
    STAMP(1);
    if (hw < 2) tag = __builtin_amdgcn_readfirstlane(e) + 1u;
    if (hw == 0) {
      unsigned r0, r1;
      PROJ_SUM(0, r0, r1)
      if (g == 0) {
        __builtin_amdgcn_raw_buffer_store_b128(u32x4{r0, tag, r1, tag}, RSRC(xme, 512), c * 16, 0, AUX_SC1);
        *(LAS u32x2*)(L + L_Q + 128 * s + 8 * c) = u32x2{r0, r1};
      }
      if (lane == 0) *(LAS unsigned*)(L + F_TAG) = tag;
      LGKM0();
      if (lane == 0) __hip_atomic_fetch_add((LAS unsigned*)(L + F_Q), 1u, RLX_WG);
      STAMP(6);
    } else if (hw == 1) {
      lds_wait_ge(L + F_P0, 8u);
      float v[1];
      sweep<1, 1>(xot + (lane & 31), 1, tag, v);
      if (lane < 32) *(LAS float*)(L + L_Q + 128 * (s ^ 1) + 4 * lane) = v[0];
      LGKM0();
      if (lane == 0) __hip_atomic_fetch_add((LAS unsigned*)(L + F_Q), 1u, RLX_WG);
      STAMP(6);
    }
  }
  __builtin_amdgcn_s_setprio(0);
  lds_wait_ge(L + F_Q, 2u);
  tag = __builtin_amdgcn_readfirstlane(lds_ld(L + F_TAG));
  const unsigned tb = __builtin_amdgcn_readfirstlane(lds_ld(L + F_T0));
  const __amdgpu_buffer_rsrc_t rpub = RSRC(ws + G_Y + (size_t)(b * 64 + h * 2 + s) * DM + 80 * hw, 80 * 8);
  {
    const u32x4 pre = __builtin_amdgcn_raw_buffer_load_b128(rpub, lane * 16, 0, 0);
    asm volatile("" ::"v"(pre));
    if (hw == ((mem + 5) & 7)) {
      const GAS unsigned short* a = lane < 32 ? vc + ((size_t)(b * NH + h) * SK + (size_t)s * (SK / 2)) * DH : out;
      const unsigned short pre2 = __builtin_nontemporal_load(a);
      asm volatile("" ::"v"(pre2));
    }
  }
#define WAIT_UNTIL(t) while (CLOCK() - tb < (t)) __builtin_amdgcn_s_sleep(4)
  const int pc0 = 80 * hw + (g < 2 ? 32 * g + c : 64 + (c & 7)), pc1 = pc0 + (g < 2 ? 16 : 8);
  float ys0 = 0.f, ys1 = 0.f;
  {
#pragma unroll
    for (int i = 0; i < 10; ++i) FRESH(xp[i]);
    if (hw == ((mem + 2) & 7)) {
      const __amdgpu_buffer_rsrc_t rp = RSRC(wo + (size_t)(h * DH + 16 * mem) * DM, 16 * DM * 2);
#pragma unroll
      for (int i = 0; i < 20; ++i) {
        VMWAIT(W_DEPTH - 1);
        wr[i] = __builtin_amdgcn_raw_buffer_load_b128(rp, lane * 16, i * 1024, 0);
        SB();
      }
#pragma unroll
      for (int i = 0; i < 20; ++i) asm volatile("" ::"v"(wr[i]));
    }
    if ((hw >> 1) != b) WAIT_UNTIL(STAG1_0 + turn * STAG1);
    const __amdgpu_buffer_rsrc_t rg = RSRC((s ? wu : wg) + (size_t)80 * hw * FF + 64 * hs, 80 * FF * 2 - 64 * hs * 2);
    const int vg = ((20 * g + (c >> 3)) * FF + 8 * (c & 7)) * 2;
#pragma unroll
    for (int i = 0; i < 10; ++i) {
      VMWAIT(W_DEPTH - 1);
      wr[i] = __builtin_amdgcn_raw_buffer_load_b128(rg, vg, i * 2 * FF * 2, 0);
      SB();
    }
    const __amdgpu_buffer_rsrc_t rd = RSRC(wd + (size_t)(64 * hs + 32 * s + 4 * hw) * DM, 4 * DM * 2);
#pragma unroll
    for (int cb = 0; cb < 5; ++cb) {
      VMWAIT(W_DEPTH - 1);
      wr[10 + cb] = __builtin_amdgcn_raw_buffer_load_b128(rd, (g * DM + 8 * c) * 2, cb * 256, 0);
      SB();
    }
    PROJ_MFMA(1)
    if (hw == 2) {
      unsigned r0, r1;
      PROJ_SUM(1, r0, r1)
      if (g == 0 && (c >> 3) != s) __builtin_amdgcn_raw_buffer_store_b128(u32x4{r0, tag, r1, tag}, RSRC(xme + 32, 128), (c & 7) * 16, 0, AUX_SC1);
      float v[2];
      sweep<2, 16>(xot + 32 + 2 * (c & 7), 1, tag, v);
      const unsigned o0 = __float_as_uint(v[0]), o1 = __float_as_uint(v[1]);
      if (g == 0 && (c >> 3) == s)
        *(LAS u32x2*)(L + L_H + 8 * (c & 7)) = u32x2{pk2(bflo(r0) * bflo(o0), bfhi(r0) * bfhi(o0)), pk2(bflo(r1) * bflo(o1), bfhi(r1) * bfhi(o1))};
      LGKM0();
      if (lane == 0) __hip_atomic_store((LAS unsigned*)(L + F_H), 1u, RLX_WG);
    }
    lds_wait_ge(L + F_H, 1u);
    const unsigned X = *(const LAS unsigned short*)(L + L_H + 2 * (4 * hw + g));
    const u32x4 a = make_a(X, sel);
#pragma unroll
    for (int cb = 0; cb < 5; ++cb) {
      const f32x4 yf = mfma(a, wr[10 + cb], f32x4{0.f, 0.f, 0.f, 0.f});
      if (g < 2) *(LAS f32x4*)(L + L_FF + (hw * DM + 128 * cb + 8 * c + 4 * g) * 4) = yf;
    }
    LGKM0();
    if (lane == 0) __hip_atomic_fetch_add((LAS unsigned*)(L + F_FF), 1u, RLX_WG);
    if (h < NSL) {
      lds_wait_ge(L + F_FF, 8u);
#pragma unroll
      for (int p = 0; p < 8; ++p) {
        ys0 += *(const LAS float*)(L + L_FF + (p * DM + pc0) * 4);
        ys1 += *(const LAS float*)(L + L_FF + (p * DM + pc1) * 4);
      }
    }
  }
  STAMP(2);
#define ML_EXCHANGE()                                                                                                       \
  {                                                                                                                         \
    lds_wait_ge(L + F_ML, 8u);                                                                                              \
    const float mv = *(const LAS float*)(L + L_ML + 4 * (lane & 7)), lv = *(const LAS float*)(L + L_ML + 32 + 4 * (lane & 7)); \
    float M = fmaxf(mv, dppf<XOR1>(mv));                                                                                    \
    M = fmaxf(M, dppf<XOR2>(M));                                                                                            \
    M = fmaxf(M, dppf<XOR7>(M));                                                                                            \
    float Ls = __builtin_amdgcn_exp2f(mv - M) * lv;                                                                         \
    FRESH(Ls);                                                                                                              \
    Ls += dppf<XOR1>(Ls);                                                                                                   \
    Ls += dppf<XOR2>(Ls);                                                                                                   \
    Ls += dppf<XOR7>(Ls);                                                                                                   \
    if (lane < 2) put(xme + 48 + lane, lane == 0 ? M : Ls, tag);                                                            \
    float o2[2];                                                                                                            \
    sweep<2, 16>(xot + 48, 1, tag, o2);                                                                                     \
    const float Mt = fmaxf(M, o2[0]);                                                                                       \
    float e0 = __builtin_amdgcn_exp2f(M - Mt) * Ls, e1 = __builtin_amdgcn_exp2f(o2[0] - Mt) * o2[1];                        \
    FRESH(e0); FRESH(e1);                                                                                                   \
    if (lane < 8) *(LAS float*)(L + L_EW + 4 * lane) = __builtin_amdgcn_exp2f(mv - Mt) / (e0 + e1);                         \
    LGKM0();                                                                                                                \
    if (lane == 0) __hip_atomic_store((LAS unsigned*)(L + F_EW), 1u, RLX_WG);                                               \
  }
#define HELPER_SYNC(n)                                                                \
  {                                                                                   \
    LGKM0();                                                                          \
    if (lane == 0) __hip_atomic_fetch_add((LAS unsigned*)(L + F_HS), 1u, RLX_WG);     \
    lds_wait_ge(L + F_HS, 8u * (n));                                                  \
  }
  if (hw == 6 && turn >= 4) ML_EXCHANGE()
  {
    WAIT_UNTIL(STAG2_0 + turn * STAG2);
    const __amdgpu_buffer_rsrc_t ro = RSRC(wo + (size_t)(h * DH + 8 * hw) * DM, 72 * DM * 2);
    const int vo = (g * DM + 8 * c) * 2;
#pragma unroll
    for (int i = 0; i < 20; ++i) {
      VMWAIT(W_DEPTH - 1);
      wr[i] = __builtin_amdgcn_raw_buffer_load_b128(ro, vo, ((64 * (i / 10) + 4 * ((i / 5) & 1)) * DM + 128 * (i % 5)) * 2, 0);
      SB();
    }
    STAMP(7);
    const int a_w = L_ST + (8 * hw + g) * ST_LD + 16 * c;
    const int a_r = L_ST + (8 * g + (c >> 2)) * ST_LD + (80 * hw + 4 * (c & 3)) * 2;
#pragma unroll
    for (int hf = 0; hf < 2; ++hf) {
#pragma unroll
      for (int i = 0; i < 10; ++i) *(LAS u32x4*)(L + a_w + (i / 5) * 4 * ST_LD + (i % 5) * 256) = wr[10 * hf + i];
      HELPER_SYNC(2 * hf + 1)
#pragma unroll
      for (int i = 0; i < 10; ++i) {
        const u32x2 r0 = __builtin_bit_cast(u32x2, __builtin_amdgcn_ds_read_tr16_b64_v4i16((LAS s16x4*)(L + a_r + (i / 5) * 32 * ST_LD + (i % 5) * 32)));
        const u32x2 r1 = __builtin_bit_cast(u32x2, __builtin_amdgcn_ds_read_tr16_b64_v4i16((LAS s16x4*)(L + a_r + ((i / 5) * 32 + 4) * ST_LD + (i % 5) * 32)));
        wr[10 * hf + i] = u32x4{r0[0], r0[1], r1[0], r1[1]};
      }
      if (hf == 0) HELPER_SYNC(2)
    }
  }
  if (hw == 6 && turn < 4) ML_EXCHANGE()
  if (hw == 7 && bid == 0) {
    float f4[4];
    sweep<4, 64>(ws + G_X + lane * 64 + 48, 64 * 64, tag, f4);
    if (lane == 0) __hip_atomic_store((GAS unsigned*)(ws + G_E), tag, RLX_AGENT);
  }
  int a_o = L_O + c * O_LD + 16 * g, v_st = lane * 16;
  FRESH(a_o); FRESH(v_st);
  STAMP(5);
  WG_BARRIER();
  STAMP(3);
  {
    u32x4 a[4];
#pragma unroll
    for (int u = 0; u < 4; ++u) a[u] = *(const LAS u32x4*)(L + a_o + 64 * u);
    float z[5];
#pragma unroll
    for (int i = 0; i < 5; ++i) {
      f32x4 y = {0.f, 0.f, 0.f, 0.f};
#pragma unroll
      for (int u = 0; u < 4; ++u) y = mfma(a[u], wr[5 * u + i], y);
      z[i] = (y[0] + y[1]) + (y[2] + y[3]);
    }
    const float e = swap16_sum(swap32_sum(z[0], z[4]), swap32_sum(z[2], z[4]));
    float o = swap16_sum(swap32_sum(z[1], z[4]), swap32_sum(z[3], z[4]));
    const float o8 = dppf<XOR8>(o);
    o = g < 2 ? o : o8;
    __builtin_amdgcn_raw_buffer_store_b128(u32x4{__float_as_uint(e + ys0), tag, __float_as_uint(o + ys1), tag}, rpub, v_st, 0, AUX_SC1);
  }
  STAMP(4);
  STAMP_OUT();
}

extern "C" void kernel_launch(void* const* d_in, const int* in_sizes, int n_in, void* d_out, int out_size, void* d_ws,
                              size_t ws_size, hipStream_t stream) {
  (void)in_sizes; (void)n_in; (void)out_size; (void)ws_size;
  typedef const GAS unsigned short* cp;
  k_main<<<NWG, 1024, 0, stream>>>((cp)d_in[0], (cp)d_in[1], (cp)d_in[2], (cp)d_in[3], (cp)d_in[4], (cp)d_in[6], (cp)d_in[7],
                                   (cp)d_in[8], (GAS u64*)d_ws, (GAS unsigned short*)d_out);
}
```

```cpp
#define FIXSEL (bid == 0)
#include <hip/hip_runtime.h>
#include <hip/hip_bf16.h>
#include <stdint.h>

#define DEPTH 4
#define DEPTH_LO 3
#define DEPTH_HI 5
#define LAG1 70
#define W_DEPTH 6
#define XORG 105
#define PACE0 450
#define PACE_A 128
#define PACE 102
#define STAG1_0 950
#define STAG1 125
#define JUMP1 140
#define STAG2_0 1950
#define STAG2 205
#define JUMP2 240
#define TAILFREE 8
#define YDELAY 22
#define YPOLL_N 8
#define YPOLL_S 8
#define YPOLL_S0 1
#define RMODE 0
#define RWAVES 1
#define RSTAG 8
constexpr int AUX_NT = 2, AUX_SC1 = 16;

typedef unsigned u32x4 __attribute__((ext_vector_type(4)));
typedef unsigned u32x2 __attribute__((ext_vector_type(2)));
typedef float f32x4 __attribute__((ext_vector_type(4)));
typedef float f32x2 __attribute__((ext_vector_type(2)));
typedef short bf16x8 __attribute__((ext_vector_type(8)));
typedef __bf16 bf16x2_t __attribute__((ext_vector_type(2)));
typedef short s16x4 __attribute__((ext_vector_type(4)));
typedef unsigned long long u64;
#define GAS __attribute__((address_space(1)))
#define LAS __attribute__((address_space(3)))
#define RLX_AGENT __ATOMIC_RELAXED, __HIP_MEMORY_SCOPE_AGENT
#define RLX_WG __ATOMIC_RELAXED, __HIP_MEMORY_SCOPE_WORKGROUP

constexpr int NB = 4, DM = 640, NH = 32, SK = 4096, DH = 128, FF = 1728;
constexpr int NSL = FF / 64;
constexpr int NWG = 256;
constexpr float LOG2E = 1.4426950408889634f;
constexpr float QSCALE = 0.08838834764831845f;
constexpr unsigned SPIN_CAP = 1u << 18;

constexpr size_t G_E = 0;
constexpr size_t G_X = 32;
constexpr size_t G_Y = G_X + (size_t)NWG * 64;

constexpr int L_XQ = 0;
constexpr int L_Q = L_XQ + DM * 2;
constexpr int L_P = L_Q + 256;
constexpr int O_LD = 272;
constexpr int L_O = L_P + 2 * 16 * 64 * 4;
constexpr int L_ML = L_O + 16 * O_LD;
constexpr int L_EW = L_ML + 64;
constexpr int L_FLAG = L_EW + 32;
constexpr int F_Q = L_FLAG, F_ML = L_FLAG + 4, F_H = L_FLAG + 8, F_P0 = L_FLAG + 12, F_P1 = L_FLAG + 16, F_FF = L_FLAG + 20,
              F_RED = L_FLAG + 24, F_TAG = L_FLAG + 28, F_T0 = L_FLAG + 32, F_EW = L_FLAG + 36, F_HS = L_FLAG + 40;
constexpr int L_H = L_FLAG + 64;
constexpr int L_RED = L_H + 64;
constexpr int L_FF = L_RED + 512;
constexpr int ST_LD = DM * 2 + 16;
constexpr int L_ST = L_FF + 8 * DM * 4;
constexpr int L_END = L_ST + 64 * ST_LD;

__device__ __forceinline__ float bflo(unsigned u) { return __builtin_bit_cast(float, u << 16); }
__device__ __forceinline__ float bfhi(unsigned u) { return __builtin_bit_cast(float, u & 0xffff0000u); }
__device__ __forceinline__ unsigned pk2(float lo, float hi) {
  bf16x2_t r;
  r[0] = (__bf16)lo;
  r[1] = (__bf16)hi;
  return __builtin_bit_cast(unsigned, r);
}
__device__ __forceinline__ float rbf(float x) { return (float)(__bf16)x; }
__device__ __forceinline__ float dot2(unsigned a, unsigned b, float c) {
  return __builtin_amdgcn_fdot2_f32_bf16(__builtin_bit_cast(bf16x2_t, a), __builtin_bit_cast(bf16x2_t, b), c, false);
}
template <int CTRL> __device__ __forceinline__ float dppf(float x) {
  return __builtin_bit_cast(float, __builtin_amdgcn_mov_dpp(__builtin_bit_cast(int, x), CTRL, 0xf, 0xf, true));
}
template <int CTRL> __device__ __forceinline__ unsigned dppu(unsigned x) {
  return (unsigned)__builtin_amdgcn_mov_dpp((int)x, CTRL, 0xf, 0xf, true);
}
constexpr int XOR1 = 0xB1, XOR2 = 0x4E, XOR7 = 0x141, XOR8 = 0x128;
template <int CTRL> __device__ __forceinline__ float tr_merge(float lo, float hi, bool up) {
  const float t1 = lo + dppf<CTRL>(lo);
  const float t2 = hi + dppf<CTRL>(hi);
  return up ? t2 : t1;
}
__device__ __forceinline__ float row16_sum(float x) {
  x += dppf<XOR1>(x); x += dppf<XOR2>(x); x += dppf<XOR7>(x); x += dppf<XOR8>(x);
  return x;
}
__device__ __forceinline__ float row16_max(float x) {
  x = fmaxf(x, dppf<XOR1>(x)); x = fmaxf(x, dppf<XOR2>(x)); x = fmaxf(x, dppf<XOR7>(x)); x = fmaxf(x, dppf<XOR8>(x));
  return x;
}
__device__ __forceinline__ float half_sum(float x) {
  auto t = __builtin_amdgcn_permlane32_swap(__float_as_uint(x), __float_as_uint(x), false, false);
  return __uint_as_float(t[0]) + __uint_as_float(t[1]);
}
__device__ __forceinline__ float xrow_max(float x) {
  auto s = __builtin_amdgcn_permlane16_swap(__float_as_uint(x), __float_as_uint(x), false, false);
  x = fmaxf(__uint_as_float(s[0]), __uint_as_float(s[1]));
  auto t = __builtin_amdgcn_permlane32_swap(__float_as_uint(x), __float_as_uint(x), false, false);
  return fmaxf(__uint_as_float(t[0]), __uint_as_float(t[1]));
}
__device__ __forceinline__ float xrow_sum(float x) {
  auto s = __builtin_amdgcn_permlane16_swap(__float_as_uint(x), __float_as_uint(x), false, false);
  x = __uint_as_float(s[0]) + __uint_as_float(s[1]);
  return half_sum(x);
}
__device__ __forceinline__ float swap32_sum(float a, float b) {
  auto t = __builtin_amdgcn_permlane32_swap(__float_as_uint(a), __float_as_uint(b), false, false);
  return __uint_as_float(t[0]) + __uint_as_float(t[1]);
}
__device__ __forceinline__ float swap16_sum(float a, float b) {
  auto t = __builtin_amdgcn_permlane16_swap(__float_as_uint(a), __float_as_uint(b), false, false);
  return __uint_as_float(t[0]) + __uint_as_float(t[1]);
}
__device__ __forceinline__ f32x4 mfma(u32x4 a, u32x4 b, f32x4 c) {
  return __builtin_amdgcn_mfma_f32_16x16x32_bf16(__builtin_bit_cast(bf16x8, a), __builtin_bit_cast(bf16x8, b), c, 0, 0, 0);
}
#define LGKM0() asm volatile("s_waitcnt lgkmcnt(0)" ::: "memory")
#define WG_BARRIER()                \
  do {                              \
    LGKM0();                        \
    __builtin_amdgcn_s_barrier();   \
    asm volatile("" ::: "memory");  \
  } while (0)
#define SB() __builtin_amdgcn_sched_barrier(0)
#define STAMP(k)
#define STAMP_DECL
#define STAMP_OUT()
#define RSRC(p, n) __builtin_amdgcn_make_buffer_rsrc((void*)(p), 0, (n), 0x00020000)
#define FRESH(v) asm volatile("" : "+v"(v))
#define VMWAIT_UNLESS(n, v, k, m) \
  asm volatile("s_waitcnt vmcnt(%3)\n\ts_cmp_ge_u32 %1, %2\n\ts_cbranch_scc1 1f\n\ts_waitcnt vmcnt(%0)\n1:" ::"i"(n), "s"(v), "i"(k), "i"(m) : "memory", "scc")
#define VMWAIT(n) asm volatile("s_waitcnt vmcnt(%0)" ::"i"(n) : "memory")

__device__ __forceinline__ void put(GAS u64* p, float v, unsigned tag) {
  __hip_atomic_store(p, ((u64)tag << 32) | (u64)__float_as_uint(v), RLX_AGENT);
}
__device__ __forceinline__ u64 get(const GAS u64* p) { return __hip_atomic_load(p, RLX_AGENT); }
__device__ __forceinline__ unsigned lds_ld(LAS unsigned char* p) { return __hip_atomic_load((LAS unsigned*)p, RLX_WG); }
__device__ __forceinline__ void lds_wait_ge(LAS unsigned char* p, unsigned want) {
  while (lds_ld(p) < want) __builtin_amdgcn_s_sleep(1);
  asm volatile("" ::: "memory");
}
template <int N, int SLP> __device__ __forceinline__ void sweep(const GAS u64* p, int S, unsigned tag, float (&v)[N]) {
  for (unsigned spins = 0;; ++spins) {
    bool ok = true;
#pragma unroll
    for (int k = 0; k < N; ++k) {
      const u64 x = get(p + k * S);
      v[k] = __uint_as_float((unsigned)x);
      ok &= (unsigned)(x >> 32) == tag;
    }
    if (__all(ok) || spins > SPIN_CAP) return;
    __builtin_amdgcn_s_sleep(SLP);
  }
}

struct Sel { unsigned s[4]; };
__device__ __forceinline__ Sel make_sel(int lane) {
  const int m = lane & 15;
  const unsigned src = (m & 8) ? 0x0706u : 0x0504u;
  const unsigned placed = (m & 1) ? ((src << 16) | 0x0c0cu) : (0x0c0c0000u | src);
  Sel r;
#pragma unroll
  for (int jj = 0; jj < 4; ++jj) r.s[jj] = (((m & 7) >> 1) == jj) ? placed : 0x0c0c0c0cu;
  return r;
}
__device__ __forceinline__ u32x4 make_a(unsigned X, const Sel& s) {
  u32x4 a;
#pragma unroll
  for (int jj = 0; jj < 4; ++jj) a[jj] = __builtin_amdgcn_perm(X, 0u, s.s[jj]);
  return a;
}
__device__ __forceinline__ unsigned split_hi_lo(float v) {
  const float h = rbf(v);
  return pk2(h, v - h);
}

__device__ __forceinline__ void ln_row(const unsigned (&xr)[5], LAS unsigned char* dst, int lane) {
  float sum = 0.f;
#pragma unroll
  for (int k = 0; k < 5; ++k) sum += bflo(xr[k]) + bfhi(xr[k]);
  const float mean = xrow_sum(row16_sum(sum)) * (1.0f / DM);
  float ss = 0.f;
#pragma unroll
  for (int k = 0; k < 5; ++k) {
    const float a = bflo(xr[k]) - mean, bb = bfhi(xr[k]) - mean;
    ss = fmaf(a, a, ss);
    ss = fmaf(bb, bb, ss);
  }
  const float var = xrow_sum(row16_sum(ss)) * (1.0f / DM);
  const float mu = rbf(mean);
  const float rinv = 1.0f / rbf(sqrtf(rbf(rbf(var) + 1e-5f)));
#pragma unroll
  for (int k = 0; k < 5; ++k)
    *(LAS unsigned*)(dst + 4 * (lane + 64 * k)) = pk2(rbf(bflo(xr[k]) - mu) * rinv, rbf(bfhi(xr[k]) - mu) * rinv);
}

__global__ void __launch_bounds__(1024)
k_main(const GAS unsigned short* __restrict__ x, const GAS unsigned short* __restrict__ kc, const GAS unsigned short* __restrict__ vc,
       const GAS unsigned short* __restrict__ wq, const GAS unsigned short* __restrict__ wo, const GAS unsigned short* __restrict__ wg,
       const GAS unsigned short* __restrict__ wu, const GAS unsigned short* __restrict__ wd, GAS u64* ws, GAS unsigned short* out) {
  __shared__ __attribute__((aligned(16))) unsigned char smem[L_END];
  LAS unsigned char* L = (LAS unsigned char*)smem;
  const int tid = threadIdx.x, lane = tid & 63, wave = __builtin_amdgcn_readfirstlane(tid >> 6);
  const int g = lane >> 4, c = lane & 15;
  const int bid = blockIdx.x;
  const int jx = bid >> 3;
  const int h = (jx >> 3) * 8 + (bid & 7), mem = jx & 7, b = mem >> 1, s = mem & 1;
  const int slot = (b * NH + h) * 2 + s;
  const int hs = h < NSL ? h : h - 8;
  const unsigned turn = (mem + 2 * (jx >> 3)) & 7;
  const unsigned t_org = (unsigned)__builtin_amdgcn_s_memrealtime();
  SB();
  STAMP_DECL;
  STAMP(0);
  if (tid < 16) *(LAS unsigned*)(L + L_FLAG + 4 * tid) = 0u;
  asm volatile("" ::"s"(x), "s"(kc), "s"(vc), "s"(wq), "s"(wo), "s"(wg), "s"(wu), "s"(wd), "s"(ws), "s"(out));
  WG_BARRIER();
  const Sel sel = make_sel(lane);
  GAS u64* const xme = ws + G_X + (size_t)slot * 64;
  const GAS u64* const xot = ws + G_X + (size_t)(slot ^ 1) * 64;
#define CLOCK() ((unsigned)__builtin_amdgcn_s_memrealtime())

  if (wave >= 8) {
    const int w = wave - 8;
    const size_t base = ((size_t)(b * NH + h) * SK + (size_t)s * (SK / 2)) * DH;
    const GAS unsigned short* kb = kc + base;
    const GAS unsigned short* vb = vc + base;
    int voff = lane * 16 + w * 4096;
#define TILE_LD(r, so, t) __builtin_amdgcn_raw_buffer_load_b128((r), voff + ((t) & 3) * 1024, (so) + ((t) >> 2) * 32768, AUX_NT)
    u32x4 ring[16];
    {
      const __amdgpu_buffer_rsrc_t rk = RSRC(kb, SK / 2 * DH * 2);
#pragma unroll
      for (int t = 0; t < 16; ++t) {
        if (t >= DEPTH) VMWAIT(DEPTH - 1);
        ring[t] = TILE_LD(rk, 0, t);
        SB();
      }
    }
    STAMP(6);
    lds_wait_ge(L + F_Q, 2u);
    STAMP(1);
    const u32x4 q = *(const LAS u32x4*)(L + L_Q + 16 * c);
    const unsigned tb = __builtin_amdgcn_readfirstlane(lds_ld(L + F_T0));
    unsigned pstep = PACE_A, lag = 0;
    unsigned tgt = PACE0 + w * (PACE / 8), j1 = STAG1_0 + turn * STAG1, j2 = STAG2_0 + turn * STAG2;
#define PACED_WAIT(t)                                                         \
  if (((t) & 3) == 0) {                                                       \
    tgt += ((t) > 16 - TAILFREE) ? ptail : pstep;                             \
    if (tgt >= j1) { tgt += JUMP1; j1 = ~0u; }                                \
    if (tgt >= j2) { tgt += JUMP2; j2 = ~0u; }                                \
    unsigned now;                                                             \
    while ((now = CLOCK() - tb) < tgt && now < (1u << 20)) __builtin_amdgcn_s_sleep(1); \
    lag = now - tgt;                                                          \
  }                                                                           \
  if ((t) == 16 - TAILFREE) lag |= dtail;                                     \
  VMWAIT_UNLESS(DEPTH_LO - 1, lag, LAG1, DEPTH_HI - 1);
    float sc0 = 0.f, sc1 = 0.f, sc2 = 0.f, sc3 = 0.f;
#define MERGE(LO, HI, CTRL, BIT) tr_merge<CTRL>((LO), (HI), (c & (BIT)) != 0)
#pragma unroll 1
    for (int grp = 0; grp < 4; ++grp) {
      const __amdgpu_buffer_rsrc_t rn = RSRC(grp < 3 ? kb : vb, SK / 2 * DH * 2);
      const int so = ((grp + 1) & 3) * 131072;
      const unsigned ptail = pstep, dtail = 0;
      FRESH(voff);
      float d1 = 0.f, d2 = 0.f, d4 = 0.f, d8 = 0.f;
#pragma unroll
      for (int t = 0; t < 16; ++t) {
        float a = dot2(ring[t][0], q[0], 0.f);
        a = dot2(ring[t][1], q[1], a);
        a = dot2(ring[t][2], q[2], a);
        a = dot2(ring[t][3], q[3], a);
        FRESH(a);
        SB();
        PACED_WAIT(t)
        ring[t] = TILE_LD(rn, so, t);
        if (!(t & 1)) d1 = a;
        else {
          a = MERGE(d1, a, XOR8, 8);
          if (!(t & 2)) d2 = a;
          else {
            a = MERGE(d2, a, XOR7, 4);
            if (!(t & 4)) d4 = a;
            else {
              a = MERGE(d4, a, XOR2, 2);
              if (!(t & 8)) d8 = a;
              else {
                a = MERGE(d8, a, XOR1, 1);
                sc0 = sc1; sc1 = sc2; sc2 = sc3;
                sc3 = rbf(rbf(a) * QSCALE) * LOG2E;
              }
            }
          }
        }
        SB();
      }
      pstep = PACE;
    }
#undef MERGE
    STAMP(2);
    float mw = fmaxf(fmaxf(sc0, sc1), fmaxf(sc2, sc3));
    mw = xrow_max(row16_max(mw));
    const float p0 = __builtin_amdgcn_exp2f(sc0 - mw), p1 = __builtin_amdgcn_exp2f(sc1 - mw);
    const float p2 = __builtin_amdgcn_exp2f(sc2 - mw), p3 = __builtin_amdgcn_exp2f(sc3 - mw);
    unsigned pk0 = split_hi_lo(p0), pk1 = split_hi_lo(p1), pk2_ = split_hi_lo(p2), pk3 = split_hi_lo(p3);
    const float lw = xrow_sum(row16_sum((p0 + p1) + (p2 + p3)));
    if (lane == 0) {
      *(LAS float*)(L + L_ML + w * 4) = mw;
      *(LAS float*)(L + L_ML + 32 + w * 4) = lw;
      LGKM0();
      __hip_atomic_fetch_add((LAS unsigned*)(L + F_ML), 1u, RLX_WG);
    }
    SB();
    f32x4 acc = {0.f, 0.f, 0.f, 0.f};
#define BREV4(T) ((((T) & 1) << 3) | (((T) & 2) << 1) | (((T) & 4) >> 1) | (((T) & 8) >> 3))
#define PV_STEP(T, REFILL)                              \
  {                                                     \
    const unsigned X = dppu<0x150 + BREV4(T)>(pk0);     \
    acc = mfma(make_a(X, sel), ring[T], acc);           \
    if (REFILL) {                                       \
      SB();                                             \
      PACED_WAIT(T)                                     \
      ring[T] = TILE_LD(rv, so, T);                     \
    }                                                   \
    SB();                                               \
  }
#define PV_GROUP(R)                                                                                               \
  PV_STEP(0, R) PV_STEP(1, R) PV_STEP(2, R) PV_STEP(3, R) PV_STEP(4, R) PV_STEP(5, R) PV_STEP(6, R) PV_STEP(7, R) \
  PV_STEP(8, R) PV_STEP(9, R) PV_STEP(10, R) PV_STEP(11, R) PV_STEP(12, R) PV_STEP(13, R) PV_STEP(14, R) PV_STEP(15, R)
    const __amdgpu_buffer_rsrc_t rv = RSRC(vb, SK / 2 * DH * 2);
#pragma unroll 1
    for (int grp = 0; grp < 3; ++grp) {
      const int so = (grp + 1) * 131072;
      const unsigned ptail = grp == 2 ? 0u : pstep, dtail = (unsigned)(grp >> 1) << 20;
      FRESH(voff);
      PV_GROUP(true)
      pk0 = pk1; pk1 = pk2_; pk2_ = pk3;
    }
    lds_wait_ge(L + F_EW, 1u);
    const float ew = *(const LAS float*)(L + L_EW + 4 * w);
    int a_o = L_O + (w * 2 + (g >> 1)) * O_LD + (8 * c + 4 * (g & 1)) * 2;
    FRESH(a_o);
    SB();
    {
      const int so = 0;
      const unsigned ptail = 0, dtail = 0;
      PV_GROUP(false)
    }
#undef PV_GROUP
#undef PV_STEP
    STAMP(3);
    *(LAS u32x2*)(L + a_o) = u32x2{pk2(acc[0] * ew, acc[1] * ew), pk2(acc[2] * ew, acc[3] * ew)};
    WG_BARRIER();
    const int j = h * 2 + s;
    if (j >= 40) { STAMP_OUT(); return; }
    const int r5 = j % 5, k8 = lane & 7;
    const int col0 = b * DM + 80 * (j / 5) + (r5 < 4 ? 32 * (r5 >> 1) + 8 * (r5 & 1) : 64) + k8, col1 = col0 + (r5 < 4 ? 16 : 8);
    const unsigned x0 = x[col0], x1 = x[col1];
    const unsigned tag = lds_ld(L + F_TAG);
    STAMP(4);
    const __amdgpu_buffer_rsrc_t ry = RSRC(ws + G_Y + (size_t)(b * 64 + 8 * w) * DM + 16 * j, 8 * DM * 8);
    u32x4 yv = {0u, 0u, 0u, 0u};
    bool miss = true;
    __builtin_amdgcn_s_sleep(YDELAY);
    for (unsigned spins = 0; spins < SPIN_CAP; ++spins) {
      if (miss) {
        yv = __builtin_amdgcn_raw_buffer_load_b128(ry, (lane >> 3) * (DM * 8) + k8 * 16, 0, AUX_SC1);
        miss = yv[1] != tag || yv[3] != tag;
      }
      const int nmiss = __builtin_popcountll(__builtin_amdgcn_ballot_w64(miss));
      if (nmiss == 0) break;
      if (nmiss > YPOLL_N) __builtin_amdgcn_s_sleep(YPOLL_S); else __builtin_amdgcn_s_sleep(YPOLL_S0);
    }
    float s0 = __uint_as_float(yv[0]), s1 = __uint_as_float(yv[2]);
    s0 += dppf<XOR8>(s0); s1 += dppf<XOR8>(s1);
    s0 = xrow_sum(s0); s1 = xrow_sum(s1);
    if (lane < 8) *(LAS f32x2*)(L + L_RED + (w * 8 + lane) * 8) = f32x2{s0, s1};
    LGKM0();
    unsigned old = 0;
    if (lane == 0) old = __hip_atomic_fetch_add((LAS unsigned*)(L + F_RED), 1u, RLX_WG);
    asm volatile("" ::: "memory");
    if (__builtin_amdgcn_readfirstlane(old) == 7u) {
      const f32x2 t = *(const LAS f32x2*)(L + L_RED + lane * 8);
      s0 = t[0]; s1 = t[1];
      s0 += dppf<XOR8>(s0); s1 += dppf<XOR8>(s1);
      s0 = xrow_sum(s0); s1 = xrow_sum(s1);
      const unsigned r = pk2(bflo(x0) + s0, bflo(x1) + s1);
      if (lane < 8) {
        out[col0] = (unsigned short)r;
        out[col1] = (unsigned short)(r >> 16);
      }
    }
    STAMP(5);
    STAMP_OUT();
    return;
  }

  const int hw = wave;
  __builtin_amdgcn_s_setprio(3);
  u32x4 wr[20];
  unsigned xp[10];
#define PROJ_MFMA(PH)                                                                                              \
  {                                                                                                                \
    f32x4 ga = {0.f, 0.f, 0.f, 0.f};                                                                               \
    _Pragma("unroll") for (int i = 0; i < 10; ++i) ga = mfma(make_a(xp[i], sel), wr[i], ga);                       \
    if ((g >> 1) == (c >> 3))                                                                                      \
      *(LAS f32x4*)(L + L_P + ((PH) * 1024 + (hw * 2 + (g >> 1)) * 64 + 8 * (c & 7) + 4 * (g & 1)) * 4) = ga;      \
    LGKM0();                                                                                                       \
    if (lane == 0) __hip_atomic_fetch_add((LAS unsigned*)(L + ((PH) ? F_P1 : F_P0)), 1u, RLX_WG);                   \
  }
#define PROJ_SUM(PH, R0, R1)                                                                                       \
  {                                                                                                                \
    lds_wait_ge(L + ((PH) ? F_P1 : F_P0), 8u);                                                                     \
    f32x4 t4 = {0.f, 0.f, 0.f, 0.f};                                                                               \
    _Pragma("unroll") for (int k = 0; k < 4; ++k) t4 += *(const LAS f32x4*)(L + L_P + ((PH) * 1024 + (4 * g + k) * 64 + 4 * c) * 4); \
    _Pragma("unroll") for (int k = 0; k < 4; ++k) t4[k] = xrow_sum(t4[k]);                                         \
    R0 = pk2(t4[0], t4[1]);                                                                                        \
    R1 = pk2(t4[2], t4[3]);                                                                                        \
  }
  unsigned tag = 0;
  {
    unsigned xr[5];
#pragma unroll
    for (int k = 0; k < 5; ++k) xr[k] = ((const GAS unsigned*)(x + b * DM))[lane + 64 * k];
    unsigned e = 0;
    if (hw < 2) e = *(const GAS unsigned*)(ws + G_E);
    const __amdgpu_buffer_rsrc_t rq = RSRC(wq + (size_t)80 * hw * (NH * DH) + h * DH + 64 * s, 80 * NH * DH * 2 - (h * DH + 64 * s) * 2);
    const int vq = ((20 * g + (c >> 3)) * NH * DH + 8 * (c & 7)) * 2;
#pragma unroll
    for (int i = 0; i < 10; ++i) {
      wr[i] = __builtin_amdgcn_raw_buffer_load_b128(rq, vq, i * 2 * NH * DH * 2, 0);
      SB();
    }
    ln_row(xr, L + L_XQ, lane);
    SB();
    if (hw == 0) {
      const unsigned t1 = CLOCK() - XORG;
      if (lane == 0) *(LAS unsigned*)(L + F_T0) = t1;
    }
    LGKM0();
#pragma unroll
    for (int k = 0; k < 5; ++k) {
      const u32x2 t = *(const LAS u32x2*)(L + L_XQ + (80 * hw + 20 * g + 4 * k) * 2);
      xp[2 * k] = t[0];
      xp[2 * k + 1] = t[1];
    }
    PROJ_MFMA(0)
    STAMP(1);
    if (hw < 2) tag = __builtin_amdgcn_readfirstlane(e) + 1u;
    if (hw == 0) {
      unsigned r0, r1;
      PROJ_SUM(0, r0, r1)
      if (g == 0) {
        __builtin_amdgcn_raw_buffer_store_b128(u32x4{r0, tag, r1, tag}, RSRC(xme, 512), c * 16, 0, AUX_SC1);
        *(LAS u32x2*)(L + L_Q + 128 * s + 8 * c) = u32x2{r0, r1};
      }
      if (lane == 0) *(LAS unsigned*)(L + F_TAG) = tag;
      LGKM0();
      if (lane == 0) __hip_atomic_fetch_add((LAS unsigned*)(L + F_Q), 1u, RLX_WG);
      STAMP(6);
    } else if (hw == 1) {
      lds_wait_ge(L + F_P0, 8u);
      float v[1];
      sweep<1, 1>(xot + (lane & 31), 1, tag, v);
      if (lane < 32) *(LAS float*)(L + L_Q + 128 * (s ^ 1) + 4 * lane) = v[0];
      LGKM0();
      if (lane == 0) __hip_atomic_fetch_add((LAS unsigned*)(L + F_Q), 1u, RLX_WG);
      STAMP(6);
    }
  }
  __builtin_amdgcn_s_setprio(0);
  lds_wait_ge(L + F_Q, 2u);
  tag = __builtin_amdgcn_readfirstlane(lds_ld(L + F_TAG));
  const unsigned tb = __builtin_amdgcn_readfirstlane(lds_ld(L + F_T0));
  const __amdgpu_buffer_rsrc_t rpub = RSRC(ws + G_Y + (size_t)(b * 64 + h * 2 + s) * DM + 80 * hw, 80 * 8);
  {
    const u32x4 pre = __builtin_amdgcn_raw_buffer_load_b128(rpub, lane * 16, 0, 0);
    asm volatile("" ::"v"(pre));
    if (hw == ((mem + 5) & 7)) {
      const GAS unsigned short* a = lane < 32 ? vc + ((size_t)(b * NH + h) * SK + (size_t)s * (SK / 2)) * DH : out;
      const unsigned short pre2 = __builtin_nontemporal_load(a);
      asm volatile("" ::"v"(pre2));
    }
  }
#define WAIT_UNTIL(t) while (CLOCK() - tb < (t)) __builtin_amdgcn_s_sleep(4)
  const int pc0 = 80 * hw + (g < 2 ? 32 * g + c : 64 + (c & 7)), pc1 = pc0 + (g < 2 ? 16 : 8);
  float ys0 = 0.f, ys1 = 0.f;
  {
#pragma unroll
    for (int i = 0; i < 10; ++i) FRESH(xp[i]);
    if (hw == ((mem + 2) & 7)) {
      const __amdgpu_buffer_rsrc_t rp = RSRC(wo + (size_t)(h * DH + 16 * mem) * DM, 16 * DM * 2);
#pragma unroll
      for (int i = 0; i < 20; ++i) {
        VMWAIT(W_DEPTH - 1);
        wr[i] = __builtin_amdgcn_raw_buffer_load_b128(rp, lane * 16, i * 1024, 0);
        SB();
      }
#pragma unroll
      for (int i = 0; i < 20; ++i) asm volatile("" ::"v"(wr[i]));
    }
    if ((hw >> 1) != b) WAIT_UNTIL(STAG1_0 + turn * STAG1);
    const __amdgpu_buffer_rsrc_t rg = RSRC((s ? wu : wg) + (size_t)80 * hw * FF + 64 * hs, 80 * FF * 2 - 64 * hs * 2);
    const int vg = ((20 * g + (c >> 3)) * FF + 8 * (c & 7)) * 2;
#pragma unroll
    for (int i = 0; i < 10; ++i) {
      VMWAIT(W_DEPTH - 1);
      wr[i] = __builtin_amdgcn_raw_buffer_load_b128(rg, vg, i * 2 * FF * 2, 0);
      SB();
    }
    const __amdgpu_buffer_rsrc_t rd = RSRC(wd + (size_t)(64 * hs + 32 * s + 4 * hw) * DM, 4 * DM * 2);
#pragma unroll
    for (int cb = 0; cb < 5; ++cb) {
      VMWAIT(W_DEPTH - 1);
      wr[10 + cb] = __builtin_amdgcn_raw_buffer_load_b128(rd, (g * DM + 8 * c) * 2, cb * 256, 0);
      SB();
    }
    PROJ_MFMA(1)
    if (hw == 2) {
      unsigned r0, r1;
      PROJ_SUM(1, r0, r1)
      if (g == 0 && (c >> 3) != s) __builtin_amdgcn_raw_buffer_store_b128(u32x4{r0, tag, r1, tag}, RSRC(xme + 32, 128), (c & 7) * 16, 0, AUX_SC1);
      float v[2];
      sweep<2, 16>(xot + 32 + 2 * (c & 7), 1, tag, v);
      const unsigned o0 = __float_as_uint(v[0]), o1 = __float_as_uint(v[1]);
      if (g == 0 && (c >> 3) == s)
        *(LAS u32x2*)(L + L_H + 8 * (c & 7)) = u32x2{pk2(bflo(r0) * bflo(o0), bfhi(r0) * bfhi(o0)), pk2(bflo(r1) * bflo(o1), bfhi(r1) * bfhi(o1))};
      LGKM0();
      if (lane == 0) __hip_atomic_store((LAS unsigned*)(L + F_H), 1u, RLX_WG);
    }
    lds_wait_ge(L + F_H, 1u);
    const unsigned X = *(const LAS unsigned short*)(L + L_H + 2 * (4 * hw + g));
    const u32x4 a = make_a(X, sel);
#pragma unroll
    for (int cb = 0; cb < 5; ++cb) {
      const f32x4 yf = mfma(a, wr[10 + cb], f32x4{0.f, 0.f, 0.f, 0.f});
      if (g < 2) *(LAS f32x4*)(L + L_FF + (hw * DM + 128 * cb + 8 * c + 4 * g) * 4) = yf;
    }
    LGKM0();
    if (lane == 0) __hip_atomic_fetch_add((LAS unsigned*)(L + F_FF), 1u, RLX_WG);
    if (h < NSL) {
      lds_wait_ge(L + F_FF, 8u);
#pragma unroll
      for (int p = 0; p < 8; ++p) {
        ys0 += *(const LAS float*)(L + L_FF + (p * DM + pc0) * 4);
        ys1 += *(const LAS float*)(L + L_FF + (p * DM + pc1) * 4);
      }
    }
  }
  STAMP(2);
#define ML_EXCHANGE()                                                                                                       \
  {                                                                                                                         \
    lds_wait_ge(L + F_ML, 8u);                                                                                              \
    const float mv = *(const LAS float*)(L + L_ML + 4 * (lane & 7)), lv = *(const LAS float*)(L + L_ML + 32 + 4 * (lane & 7)); \
    float M = fmaxf(mv, dppf<XOR1>(mv));                                                                                    \
    M = fmaxf(M, dppf<XOR2>(M));                                                                                            \
    M = fmaxf(M, dppf<XOR7>(M));                                                                                            \
    float Ls = __builtin_amdgcn_exp2f(mv - M) * lv;                                                                         \
    FRESH(Ls);                                                                                                              \
    Ls += dppf<XOR1>(Ls);                                                                                                   \
    Ls += dppf<XOR2>(Ls);                                                                                                   \
    Ls += dppf<XOR7>(Ls);                                                                                                   \
    if (lane < 2) put(xme + 48 + lane, lane == 0 ? M : Ls, tag);                                                            \
    float o2[2];                                                                                                            \
    sweep<2, 16>(xot + 48, 1, tag, o2);                                                                                     \
    const float Mt = fmaxf(M, o2[0]);                                                                                       \
    float e0 = __builtin_amdgcn_exp2f(M - Mt) * Ls, e1 = __builtin_amdgcn_exp2f(o2[0] - Mt) * o2[1];                        \
    FRESH(e0); FRESH(e1);                                                                                                   \
    if (lane < 8) *(LAS float*)(L + L_EW + 4 * lane) = __builtin_amdgcn_exp2f(mv - Mt) / (e0 + e1);                         \
    LGKM0();                                                                                                                \
    if (lane == 0) __hip_atomic_store((LAS unsigned*)(L + F_EW), 1u, RLX_WG);                                               \
  }
#define HELPER_SYNC(n)                                                                \
  {                                                                                   \
    LGKM0();                                                                          \
    if (lane == 0) __hip_atomic_fetch_add((LAS unsigned*)(L + F_HS), 1u, RLX_WG);     \
    lds_wait_ge(L + F_HS, 8u * (n));                                                  \
  }
  if (hw == 6 && turn >= 4) ML_EXCHANGE()
  {
    WAIT_UNTIL(STAG2_0 + turn * STAG2);
    const __amdgpu_buffer_rsrc_t ro = RSRC(wo + (size_t)(h * DH + 8 * hw) * DM, 72 * DM * 2);
    const int vo = (g * DM + 8 * c) * 2;
#pragma unroll
    for (int i = 0; i < 20; ++i) {
      VMWAIT(W_DEPTH - 1);
      wr[i] = __builtin_amdgcn_raw_buffer_load_b128(ro, vo, ((64 * (i / 10) + 4 * ((i / 5) & 1)) * DM + 128 * (i % 5)) * 2, 0);
      SB();
    }
    STAMP(7);
    const int a_w = L_ST + (8 * hw + g) * ST_LD + 16 * c;
    const int a_r = L_ST + (8 * g + (c >> 2)) * ST_LD + (80 * hw + 4 * (c & 3)) * 2;
#pragma unroll
    for (int hf = 0; hf < 2; ++hf) {
#pragma unroll
      for (int i = 0; i < 10; ++i) *(LAS u32x4*)(L + a_w + (i / 5) * 4 * ST_LD + (i % 5) * 256) = wr[10 * hf + i];
      HELPER_SYNC(2 * hf + 1)
#pragma unroll
      for (int i = 0; i < 10; ++i) {
        const u32x2 r0 = __builtin_bit_cast(u32x2, __builtin_amdgcn_ds_read_tr16_b64_v4i16((LAS s16x4*)(L + a_r + (i / 5) * 32 * ST_LD + (i % 5) * 32)));
        const u32x2 r1 = __builtin_bit_cast(u32x2, __builtin_amdgcn_ds_read_tr16_b64_v4i16((LAS s16x4*)(L + a_r + ((i / 5) * 32 + 4) * ST_LD + (i % 5) * 32)));
        wr[10 * hf + i] = u32x4{r0[0], r0[1], r1[0], r1[1]};
      }
      if (hf == 0) HELPER_SYNC(2)
    }
  }
  if (hw == 6 && turn < 4) ML_EXCHANGE()
  if (hw == 7 && bid == 0) {
    float f4[4];
    sweep<4, 64>(ws + G_X + lane * 64 + 48, 64 * 64, tag, f4);
    if (lane == 0) __hip_atomic_store((GAS unsigned*)(ws + G_E), tag, RLX_AGENT);
  }
  int a_o = L_O + c * O_LD + 16 * g, v_st = lane * 16;
  FRESH(a_o); FRESH(v_st);
  STAMP(5);
  WG_BARRIER();
  STAMP(3);
  {
    u32x4 a[4];
#pragma unroll
    for (int u = 0; u < 4; ++u) a[u] = *(const LAS u32x4*)(L + a_o + 64 * u);
    float z[5];
#pragma unroll
    for (int i = 0; i < 5; ++i) {
      f32x4 y = {0.f, 0.f, 0.f, 0.f};
#pragma unroll
      for (int u = 0; u < 4; ++u) y = mfma(a[u], wr[5 * u + i], y);
      z[i] = (y[0] + y[1]) + (y[2] + y[3]);
    }
    const float e = swap16_sum(swap32_sum(z[0], z[4]), swap32_sum(z[2], z[4]));
    float o = swap16_sum(swap32_sum(z[1], z[4]), swap32_sum(z[3], z[4]));
    const float o8 = dppf<XOR8>(o);
    o = g < 2 ? o : o8;
    __builtin_amdgcn_raw_buffer_store_b128(u32x4{__float_as_uint(e + ys0), tag, __float_as_uint(o + ys1), tag}, rpub, v_st, 0, AUX_SC1);
  }
  if (FIXSEL) while ((int)((unsigned)__builtin_amdgcn_s_memrealtime() - t_org - 6000u) < 0) __builtin_amdgcn_s_sleep(1);
}

extern "C" void kernel_launch(void* const* d_in, const int* in_sizes, int n_in, void* d_out, int out_size, void* d_ws,
                              size_t ws_size, hipStream_t stream) {
  (void)in_sizes; (void)n_in; (void)out_size; (void)ws_size;
  typedef const GAS unsigned short* cp;
  k_main<<<NWG, 1024, 0, stream>>>((cp)d_in[0], (cp)d_in[1], (cp)d_in[2], (cp)d_in[3], (cp)d_in[4], (cp)d_in[6], (cp)d_in[7],
                                   (cp)d_in[8], (GAS u64*)d_ws, (GAS unsigned short*)d_out);
}
```

```cpp
#include <hip/hip_runtime.h>
#include <hip/hip_bf16.h>
#include <stdint.h>

#define DEPTH 3
#define DEPTH_LO 3
#define DEPTH_HI 5
#define LAG1 70
#define W_DEPTH 6
#define XORG 105
#define PACE0 450
#define PACE_A 128
#define PACE 101
#define STAG1_0 950
#define STAG1 125
#define JUMP1 140
#define STAG2_0 1950
#define STAG2 205
#define JUMP2 240
#define TAILFREE 8
#define YDELAY 18
#define YPOLL_N 8
#define YPOLL_S 8
#define YPOLL_S0 1
#define WSTAG 12
#define WIN1 110
#define WIN2 200
#define WINPRE 40
#define QROT 2
#define FT_DEPTH 5
constexpr int AUX_NT = 2, AUX_SC1 = 16;

typedef unsigned u32x4 __attribute__((ext_vector_type(4)));
typedef unsigned u32x2 __attribute__((ext_vector_type(2)));
typedef float f32x4 __attribute__((ext_vector_type(4)));
typedef float f32x2 __attribute__((ext_vector_type(2)));
typedef short bf16x8 __attribute__((ext_vector_type(8)));
typedef __bf16 bf16x2_t __attribute__((ext_vector_type(2)));
typedef short s16x4 __attribute__((ext_vector_type(4)));
typedef unsigned long long u64;
#define GAS __attribute__((address_space(1)))
#define LAS __attribute__((address_space(3)))
#define RLX_AGENT __ATOMIC_RELAXED, __HIP_MEMORY_SCOPE_AGENT
#define RLX_WG __ATOMIC_RELAXED, __HIP_MEMORY_SCOPE_WORKGROUP

constexpr int NB = 4, DM = 640, NH = 32, SK = 4096, DH = 128, FF = 1728;
constexpr int NSL = FF / 64;
constexpr int NWG = 256;
constexpr float LOG2E = 1.4426950408889634f;
constexpr float QSCALE = 0.08838834764831845f;
constexpr unsigned SPIN_CAP = 1u << 18;

constexpr size_t G_E = 0;
constexpr size_t G_T = 16;
constexpr size_t G_X = 32;
constexpr size_t G_Y = G_X + (size_t)NWG * 64;

constexpr int L_XQ = 0;
constexpr int L_Q = L_XQ + DM * 2;
constexpr int L_P = L_Q + 256;
constexpr int O_LD = 272;
constexpr int L_O = L_P + 2 * 16 * 64 * 4;
constexpr int L_ML = L_O + 16 * O_LD;
constexpr int L_EW = L_ML + 64;
constexpr int L_FLAG = L_EW + 32;
constexpr int F_Q = L_FLAG, F_ML = L_FLAG + 4, F_H = L_FLAG + 8, F_P0 = L_FLAG + 12, F_P1 = L_FLAG + 16, F_FF = L_FLAG + 20,
              F_RED = L_FLAG + 24, F_TAG = L_FLAG + 28, F_T0 = L_FLAG + 32, F_EW = L_FLAG + 36, F_HS = L_FLAG + 40, F_ADJ = L_FLAG + 44;
constexpr int L_H = L_FLAG + 64;
constexpr int L_RED = L_H + 64;
constexpr int L_FF = L_RED + 512;
constexpr int ST_LD = DM * 2 + 16;
constexpr int L_ST = L_FF + 8 * DM * 4;
constexpr int L_END = L_ST + 64 * ST_LD;

__device__ __forceinline__ float bflo(unsigned u) { return __builtin_bit_cast(float, u << 16); }
__device__ __forceinline__ float bfhi(unsigned u) { return __builtin_bit_cast(float, u & 0xffff0000u); }
__device__ __forceinline__ unsigned pk2(float lo, float hi) {
  bf16x2_t r;
  r[0] = (__bf16)lo;
  r[1] = (__bf16)hi;
  return __builtin_bit_cast(unsigned, r);
}
__device__ __forceinline__ float rbf(float x) { return (float)(__bf16)x; }
__device__ __forceinline__ float dot2(unsigned a, unsigned b, float c) {
  return __builtin_amdgcn_fdot2_f32_bf16(__builtin_bit_cast(bf16x2_t, a), __builtin_bit_cast(bf16x2_t, b), c, false);
}
template <int CTRL> __device__ __forceinline__ float dppf(float x) {
  return __builtin_bit_cast(float, __builtin_amdgcn_mov_dpp(__builtin_bit_cast(int, x), CTRL, 0xf, 0xf, true));
}
template <int CTRL> __device__ __forceinline__ unsigned dppu(unsigned x) {
  return (unsigned)__builtin_amdgcn_mov_dpp((int)x, CTRL, 0xf, 0xf, true);
}
constexpr int XOR1 = 0xB1, XOR2 = 0x4E, XOR7 = 0x141, XOR8 = 0x128;
template <int CTRL> __device__ __forceinline__ float tr_merge(float lo, float hi, bool up) {
  const float t1 = lo + dppf<CTRL>(lo);
  const float t2 = hi + dppf<CTRL>(hi);
  return up ? t2 : t1;
}
__device__ __forceinline__ float row16_sum(float x) {
  x += dppf<XOR1>(x); x += dppf<XOR2>(x); x += dppf<XOR7>(x); x += dppf<XOR8>(x);
  return x;
}
__device__ __forceinline__ float row16_max(float x) {
  x = fmaxf(x, dppf<XOR1>(x)); x = fmaxf(x, dppf<XOR2>(x)); x = fmaxf(x, dppf<XOR7>(x)); x = fmaxf(x, dppf<XOR8>(x));
  return x;
}
__device__ __forceinline__ float half_sum(float x) {
  auto t = __builtin_amdgcn_permlane32_swap(__float_as_uint(x), __float_as_uint(x), false, false);
  return __uint_as_float(t[0]) + __uint_as_float(t[1]);
}
__device__ __forceinline__ float xrow_max(float x) {
  auto s = __builtin_amdgcn_permlane16_swap(__float_as_uint(x), __float_as_uint(x), false, false);
  x = fmaxf(__uint_as_float(s[0]), __uint_as_float(s[1]));
  auto t = __builtin_amdgcn_permlane32_swap(__float_as_uint(x), __float_as_uint(x), false, false);
  return fmaxf(__uint_as_float(t[0]), __uint_as_float(t[1]));
}
__device__ __forceinline__ float xrow_sum(float x) {
  auto s = __builtin_amdgcn_permlane16_swap(__float_as_uint(x), __float_as_uint(x), false, false);
  x = __uint_as_float(s[0]) + __uint_as_float(s[1]);
  return half_sum(x);
}
__device__ __forceinline__ float swap32_sum(float a, float b) {
  auto t = __builtin_amdgcn_permlane32_swap(__float_as_uint(a), __float_as_uint(b), false, false);
  return __uint_as_float(t[0]) + __uint_as_float(t[1]);
}
__device__ __forceinline__ float swap16_sum(float a, float b) {
  auto t = __builtin_amdgcn_permlane16_swap(__float_as_uint(a), __float_as_uint(b), false, false);
  return __uint_as_float(t[0]) + __uint_as_float(t[1]);
}
__device__ __forceinline__ f32x4 mfma(u32x4 a, u32x4 b, f32x4 c) {
  return __builtin_amdgcn_mfma_f32_16x16x32_bf16(__builtin_bit_cast(bf16x8, a), __builtin_bit_cast(bf16x8, b), c, 0, 0, 0);
}
#define LGKM0() asm volatile("s_waitcnt lgkmcnt(0)" ::: "memory")
#define WG_BARRIER()                \
  do {                              \
    LGKM0();                        \
    __builtin_amdgcn_s_barrier();   \
    asm volatile("" ::: "memory");  \
  } while (0)
#define SB() __builtin_amdgcn_sched_barrier(0)
#define RSRC(p, n) __builtin_amdgcn_make_buffer_rsrc((void*)(p), 0, (n), 0x00020000)
#define FRESH(v) asm volatile("" : "+v"(v))
#define VMWAIT_UNLESS(n, v, k, m) \
  asm volatile("s_waitcnt vmcnt(%3)\n\ts_cmp_ge_u32 %1, %2\n\ts_cbranch_scc1 1f\n\ts_waitcnt vmcnt(%0)\n1:" ::"i"(n), "s"(v), "i"(k), "i"(m) : "memory", "scc")
#define VMWAIT(n) asm volatile("s_waitcnt vmcnt(%0)" ::"i"(n) : "memory")

__device__ __forceinline__ void put(GAS u64* p, float v, unsigned tag) {
  __hip_atomic_store(p, ((u64)tag << 32) | (u64)__float_as_uint(v), RLX_AGENT);
}
__device__ __forceinline__ u64 get(const GAS u64* p) { return __hip_atomic_load(p, RLX_AGENT); }
__device__ __forceinline__ unsigned lds_ld(LAS unsigned char* p) { return __hip_atomic_load((LAS unsigned*)p, RLX_WG); }
__device__ __forceinline__ void lds_wait_ge(LAS unsigned char* p, unsigned want) {
  while (lds_ld(p) < want) __builtin_amdgcn_s_sleep(1);
  asm volatile("" ::: "memory");
}
template <int N, int SLP> __device__ __forceinline__ void sweep(const GAS u64* p, int S, unsigned tag, float (&v)[N]) {
  for (unsigned spins = 0;; ++spins) {
    bool ok = true;
#pragma unroll
    for (int k = 0; k < N; ++k) {
      const u64 x = get(p + k * S);
      v[k] = __uint_as_float((unsigned)x);
      ok &= (unsigned)(x >> 32) == tag;
    }
    if (__all(ok) || spins > SPIN_CAP) return;
    __builtin_amdgcn_s_sleep(SLP);
  }
}

struct Sel { unsigned s[4]; };
__device__ __forceinline__ Sel make_sel(int lane) {
  const int m = lane & 15;
  const unsigned src = (m & 8) ? 0x0706u : 0x0504u;
  const unsigned placed = (m & 1) ? ((src << 16) | 0x0c0cu) : (0x0c0c0000u | src);
  Sel r;
#pragma unroll
  for (int jj = 0; jj < 4; ++jj) r.s[jj] = (((m & 7) >> 1) == jj) ? placed : 0x0c0c0c0cu;
  return r;
}
__device__ __forceinline__ u32x4 make_a(unsigned X, const Sel& s) {
  u32x4 a;
#pragma unroll
  for (int jj = 0; jj < 4; ++jj) a[jj] = __builtin_amdgcn_perm(X, 0u, s.s[jj]);
  return a;
}
__device__ __forceinline__ unsigned split_hi_lo(float v) {
  const float h = rbf(v);
  return pk2(h, v - h);
}


__device__ __forceinline__ void ln_row(const unsigned (&xr)[5], LAS unsigned char* dst, int lane) {
  float sum = 0.f;
#pragma unroll
  for (int k = 0; k < 5; ++k) sum += bflo(xr[k]) + bfhi(xr[k]);
  const float mean = xrow_sum(row16_sum(sum)) * (1.0f / DM);
  float ss = 0.f;
#pragma unroll
  for (int k = 0; k < 5; ++k) {
    const float a = bflo(xr[k]) - mean, bb = bfhi(xr[k]) - mean;
    ss = fmaf(a, a, ss);
    ss = fmaf(bb, bb, ss);
  }
  const float var = xrow_sum(row16_sum(ss)) * (1.0f / DM);
  const float mu = rbf(mean);
  const float rinv = 1.0f / rbf(sqrtf(rbf(rbf(var) + 1e-5f)));
#pragma unroll
  for (int k = 0; k < 5; ++k)
    *(LAS unsigned*)(dst + 4 * (lane + 64 * k)) = pk2(rbf(bflo(xr[k]) - mu) * rinv, rbf(bfhi(xr[k]) - mu) * rinv);
}

__global__ void __launch_bounds__(1024, 4)
k_main(const GAS unsigned short* __restrict__ x, const GAS unsigned short* __restrict__ kc, const GAS unsigned short* __restrict__ vc,
       const GAS unsigned short* __restrict__ wq, const GAS unsigned short* __restrict__ wo, const GAS unsigned short* __restrict__ wg,
       const GAS unsigned short* __restrict__ wu, const GAS unsigned short* __restrict__ wd, GAS u64* ws, GAS unsigned short* out) {
  __shared__ __attribute__((aligned(16))) unsigned char smem[L_END];
  LAS unsigned char* L = (LAS unsigned char*)smem;
  const int tid = threadIdx.x, lane = tid & 63, wave = __builtin_amdgcn_readfirstlane(tid >> 6);
  const int g = lane >> 4, c = lane & 15;
  const int bid = blockIdx.x;
  const int jx = bid >> 3;
  const int h = (jx >> 3) * 8 + (bid & 7), mem = jx & 7, b = mem >> 1, s = mem & 1;
  const int slot = (b * NH + h) * 2 + s;
  const int hs = h < NSL ? h : h - 8;
  const unsigned turn = (mem + 2 * (jx >> 3)) & 7;
  if (tid < 16) *(LAS unsigned*)(L + L_FLAG + 4 * tid) = 0u;
  asm volatile("" ::"s"(x), "s"(kc), "s"(vc), "s"(wq), "s"(wo), "s"(wg), "s"(wu), "s"(wd), "s"(ws), "s"(out));
  WG_BARRIER();
  const Sel sel = make_sel(lane);
  GAS u64* const xme = ws + G_X + (size_t)slot * 64;
  const GAS u64* const xot = ws + G_X + (size_t)(slot ^ 1) * 64;
#define CLOCK() ((unsigned)__builtin_amdgcn_s_memrealtime())

  if (wave >= 8) {
    const int w = wave - 8;
    const size_t base = (size_t)(b * NH + h) * SK * DH;
    const GAS unsigned short* kb = kc + base;
    const GAS unsigned short* vb = vc + base;
    int voff = lane * 16 + ((w & 3) + 4 * s + 8 * (w >> 2)) * 4096;
#define TILE_LD(r, so, t) __builtin_amdgcn_raw_buffer_load_b128((r), voff + ((t) & 3) * 1024, (so) + ((t) >> 2) * 65536, AUX_NT)
    u32x4 ring[16];
    {
      const __amdgpu_buffer_rsrc_t rk = RSRC(kb, SK * DH * 2);
#pragma unroll
      for (int t = 0; t < 16; ++t) {
        if (t >= DEPTH) VMWAIT(DEPTH - 1);
        ring[t] = TILE_LD(rk, 0, t);
        SB();
      }
    }
    lds_wait_ge(L + F_Q, 2u);
    const u32x4 q = *(const LAS u32x4*)(L + L_Q + 16 * c);
    unsigned tb = __builtin_amdgcn_readfirstlane(lds_ld(L + F_T0));
    unsigned pstep = PACE_A, lag = 0;
    unsigned tgt = PACE0 + w * WSTAG, j1 = STAG1_0 + turn * STAG1, j2 = STAG2_0 + turn * STAG2;
    unsigned w1 = j1 - WINPRE;
    const unsigned w2 = j2 - WINPRE;
#define PACED_WAIT(t)                                                         \
  if (((t) & 3) == 0) {                                                       \
    tgt += ((t) > 16 - TAILFREE) ? ptail : pstep;                             \
    if (tgt >= j1) { tgt += JUMP1; j1 = ~0u; }                                \
    if (tgt >= j2) { tgt += JUMP2; j2 = ~0u; }                                \
    unsigned now;                                                             \
    while (((now = CLOCK() - tb) < tgt || now - w1 < WIN1 || now - w2 < WIN2) && now < (1u << 20)) __builtin_amdgcn_s_sleep(1); \
    lag = now - tgt;                                                          \
  }                                                                           \
  if ((t) == 16 - TAILFREE) lag |= dtail;                                     \
  VMWAIT_UNLESS(DEPTH_LO - 1, lag, LAG1, DEPTH_HI - 1);
    float sc0 = 0.f, sc1 = 0.f, sc2 = 0.f, sc3 = 0.f;
#define MERGE(LO, HI, CTRL, BIT) tr_merge<CTRL>((LO), (HI), (c & (BIT)) != 0)
#define QK_GROUP()                                      \
  {                                                     \
    const unsigned ptail = pstep, dtail = 0;            \
    FRESH(voff);                                        \
    float d1 = 0.f, d2 = 0.f, d4 = 0.f, d8 = 0.f;       \
    _Pragma("unroll") for (int t = 0; t < 16; ++t) {    \
      float a = dot2(ring[t][0], q[0], 0.f);            \
      a = dot2(ring[t][1], q[1], a);                    \
      a = dot2(ring[t][2], q[2], a);                    \
      a = dot2(ring[t][3], q[3], a);                    \
      FRESH(a);                                         \
      SB();                                             \
      PACED_WAIT(t)                                     \
      ring[t] = TILE_LD(rn, so, t);                     \
      if (!(t & 1)) d1 = a;                             \
      else {                                            \
        a = MERGE(d1, a, XOR8, 8);                      \
        if (!(t & 2)) d2 = a;                           \
        else {                                          \
          a = MERGE(d2, a, XOR7, 4);                    \
          if (!(t & 4)) d4 = a;                         \
          else {                                        \
            a = MERGE(d4, a, XOR2, 2);                  \
            if (!(t & 8)) d8 = a;                       \
            else {                                      \
              a = MERGE(d8, a, XOR1, 1);                \
              sc0 = sc1; sc1 = sc2; sc2 = sc3;          \
              sc3 = rbf(rbf(a) * QSCALE) * LOG2E;       \
            }                                           \
          }                                             \
        }                                               \
      }                                                 \
      SB();                                             \
    }                                                   \
  }
#define ADOPT_ORIGIN()                                                                \
  {                                                                                   \
    const unsigned adj = __builtin_amdgcn_readfirstlane(lds_ld(L + F_ADJ));           \
    tb -= adj;                                                                        \
    if (j1 != ~0u) j1 += adj;                                                         \
    w1 += adj;                                                                        \
  }
#pragma unroll 1
    for (int grp = 0; grp < 4; ++grp) {
      const __amdgpu_buffer_rsrc_t rn = RSRC(grp < 3 ? kb : vb, SK * DH * 2);
      const int so = ((grp + 1) & 3) * 262144;
      QK_GROUP()
      pstep = PACE;
      if (grp == 0) ADOPT_ORIGIN()
    }
#undef QK_GROUP
#undef MERGE
    float mw = fmaxf(fmaxf(sc0, sc1), fmaxf(sc2, sc3));
    mw = xrow_max(row16_max(mw));
    const float p0 = __builtin_amdgcn_exp2f(sc0 - mw), p1 = __builtin_amdgcn_exp2f(sc1 - mw);
    const float p2 = __builtin_amdgcn_exp2f(sc2 - mw), p3 = __builtin_amdgcn_exp2f(sc3 - mw);
    unsigned pk0 = split_hi_lo(p0), pk1 = split_hi_lo(p1), pk2_ = split_hi_lo(p2), pk3 = split_hi_lo(p3);
    const float lw = xrow_sum(row16_sum((p0 + p1) + (p2 + p3)));
    if (lane == 0) {
      *(LAS float*)(L + L_ML + w * 4) = mw;
      *(LAS float*)(L + L_ML + 32 + w * 4) = lw;
      LGKM0();
      __hip_atomic_fetch_add((LAS unsigned*)(L + F_ML), 1u, RLX_WG);
    }
    SB();
    f32x4 acc = {0.f, 0.f, 0.f, 0.f};
#define BREV4(T) ((((T) & 1) << 3) | (((T) & 2) << 1) | (((T) & 4) >> 1) | (((T) & 8) >> 3))
#define PV_STEP(S, PK, T)                               \
  {                                                     \
    const unsigned X = dppu<0x150 + BREV4(S)>(PK);      \
    acc = mfma(make_a(X, sel), ring[S], acc);           \
    if ((T) >= 0) {                                     \
      SB();                                             \
      PACED_WAIT(T)                                     \
      ring[(T) & 15] = TILE_LD(rv, so, T);              \
    }                                                   \
    SB();                                               \
  }
#define PV_HALF(S, PK, T, D)                                                                               \
  PV_STEP((S), PK, (T)) PV_STEP((S) + 1, PK, (T) + (D)) PV_STEP((S) + 2, PK, (T) + 2 * (D)) PV_STEP((S) + 3, PK, (T) + 3 * (D)) \
  PV_STEP((S) + 4, PK, (T) + 4 * (D)) PV_STEP((S) + 5, PK, (T) + 5 * (D)) PV_STEP((S) + 6, PK, (T) + 6 * (D)) PV_STEP((S) + 7, PK, (T) + 7 * (D))
    const __amdgpu_buffer_rsrc_t rv = RSRC(vb, SK * DH * 2);
    {
      const int so = 0;
      const unsigned ptail = 0, dtail = 0;
      PV_HALF(0, pk0, -1, 0)
    }
#pragma unroll 1
    for (int grp = 0; grp < 3; ++grp) {
      const int so = (grp + 1) * 262144;
      const unsigned ptail = grp == 2 ? 0u : pstep, dtail = (unsigned)(grp >> 1) << 20;
      FRESH(voff);
      PV_HALF(8, pk0, 0, 1)
      PV_HALF(0, pk1, 8, 1)
      pk0 = pk1; pk1 = pk2_; pk2_ = pk3;
    }
    lds_wait_ge(L + F_EW, 1u);
    const float ew = *(const LAS float*)(L + L_EW + 4 * w);
    int a_o = L_O + (w * 2 + (g >> 1)) * O_LD + (8 * c + 4 * (g & 1)) * 2;
    FRESH(a_o);
    SB();
    {
      const int so = 0;
      const unsigned ptail = 0, dtail = 0;
      PV_HALF(8, pk0, -1, 0)
    }
#undef PV_HALF
#undef PV_STEP
    *(LAS u32x2*)(L + a_o) = u32x2{pk2(acc[0] * ew, acc[1] * ew), pk2(acc[2] * ew, acc[3] * ew)};
    WG_BARRIER();
    const int j = h * 2 + s;
    if (j >= 40) return;
    const int r5 = j % 5, k8 = lane & 7;
    const int colq = b * DM + 80 * (j / 5) + (r5 < 4 ? 32 * (r5 >> 1) + 8 * (r5 & 1) : 64) + (lane & 1) * 4 + ((lane >> 1) & 1) * (r5 < 4 ? 16 : 8);
    const u32x2 xq = *(const GAS u32x2*)(x + colq);
    const unsigned tag = lds_ld(L + F_TAG);
    const __amdgpu_buffer_rsrc_t ry = RSRC(ws + G_Y + (size_t)(b * 64 + 8 * w) * DM + 16 * j, 8 * DM * 8);
    u32x4 yv = {0u, 0u, 0u, 0u};
    bool miss = true;
    __builtin_amdgcn_s_sleep(YDELAY);
    for (unsigned spins = 0; spins < SPIN_CAP; ++spins) {
      if (miss) {
        yv = __builtin_amdgcn_raw_buffer_load_b128(ry, (lane >> 3) * (DM * 8) + k8 * 16, 0, AUX_SC1);
        miss = yv[1] != tag || yv[3] != tag;
      }
      const int nmiss = __builtin_popcountll(__builtin_amdgcn_ballot_w64(miss));
      if (nmiss == 0) break;
      if (nmiss > YPOLL_N) __builtin_amdgcn_s_sleep(YPOLL_S); else __builtin_amdgcn_s_sleep(YPOLL_S0);
    }
    float s0 = __uint_as_float(yv[0]), s1 = __uint_as_float(yv[2]);
    s0 += dppf<XOR8>(s0); s1 += dppf<XOR8>(s1);
    s0 = xrow_sum(s0); s1 = xrow_sum(s1);
    if (lane < 8) {
      *(LAS float*)(L + L_RED + (w * 16 + lane) * 4) = s0;
      *(LAS float*)(L + L_RED + (w * 16 + 8 + lane) * 4) = s1;
    }
    LGKM0();
    unsigned old = 0;
    if (lane == 0) old = __hip_atomic_fetch_add((LAS unsigned*)(L + F_RED), 1u, RLX_WG);
    asm volatile("" ::: "memory");
    if (__builtin_amdgcn_readfirstlane(old) == 7u) {
      f32x4 t = *(const LAS f32x4*)(L + L_RED + ((lane >> 2) & 7) * 64 + (lane & 3) * 16);
#pragma unroll
      for (int k = 0; k < 4; ++k) t[k] += dppf<0x124>(t[k]);
#pragma unroll
      for (int k = 0; k < 4; ++k) t[k] += dppf<XOR8>(t[k]);
#pragma unroll
      for (int k = 0; k < 4; ++k) t[k] = swap16_sum(t[k], t[k]);
      const u64 r = (u64)pk2(bflo(xq[0]) + t[0], bfhi(xq[0]) + t[1]) | ((u64)pk2(bflo(xq[1]) + t[2], bfhi(xq[1]) + t[3]) << 32);
      if (lane < 4) __hip_atomic_store((GAS u64*)(out + colq), r, RLX_AGENT);
    }
    return;
  }

  const int hw = wave;
  const int hr = (hw + QROT * b) & 7;
  __builtin_amdgcn_s_setprio(3);
  u32x4 wr[20];
  unsigned xp[10];
#define PROJ_MFMA(PH)                                                                                              \
  {                                                                                                                \
    f32x4 ga = {0.f, 0.f, 0.f, 0.f};                                                                               \
    _Pragma("unroll") for (int i = 0; i < 10; ++i) ga = mfma(make_a(xp[i], sel), wr[i], ga);                       \
    if ((g >> 1) == (c >> 3))                                                                                      \
      *(LAS f32x4*)(L + L_P + ((PH) * 1024 + (hr * 2 + (g >> 1)) * 64 + 8 * (c & 7) + 4 * (g & 1)) * 4) = ga;      \
    LGKM0();                                                                                                       \
    if (lane == 0) __hip_atomic_fetch_add((LAS unsigned*)(L + ((PH) ? F_P1 : F_P0)), 1u, RLX_WG);                   \
  }
#define PROJ_SUM(PH, R0, R1)                                                                                       \
  {                                                                                                                \
    lds_wait_ge(L + ((PH) ? F_P1 : F_P0), 8u);                                                                     \
    f32x4 t4 = {0.f, 0.f, 0.f, 0.f};                                                                               \
    _Pragma("unroll") for (int k = 0; k < 4; ++k) t4 += *(const LAS f32x4*)(L + L_P + ((PH) * 1024 + (4 * g + k) * 64 + 4 * c) * 4); \
    _Pragma("unroll") for (int k = 0; k < 4; ++k) t4[k] = xrow_sum(t4[k]);                                         \
    R0 = pk2(t4[0], t4[1]);                                                                                        \
    R1 = pk2(t4[2], t4[3]);                                                                                        \
  }
  unsigned tag = 0;
  {
    unsigned xr[5];
#pragma unroll
    for (int k = 0; k < 5; ++k) xr[k] = ((const GAS unsigned*)(x + b * DM))[lane + 64 * k];
    unsigned e = 0;
    if (hw < 2) e = *(const GAS unsigned*)(ws + G_E);
    const __amdgpu_buffer_rsrc_t rq = RSRC(wq + (size_t)80 * hr * (NH * DH) + h * DH + 64 * s, 80 * NH * DH * 2 - (h * DH + 64 * s) * 2);
    const int vq = ((20 * g + (c >> 3)) * NH * DH + 8 * (c & 7)) * 2;
#pragma unroll
    for (int i = 0; i < 10; ++i) {
      wr[i] = __builtin_amdgcn_raw_buffer_load_b128(rq, vq, i * 2 * NH * DH * 2, 0);
      SB();
    }
    ln_row(xr, L + L_XQ, lane);
    SB();
    if (hw == 0) {
      const unsigned t1 = CLOCK() - XORG;
      if (lane == 0) *(LAS unsigned*)(L + F_T0) = t1;
      if (bid < 8 && lane == 0) put(ws + G_T + bid, __uint_as_float(t1), e + 1u);
    }
    LGKM0();
#pragma unroll
    for (int k = 0; k < 5; ++k) {
      const u32x2 t = *(const LAS u32x2*)(L + L_XQ + (80 * hr + 20 * g + 4 * k) * 2);
      xp[2 * k] = t[0];
      xp[2 * k + 1] = t[1];
    }
    PROJ_MFMA(0)
    if (hw < 2) tag = __builtin_amdgcn_readfirstlane(e) + 1u;
    if (hw == 0) {
      unsigned r0, r1;
      PROJ_SUM(0, r0, r1)
      if (g == 0) {
        __builtin_amdgcn_raw_buffer_store_b128(u32x4{r0, tag, r1, tag}, RSRC(xme, 512), c * 16, 0, AUX_SC1);
        *(LAS u32x2*)(L + L_Q + 128 * s + 8 * c) = u32x2{r0, r1};
      }
      if (lane == 0) *(LAS unsigned*)(L + F_TAG) = tag;
      LGKM0();
      if (lane == 0) __hip_atomic_fetch_add((LAS unsigned*)(L + F_Q), 1u, RLX_WG);
    } else if (hw == 1) {
      lds_wait_ge(L + F_P0, 8u);
      float v[1];
      sweep<1, 1>(xot + (lane & 31), 1, tag, v);
      if (lane < 32) *(LAS float*)(L + L_Q + 128 * (s ^ 1) + 4 * lane) = v[0];
      LGKM0();
      if (lane == 0) __hip_atomic_fetch_add((LAS unsigned*)(L + F_Q), 1u, RLX_WG);
    }
  }
  __builtin_amdgcn_s_setprio(0);
  lds_wait_ge(L + F_Q, 2u);
  tag = __builtin_amdgcn_readfirstlane(lds_ld(L + F_TAG));
  unsigned tb = __builtin_amdgcn_readfirstlane(lds_ld(L + F_T0));
  if (hr == ((mem + 4) & 7)) {
    const u64 tv = get(ws + G_T + (lane & 7));
    const bool ok = (unsigned)(tv >> 32) == tag;
    float d = ok ? (float)(int)(tb - (unsigned)tv) : 0.f, n = ok ? 1.f : 0.f;
    d += dppf<XOR1>(d); d += dppf<XOR2>(d); d += dppf<XOR7>(d);
    n += dppf<XOR1>(n); n += dppf<XOR2>(n); n += dppf<XOR7>(n);
    const int adj = (int)fminf(fmaxf(d / fmaxf(n, 1.f), -400.f), 400.f);
    if (lane == 0) *(LAS int*)(L + F_ADJ) = adj;
  }
  const __amdgpu_buffer_rsrc_t rpub = RSRC(ws + G_Y + (size_t)(b * 64 + h * 2 + s) * DM + 80 * hw, 80 * 8);
  {
    const u32x4 pre = __builtin_amdgcn_raw_buffer_load_b128(rpub, lane * 16, 0, 0);
    asm volatile("" ::"v"(pre));
    if (hr == ((mem + 5) & 7)) {
      const unsigned short pre2 = __builtin_nontemporal_load(vc + (size_t)(b * NH + h) * SK * DH);
      asm volatile("" ::"v"(pre2));
      const unsigned short pre3 = out[b * DM + 10 * lane];
      asm volatile("" ::"v"(pre3));
    }
  }
#define WAIT_UNTIL(t) while (CLOCK() - tb < (t)) __builtin_amdgcn_s_sleep(4)
  const int pc0 = 80 * hw + (g < 2 ? 32 * g + c : 64 + (c & 7)), pc1 = pc0 + (g < 2 ? 16 : 8);
  float ys0 = 0.f, ys1 = 0.f;
  {
#pragma unroll
    for (int i = 0; i < 10; ++i) FRESH(xp[i]);
    if (hr == ((mem + 2) & 7)) {
      const __amdgpu_buffer_rsrc_t rp = RSRC(wo + (size_t)(h * DH + 16 * mem) * DM, 16 * DM * 2);
#pragma unroll
      for (int i = 0; i < 20; ++i) {
        VMWAIT(FT_DEPTH - 1);
        wr[i] = __builtin_amdgcn_raw_buffer_load_b128(rp, lane * 16, i * 1024, 0);
        SB();
      }
#pragma unroll
      for (int i = 0; i < 20; ++i) asm volatile("" ::"v"(wr[i]));
    }
    const unsigned isb = (unsigned)(hr >> 1) ^ (unsigned)b;
    if (isb) WAIT_UNTIL(STAG1_0 + turn * STAG1);
    const __amdgpu_buffer_rsrc_t rg = RSRC((s ? wu : wg) + (size_t)80 * hr * FF + 64 * hs, 80 * FF * 2 - 64 * hs * 2);
    const int vg = ((20 * g + (c >> 3)) * FF + 8 * (c & 7)) * 2;
#pragma unroll
    for (int i = 0; i < 10; ++i) {
      VMWAIT_UNLESS(FT_DEPTH - 1, isb, 1, W_DEPTH - 1);
      wr[i] = __builtin_amdgcn_raw_buffer_load_b128(rg, vg, i * 2 * FF * 2, 0);
      SB();
    }
    const __amdgpu_buffer_rsrc_t rd = RSRC(wd + (size_t)(64 * hs + 32 * s + 4 * hr) * DM, 4 * DM * 2);
#pragma unroll
    for (int cb = 0; cb < 5; ++cb) {
      VMWAIT_UNLESS(FT_DEPTH - 1, isb, 1, W_DEPTH - 1);
      wr[10 + cb] = __builtin_amdgcn_raw_buffer_load_b128(rd, (g * DM + 8 * c) * 2, cb * 256, 0);
      SB();
    }
    PROJ_MFMA(1)
    if (hw == 2) {
      unsigned r0, r1;
      PROJ_SUM(1, r0, r1)
      if (g == 0 && (c >> 3) != s) __builtin_amdgcn_raw_buffer_store_b128(u32x4{r0, tag, r1, tag}, RSRC(xme + 32, 128), (c & 7) * 16, 0, AUX_SC1);
      float v[2];
      sweep<2, 16>(xot + 32 + 2 * (c & 7), 1, tag, v);
      const unsigned o0 = __float_as_uint(v[0]), o1 = __float_as_uint(v[1]);
      if (g == 0 && (c >> 3) == s)
        *(LAS u32x2*)(L + L_H + 8 * (c & 7)) = u32x2{pk2(bflo(r0) * bflo(o0), bfhi(r0) * bfhi(o0)), pk2(bflo(r1) * bflo(o1), bfhi(r1) * bfhi(o1))};
      LGKM0();
      if (lane == 0) __hip_atomic_store((LAS unsigned*)(L + F_H), 1u, RLX_WG);
    }
    lds_wait_ge(L + F_H, 1u);
    const unsigned X = *(const LAS unsigned short*)(L + L_H + 2 * (4 * hr + g));
    const u32x4 a = make_a(X, sel);
#pragma unroll
    for (int cb = 0; cb < 5; ++cb) {
      const f32x4 yf = mfma(a, wr[10 + cb], f32x4{0.f, 0.f, 0.f, 0.f});
      if (g < 2) *(LAS f32x4*)(L + L_FF + (hr * DM + 128 * cb + 8 * c + 4 * g) * 4) = yf;
    }
    LGKM0();
    if (lane == 0) __hip_atomic_fetch_add((LAS unsigned*)(L + F_FF), 1u, RLX_WG);
    if (h < NSL) {
      lds_wait_ge(L + F_FF, 8u);
#pragma unroll
      for (int p = 0; p < 8; ++p) {
        ys0 += *(const LAS float*)(L + L_FF + (p * DM + pc0) * 4);
        ys1 += *(const LAS float*)(L + L_FF + (p * DM + pc1) * 4);
      }
    }
  }
#define ML_EXCHANGE()                                                                                                       \
  {                                                                                                                         \
    lds_wait_ge(L + F_ML, 8u);                                                                                              \
    const float mv = *(const LAS float*)(L + L_ML + 4 * (lane & 7)), lv = *(const LAS float*)(L + L_ML + 32 + 4 * (lane & 7)); \
    float M = fmaxf(mv, dppf<XOR1>(mv));                                                                                    \
    M = fmaxf(M, dppf<XOR2>(M));                                                                                            \
    M = fmaxf(M, dppf<XOR7>(M));                                                                                            \
    float Ls = __builtin_amdgcn_exp2f(mv - M) * lv;                                                                         \
    FRESH(Ls);                                                                                                              \
    Ls += dppf<XOR1>(Ls);                                                                                                   \
    Ls += dppf<XOR2>(Ls);                                                                                                   \
    Ls += dppf<XOR7>(Ls);                                                                                                   \
    if (lane < 2) put(xme + 48 + lane, lane == 0 ? M : Ls, tag);                                                            \
    float o2[2];                                                                                                            \
    sweep<2, 16>(xot + 48, 1, tag, o2);                                                                                     \
    const float Mt = fmaxf(M, o2[0]);                                                                                       \
    float e0 = __builtin_amdgcn_exp2f(M - Mt) * Ls, e1 = __builtin_amdgcn_exp2f(o2[0] - Mt) * o2[1];                        \
    FRESH(e0); FRESH(e1);                                                                                                   \
    if (lane < 8) *(LAS float*)(L + L_EW + 4 * lane) = __builtin_amdgcn_exp2f(mv - Mt) / (e0 + e1);                         \
    LGKM0();                                                                                                                \
    if (lane == 0) __hip_atomic_store((LAS unsigned*)(L + F_EW), 1u, RLX_WG);                                               \
  }
#define HELPER_SYNC(n)                                                                \
  {                                                                                   \
    LGKM0();                                                                          \
    if (lane == 0) __hip_atomic_fetch_add((LAS unsigned*)(L + F_HS), 1u, RLX_WG);     \
    lds_wait_ge(L + F_HS, 8u * (n));                                                  \
  }
  if (hw == 6 && turn >= 4) ML_EXCHANGE()
  {
    tb -= __builtin_amdgcn_readfirstlane(lds_ld(L + F_ADJ));
    WAIT_UNTIL(STAG2_0 + turn * STAG2);
    const __amdgpu_buffer_rsrc_t ro = RSRC(wo + (size_t)(h * DH + 8 * hw) * DM, 72 * DM * 2);
    const int vo = (g * DM + 8 * c) * 2;
#pragma unroll
    for (int i = 0; i < 20; ++i) {
      VMWAIT(W_DEPTH - 1);
      wr[i] = __builtin_amdgcn_raw_buffer_load_b128(ro, vo, ((64 * (i / 10) + 4 * ((i / 5) & 1)) * DM + 128 * (i % 5)) * 2, 0);
      SB();
    }
    const int a_w = L_ST + (8 * hw + g) * ST_LD + 16 * c;
    const int a_r = L_ST + (8 * g + (c >> 2)) * ST_LD + (80 * hw + 4 * (c & 3)) * 2;
#pragma unroll
    for (int hf = 0; hf < 2; ++hf) {
#pragma unroll
      for (int i = 0; i < 10; ++i) *(LAS u32x4*)(L + a_w + (i / 5) * 4 * ST_LD + (i % 5) * 256) = wr[10 * hf + i];
      HELPER_SYNC(2 * hf + 1)
#pragma unroll
      for (int i = 0; i < 10; ++i) {
        const u32x2 r0 = __builtin_bit_cast(u32x2, __builtin_amdgcn_ds_read_tr16_b64_v4i16((LAS s16x4*)(L + a_r + (i / 5) * 32 * ST_LD + (i % 5) * 32)));
        const u32x2 r1 = __builtin_bit_cast(u32x2, __builtin_amdgcn_ds_read_tr16_b64_v4i16((LAS s16x4*)(L + a_r + ((i / 5) * 32 + 4) * ST_LD + (i % 5) * 32)));
        wr[10 * hf + i] = u32x4{r0[0], r0[1], r1[0], r1[1]};
      }
      if (hf == 0) HELPER_SYNC(2)
    }
  }
  if (hw == 6 && turn < 4) ML_EXCHANGE()
  if (hw == 7 && bid == 0) {
    float f4[4];
    sweep<4, 64>(ws + G_X + lane * 64 + 48, 64 * 64, tag, f4);
    if (lane == 0) __hip_atomic_store((GAS unsigned*)(ws + G_E), tag, RLX_AGENT);
  }
  int a_o = L_O + c * O_LD + 16 * g, v_st = lane * 16;
  FRESH(a_o); FRESH(v_st);
  WG_BARRIER();
  {
    u32x4 a[4];
#pragma unroll
    for (int u = 0; u < 4; ++u) a[u] = *(const LAS u32x4*)(L + a_o + 64 * u);
    float z[5];
#pragma unroll
    for (int i = 0; i < 5; ++i) {
      f32x4 y = {0.f, 0.f, 0.f, 0.f};
#pragma unroll
      for (int u = 0; u < 4; ++u) y = mfma(a[u], wr[5 * u + i], y);
      z[i] = (y[0] + y[1]) + (y[2] + y[3]);
    }
    const float e = swap16_sum(swap32_sum(z[0], z[4]), swap32_sum(z[2], z[4]));
    float o = swap16_sum(swap32_sum(z[1], z[4]), swap32_sum(z[3], z[4]));
    const float o8 = dppf<XOR8>(o);
    o = g < 2 ? o : o8;
    __builtin_amdgcn_raw_buffer_store_b128(u32x4{__float_as_uint(e + ys0), tag, __float_as_uint(o + ys1), tag}, rpub, v_st, 0, AUX_SC1);
  }
}

extern "C" void kernel_launch(void* const* d_in, const int* in_sizes, int n_in, void* d_out, int out_size, void* d_ws,
                              size_t ws_size, hipStream_t stream) {
  (void)in_sizes; (void)n_in; (void)out_size; (void)ws_size;
  typedef const GAS unsigned short* cp;
  k_main<<<NWG, 1024, 0, stream>>>((cp)d_in[0], (cp)d_in[1], (cp)d_in[2], (cp)d_in[3], (cp)d_in[4], (cp)d_in[6], (cp)d_in[7],
                                   (cp)d_in[8], (GAS u64*)d_ws, (GAS unsigned short*)d_out);
}
```

```cpp
#include <hip/hip_runtime.h>
#include <hip/hip_bf16.h>
#include <stdint.h>

#define DEPTH 3
#define DEPTH_LO 3
#define DEPTH_HI 5
#define LAG1 110
#define W_DEPTH 6
#define XORG 105
#define PACE0 450
#define PACE_A 128
#define PACE 101
#define STAG1_0 950
#define STAG1 125
#define JUMP1 140
#define STAG2_0 1950
#define STAG2 205
#define JUMP2 240
#define TAILFREE 8
#define YDELAY 26
#define YPOLL_N 8
#define YPOLL_S 3
#define YPOLL_S0 1
#define WSTAG 12
#define WIN1 110
#define WIN2 200
#define WINPRE 40
#define QROT 2
#define FT_DEPTH 5
constexpr int AUX_NT = 2, AUX_SC1 = 16;

typedef unsigned u32x4 __attribute__((ext_vector_type(4)));
typedef unsigned u32x2 __attribute__((ext_vector_type(2)));
typedef float f32x4 __attribute__((ext_vector_type(4)));
typedef float f32x2 __attribute__((ext_vector_type(2)));
typedef short bf16x8 __attribute__((ext_vector_type(8)));
typedef __bf16 bf16x2_t __attribute__((ext_vector_type(2)));
typedef short s16x4 __attribute__((ext_vector_type(4)));
typedef unsigned long long u64;
#define GAS __attribute__((address_space(1)))
#define LAS __attribute__((address_space(3)))
#define RLX_AGENT __ATOMIC_RELAXED, __HIP_MEMORY_SCOPE_AGENT
#define RLX_WG __ATOMIC_RELAXED, __HIP_MEMORY_SCOPE_WORKGROUP

constexpr int NB = 4, DM = 640, NH = 32, SK = 4096, DH = 128, FF = 1728;
constexpr int NSL = FF / 64;
constexpr int NWG = 256;
constexpr float LOG2E = 1.4426950408889634f;
constexpr float QSCALE = 0.08838834764831845f;
constexpr unsigned SPIN_CAP = 1u << 18;

constexpr size_t G_E = 0;
constexpr size_t G_T = 16;
constexpr size_t G_X = 32;
constexpr size_t G_Y = G_X + (size_t)NWG * 64;

constexpr int L_XQ = 0;
constexpr int L_Q = L_XQ + DM * 2;
constexpr int L_P = L_Q + 256;
constexpr int O_LD = 272;
constexpr int L_O = L_P + 2 * 16 * 64 * 4;
constexpr int L_ML = L_O + 16 * O_LD;
constexpr int L_EW = L_ML + 64;
constexpr int L_FLAG = L_EW + 32;
constexpr int F_Q = L_FLAG, F_ML = L_FLAG + 4, F_H = L_FLAG + 8, F_P0 = L_FLAG + 12, F_P1 = L_FLAG + 16, F_FF = L_FLAG + 20,
              F_RED = L_FLAG + 24, F_TAG = L_FLAG + 28, F_T0 = L_FLAG + 32, F_EW = L_FLAG + 36, F_HS = L_FLAG + 40, F_ADJ = L_FLAG + 44;
constexpr int L_H = L_FLAG + 64;
constexpr int L_RED = L_H + 64;
constexpr int L_FF = L_RED + 512;
constexpr int ST_LD = DM * 2 + 16;
constexpr int L_ST = L_FF + 8 * DM * 4;
constexpr int L_END = L_ST + 64 * ST_LD;

__device__ __forceinline__ float bflo(unsigned u) { return __builtin_bit_cast(float, u << 16); }
__device__ __forceinline__ float bfhi(unsigned u) { return __builtin_bit_cast(float, u & 0xffff0000u); }
__device__ __forceinline__ unsigned pk2(float lo, float hi) {
  bf16x2_t r;
  r[0] = (__bf16)lo;
  r[1] = (__bf16)hi;
  return __builtin_bit_cast(unsigned, r);
}
__device__ __forceinline__ float rbf(float x) { return (float)(__bf16)x; }
__device__ __forceinline__ float dot2(unsigned a, unsigned b, float c) {
  return __builtin_amdgcn_fdot2_f32_bf16(__builtin_bit_cast(bf16x2_t, a), __builtin_bit_cast(bf16x2_t, b), c, false);
}
template <int CTRL> __device__ __forceinline__ float dppf(float x) {
  return __builtin_bit_cast(float, __builtin_amdgcn_mov_dpp(__builtin_bit_cast(int, x), CTRL, 0xf, 0xf, true));
}
template <int CTRL> __device__ __forceinline__ unsigned dppu(unsigned x) {
  return (unsigned)__builtin_amdgcn_mov_dpp((int)x, CTRL, 0xf, 0xf, true);
}
constexpr int XOR1 = 0xB1, XOR2 = 0x4E, XOR7 = 0x141, XOR8 = 0x128;
template <int CTRL> __device__ __forceinline__ float tr_merge(float lo, float hi, bool up) {
  const float t1 = lo + dppf<CTRL>(lo);
  const float t2 = hi + dppf<CTRL>(hi);
  return up ? t2 : t1;
}
__device__ __forceinline__ float row16_sum(float x) {
  x += dppf<XOR1>(x); x += dppf<XOR2>(x); x += dppf<XOR7>(x); x += dppf<XOR8>(x);
  return x;
}
__device__ __forceinline__ float row16_max(float x) {
  x = fmaxf(x, dppf<XOR1>(x)); x = fmaxf(x, dppf<XOR2>(x)); x = fmaxf(x, dppf<XOR7>(x)); x = fmaxf(x, dppf<XOR8>(x));
  return x;
}
__device__ __forceinline__ float half_sum(float x) {
  auto t = __builtin_amdgcn_permlane32_swap(__float_as_uint(x), __float_as_uint(x), false, false);
  return __uint_as_float(t[0]) + __uint_as_float(t[1]);
}
__device__ __forceinline__ float xrow_max(float x) {
  auto s = __builtin_amdgcn_permlane16_swap(__float_as_uint(x), __float_as_uint(x), false, false);
  x = fmaxf(__uint_as_float(s[0]), __uint_as_float(s[1]));
  auto t = __builtin_amdgcn_permlane32_swap(__float_as_uint(x), __float_as_uint(x), false, false);
  return fmaxf(__uint_as_float(t[0]), __uint_as_float(t[1]));
}
__device__ __forceinline__ float xrow_sum(float x) {
  auto s = __builtin_amdgcn_permlane16_swap(__float_as_uint(x), __float_as_uint(x), false, false);
  x = __uint_as_float(s[0]) + __uint_as_float(s[1]);
  return half_sum(x);
}
__device__ __forceinline__ float swap32_sum(float a, float b) {
  auto t = __builtin_amdgcn_permlane32_swap(__float_as_uint(a), __float_as_uint(b), false, false);
  return __uint_as_float(t[0]) + __uint_as_float(t[1]);
}
__device__ __forceinline__ float swap16_sum(float a, float b) {
  auto t = __builtin_amdgcn_permlane16_swap(__float_as_uint(a), __float_as_uint(b), false, false);
  return __uint_as_float(t[0]) + __uint_as_float(t[1]);
}
__device__ __forceinline__ f32x4 mfma(u32x4 a, u32x4 b, f32x4 c) {
  return __builtin_amdgcn_mfma_f32_16x16x32_bf16(__builtin_bit_cast(bf16x8, a), __builtin_bit_cast(bf16x8, b), c, 0, 0, 0);
}
#define LGKM0() asm volatile("s_waitcnt lgkmcnt(0)" ::: "memory")
#define WG_BARRIER()                \
  do {                              \
    LGKM0();                        \
    __builtin_amdgcn_s_barrier();   \
    asm volatile("" ::: "memory");  \
  } while (0)
#define SB() __builtin_amdgcn_sched_barrier(0)
#define RSRC(p, n) __builtin_amdgcn_make_buffer_rsrc((void*)(p), 0, (n), 0x00020000)
#define FRESH(v) asm volatile("" : "+v"(v))
#define VMWAIT_UNLESS(n, v, k, m) \
  asm volatile("s_waitcnt vmcnt(%3)\n\ts_cmp_ge_u32 %1, %2\n\ts_cbranch_scc1 1f\n\ts_waitcnt vmcnt(%0)\n1:" ::"i"(n), "s"(v), "i"(k), "i"(m) : "memory", "scc")
#define VMWAIT(n) asm volatile("s_waitcnt vmcnt(%0)" ::"i"(n) : "memory")

__device__ __forceinline__ void put(GAS u64* p, float v, unsigned tag) {
  __hip_atomic_store(p, ((u64)tag << 32) | (u64)__float_as_uint(v), RLX_AGENT);
}
__device__ __forceinline__ u64 get(const GAS u64* p) { return __hip_atomic_load(p, RLX_AGENT); }
__device__ __forceinline__ unsigned lds_ld(LAS unsigned char* p) { return __hip_atomic_load((LAS unsigned*)p, RLX_WG); }
__device__ __forceinline__ void lds_wait_ge(LAS unsigned char* p, unsigned want) {
  while (lds_ld(p) < want) __builtin_amdgcn_s_sleep(1);
  asm volatile("" ::: "memory");
}
template <int N, int SLP> __device__ __forceinline__ void sweep(const GAS u64* p, int S, unsigned tag, float (&v)[N]) {
  for (unsigned spins = 0;; ++spins) {
    bool ok = true;
#pragma unroll
    for (int k = 0; k < N; ++k) {
      const u64 x = get(p + k * S);
      v[k] = __uint_as_float((unsigned)x);
      ok &= (unsigned)(x >> 32) == tag;
    }
    if (__all(ok) || spins > SPIN_CAP) return;
    __builtin_amdgcn_s_sleep(SLP);
  }
}

struct Sel { unsigned s[4]; };
__device__ __forceinline__ Sel make_sel(int lane) {
  const int m = lane & 15;
  const unsigned src = (m & 8) ? 0x0706u : 0x0504u;
  const unsigned placed = (m & 1) ? ((src << 16) | 0x0c0cu) : (0x0c0c0000u | src);
  Sel r;
#pragma unroll
  for (int jj = 0; jj < 4; ++jj) r.s[jj] = (((m & 7) >> 1) == jj) ? placed : 0x0c0c0c0cu;
  return r;
}
__device__ __forceinline__ u32x4 make_a(unsigned X, const Sel& s) {
  u32x4 a;
#pragma unroll
  for (int jj = 0; jj < 4; ++jj) a[jj] = __builtin_amdgcn_perm(X, 0u, s.s[jj]);
  return a;
}
__device__ __forceinline__ unsigned split_hi_lo(float v) {
  const float h = rbf(v);
  return pk2(h, v - h);
}


__device__ __forceinline__ void ln_row(const unsigned (&xr)[5], LAS unsigned char* dst, int lane) {
  float sum = 0.f;
#pragma unroll
  for (int k = 0; k < 5; ++k) sum += bflo(xr[k]) + bfhi(xr[k]);
  const float mean = xrow_sum(row16_sum(sum)) * (1.0f / DM);
  float ss = 0.f;
#pragma unroll
  for (int k = 0; k < 5; ++k) {
    const float a = bflo(xr[k]) - mean, bb = bfhi(xr[k]) - mean;
    ss = fmaf(a, a, ss);
    ss = fmaf(bb, bb, ss);
  }
  const float var = xrow_sum(row16_sum(ss)) * (1.0f / DM);
  const float mu = rbf(mean);
  const float rinv = 1.0f / rbf(sqrtf(rbf(rbf(var) + 1e-5f)));
#pragma unroll
  for (int k = 0; k < 5; ++k)
    *(LAS unsigned*)(dst + 4 * (lane + 64 * k)) = pk2(rbf(bflo(xr[k]) - mu) * rinv, rbf(bfhi(xr[k]) - mu) * rinv);
}

__global__ void __launch_bounds__(1024, 4)
k_main(const GAS unsigned short* x_, const GAS unsigned short* kc_, const GAS unsigned short* vc_, const GAS unsigned short* wq_, const GAS unsigned short* wo_,
       const GAS unsigned short* wg_, const GAS unsigned short* wu_, const GAS unsigned short* wd_, GAS u64* ws_, GAS unsigned short* out_) {
  const __attribute__((address_space(4))) u64* ka = (const __attribute__((address_space(4))) u64*)__builtin_amdgcn_kernarg_segment_ptr();
  unsigned long long t_org64;
  asm volatile("s_memrealtime %0\n\ts_waitcnt lgkmcnt(0)" : "=s"(t_org64), "+s"(ka) :: "memory");
  const unsigned t_org = (unsigned)t_org64;
  typedef const GAS unsigned short* cp;
  cp x = (cp)ka[0], kc = (cp)ka[1], vc = (cp)ka[2], wq = (cp)ka[3], wo = (cp)ka[4], wg = (cp)ka[5], wu = (cp)ka[6], wd = (cp)ka[7];
  GAS u64* ws = (GAS u64*)ka[8];
  GAS unsigned short* out = (GAS unsigned short*)ka[9];
  __shared__ __attribute__((aligned(16))) unsigned char smem[L_END];
  LAS unsigned char* L = (LAS unsigned char*)smem;
  const int tid = threadIdx.x, lane = tid & 63, wave = __builtin_amdgcn_readfirstlane(tid >> 6);
  const int g = lane >> 4, c = lane & 15;
  const int bid = blockIdx.x;
  const int jx = bid >> 3;
  const int h = (jx >> 3) * 8 + (bid & 7), mem = jx & 7, b = mem >> 1, s = mem & 1;
  const int slot = (b * NH + h) * 2 + s;
  const int hs = h < NSL ? h : h - 8;
  const unsigned turn = (mem + 2 * (jx >> 3)) & 7;
  if (tid < 16) *(LAS unsigned*)(L + L_FLAG + 4 * tid) = 0u;
  asm volatile("" ::"s"(x), "s"(kc), "s"(vc), "s"(wq), "s"(wo), "s"(wg), "s"(wu), "s"(wd), "s"(ws), "s"(out));
  WG_BARRIER();
  const Sel sel = make_sel(lane);
  GAS u64* const xme = ws + G_X + (size_t)slot * 64;
  const GAS u64* const xot = ws + G_X + (size_t)(slot ^ 1) * 64;
#define CLOCK() ((unsigned)__builtin_amdgcn_s_memrealtime())

  if (wave >= 8) {
    const int w = wave - 8;
    const size_t base = (size_t)(b * NH + h) * SK * DH;
    const GAS unsigned short* kb = kc + base;
    const GAS unsigned short* vb = vc + base;
    int voff = lane * 16 + ((w & 3) + 4 * s + 8 * (w >> 2)) * 4096;
#define TILE_LD(r, so, t) __builtin_amdgcn_raw_buffer_load_b128((r), voff + ((t) & 3) * 1024, (so) + ((t) >> 2) * 65536, AUX_NT)
    u32x4 ring[16];
    {
      const __amdgpu_buffer_rsrc_t rk = RSRC(kb, SK * DH * 2);
#pragma unroll
      for (int t = 0; t < 16; ++t) {
        if (t >= DEPTH) VMWAIT(DEPTH - 1);
        ring[t] = TILE_LD(rk, 0, t);
        SB();
      }
    }
    lds_wait_ge(L + F_Q, 2u);
    const u32x4 q = *(const LAS u32x4*)(L + L_Q + 16 * c);
    unsigned tb = __builtin_amdgcn_readfirstlane(lds_ld(L + F_T0));
    unsigned pstep = PACE_A, lag = 0;
    unsigned tgt = PACE0 + w * WSTAG, j1 = STAG1_0 + turn * STAG1, j2 = STAG2_0 + turn * STAG2;
    unsigned w1 = j1 - WINPRE;
    const unsigned w2 = j2 - WINPRE;
#define PACED_WAIT(t)                                                         \
  if (((t) & 3) == 0) {                                                       \
    tgt += ((t) > 16 - TAILFREE) ? ptail : pstep;                             \
    if (tgt >= j1) { tgt += JUMP1; j1 = ~0u; }                                \
    if (tgt >= j2) { tgt += JUMP2; j2 = ~0u; }                                \
    unsigned now;                                                             \
    while (((now = CLOCK() - tb) < tgt || now - w1 < WIN1 || now - w2 < WIN2) && now < (1u << 20)) __builtin_amdgcn_s_sleep(1); \
    lag = now - tgt;                                                          \
  }                                                                           \
  if ((t) == 16 - TAILFREE) lag |= dtail;                                     \
  VMWAIT_UNLESS(DEPTH_LO - 1, lag, LAG1, DEPTH_HI - 1);
    float sc0 = 0.f, sc1 = 0.f, sc2 = 0.f, sc3 = 0.f;
#define MERGE(LO, HI, CTRL, BIT) tr_merge<CTRL>((LO), (HI), (c & (BIT)) != 0)
#define QK_GROUP()                                      \
  {                                                     \
    const unsigned ptail = pstep, dtail = 0;            \
    FRESH(voff);                                        \
    float d1 = 0.f, d2 = 0.f, d4 = 0.f, d8 = 0.f;       \
    _Pragma("unroll") for (int t = 0; t < 16; ++t) {    \
      float a = dot2(ring[t][0], q[0], 0.f);            \
      a = dot2(ring[t][1], q[1], a);                    \
      a = dot2(ring[t][2], q[2], a);                    \
      a = dot2(ring[t][3], q[3], a);                    \
      FRESH(a);                                         \
      SB();                                             \
      PACED_WAIT(t)                                     \
      ring[t] = TILE_LD(rn, so, t);                     \
      if (!(t & 1)) d1 = a;                             \
      else {                                            \
        a = MERGE(d1, a, XOR8, 8);                      \
        if (!(t & 2)) d2 = a;                           \
        else {                                          \
          a = MERGE(d2, a, XOR7, 4);                    \
          if (!(t & 4)) d4 = a;                         \
          else {                                        \
            a = MERGE(d4, a, XOR2, 2);                  \
            if (!(t & 8)) d8 = a;                       \
            else {                                      \
              a = MERGE(d8, a, XOR1, 1);                \
              sc0 = sc1; sc1 = sc2; sc2 = sc3;          \
              sc3 = rbf(rbf(a) * QSCALE) * LOG2E;       \
            }                                           \
          }                                             \
        }                                               \
      }                                                 \
      SB();                                             \
    }                                                   \
  }
#define ADOPT_ORIGIN()                                                                \
  {                                                                                   \
    const unsigned adj = __builtin_amdgcn_readfirstlane(lds_ld(L + F_ADJ));           \
    tb -= adj;                                                                        \
    if (j1 != ~0u) j1 += adj;                                                         \
    w1 += adj;                                                                        \
  }
#pragma unroll 1
    for (int grp = 0; grp < 4; ++grp) {
      const __amdgpu_buffer_rsrc_t rn = RSRC(grp < 3 ? kb : vb, SK * DH * 2);
      const int so = ((grp + 1) & 3) * 262144;
      QK_GROUP()
      pstep = PACE;
      if (grp == 0) ADOPT_ORIGIN()
    }
#undef QK_GROUP
#undef MERGE
    float mw = fmaxf(fmaxf(sc0, sc1), fmaxf(sc2, sc3));
    mw = xrow_max(row16_max(mw));
    const float p0 = __builtin_amdgcn_exp2f(sc0 - mw), p1 = __builtin_amdgcn_exp2f(sc1 - mw);
    const float p2 = __builtin_amdgcn_exp2f(sc2 - mw), p3 = __builtin_amdgcn_exp2f(sc3 - mw);
    unsigned pk0 = split_hi_lo(p0), pk1 = split_hi_lo(p1), pk2_ = split_hi_lo(p2), pk3 = split_hi_lo(p3);
    const float lw = xrow_sum(row16_sum((p0 + p1) + (p2 + p3)));
    if (lane == 0) {
      *(LAS float*)(L + L_ML + w * 4) = mw;
      *(LAS float*)(L + L_ML + 32 + w * 4) = lw;
      LGKM0();
      __hip_atomic_fetch_add((LAS unsigned*)(L + F_ML), 1u, RLX_WG);
    }
    SB();
    f32x4 acc = {0.f, 0.f, 0.f, 0.f};
#define BREV4(T) ((((T) & 1) << 3) | (((T) & 2) << 1) | (((T) & 4) >> 1) | (((T) & 8) >> 3))
#define PV_STEP(S, PK, T)                               \
  {                                                     \
    const unsigned X = dppu<0x150 + BREV4(S)>(PK);      \
    acc = mfma(make_a(X, sel), ring[S], acc);           \
    if ((T) >= 0) {                                     \
      SB();                                             \
      PACED_WAIT(T)                                     \
      ring[(T) & 15] = TILE_LD(rv, so, T);              \
    }                                                   \
    SB();                                               \
  }
#define PV_HALF(S, PK, T, D)                                                                               \
  PV_STEP((S), PK, (T)) PV_STEP((S) + 1, PK, (T) + (D)) PV_STEP((S) + 2, PK, (T) + 2 * (D)) PV_STEP((S) + 3, PK, (T) + 3 * (D)) \
  PV_STEP((S) + 4, PK, (T) + 4 * (D)) PV_STEP((S) + 5, PK, (T) + 5 * (D)) PV_STEP((S) + 6, PK, (T) + 6 * (D)) PV_STEP((S) + 7, PK, (T) + 7 * (D))
    const __amdgpu_buffer_rsrc_t rv = RSRC(vb, SK * DH * 2);
    {
      const int so = 0;
      const unsigned ptail = 0, dtail = 0;
      PV_HALF(0, pk0, -1, 0)
    }
#pragma unroll 1
    for (int grp = 0; grp < 3; ++grp) {
      const int so = (grp + 1) * 262144;
      const unsigned ptail = grp == 2 ? 0u : pstep, dtail = (unsigned)(grp >> 1) << 20;
      FRESH(voff);
      PV_HALF(8, pk0, 0, 1)
      PV_HALF(0, pk1, 8, 1)
      pk0 = pk1; pk1 = pk2_; pk2_ = pk3;
    }
    lds_wait_ge(L + F_EW, 1u);
    const float ew = *(const LAS float*)(L + L_EW + 4 * w);
    int a_o = L_O + (w * 2 + (g >> 1)) * O_LD + (8 * c + 4 * (g & 1)) * 2;
    FRESH(a_o);
    SB();
    {
      const int so = 0;
      const unsigned ptail = 0, dtail = 0;
      PV_HALF(8, pk0, -1, 0)
    }
#undef PV_HALF
#undef PV_STEP
    *(LAS u32x2*)(L + a_o) = u32x2{pk2(acc[0] * ew, acc[1] * ew), pk2(acc[2] * ew, acc[3] * ew)};
    WG_BARRIER();
    const int j = h * 2 + s;
    if (j >= 40) return;
    const int r5 = j % 5, k8 = lane & 7;
    const int colq = b * DM + 80 * (j / 5) + (r5 < 4 ? 32 * (r5 >> 1) + 8 * (r5 & 1) : 64) + (lane & 1) * 4 + ((lane >> 1) & 1) * (r5 < 4 ? 16 : 8);
    const u32x2 xq = *(const GAS u32x2*)(x + colq);
    const unsigned tag = lds_ld(L + F_TAG);
    const __amdgpu_buffer_rsrc_t ry = RSRC(ws + G_Y + (size_t)(b * 64 + 8 * w) * DM + 16 * j, 8 * DM * 8);
    u32x4 yv = {0u, 0u, 0u, 0u};
    bool miss = true;
    __builtin_amdgcn_s_sleep(YDELAY);
    for (unsigned spins = 0; spins < SPIN_CAP; ++spins) {
      if (miss) {
        yv = __builtin_amdgcn_raw_buffer_load_b128(ry, (lane >> 3) * (DM * 8) + k8 * 16, 0, AUX_SC1);
        miss = yv[1] != tag || yv[3] != tag;
      }
      const int nmiss = __builtin_popcountll(__builtin_amdgcn_ballot_w64(miss));
      if (nmiss == 0) break;
      if (nmiss > YPOLL_N) __builtin_amdgcn_s_sleep(YPOLL_S); else __builtin_amdgcn_s_sleep(YPOLL_S0);
    }
    float s0 = __uint_as_float(yv[0]), s1 = __uint_as_float(yv[2]);
    s0 += dppf<XOR8>(s0); s1 += dppf<XOR8>(s1);
    s0 = xrow_sum(s0); s1 = xrow_sum(s1);
    if (lane < 8) {
      *(LAS float*)(L + L_RED + (w * 16 + lane) * 4) = s0;
      *(LAS float*)(L + L_RED + (w * 16 + 8 + lane) * 4) = s1;
    }
    LGKM0();
    unsigned old = 0;
    if (lane == 0) old = __hip_atomic_fetch_add((LAS unsigned*)(L + F_RED), 1u, RLX_WG);
    asm volatile("" ::: "memory");
    if (__builtin_amdgcn_readfirstlane(old) == 7u) {
      f32x4 t = *(const LAS f32x4*)(L + L_RED + ((lane >> 2) & 7) * 64 + (lane & 3) * 16);
#pragma unroll
      for (int k = 0; k < 4; ++k) t[k] += dppf<0x124>(t[k]);
#pragma unroll
      for (int k = 0; k < 4; ++k) t[k] += dppf<XOR8>(t[k]);
#pragma unroll
      for (int k = 0; k < 4; ++k) t[k] = swap16_sum(t[k], t[k]);
      const u64 r = (u64)pk2(bflo(xq[0]) + t[0], bfhi(xq[0]) + t[1]) | ((u64)pk2(bflo(xq[1]) + t[2], bfhi(xq[1]) + t[3]) << 32);
      if (lane < 4) __hip_atomic_store((GAS u64*)(out + colq), r, RLX_AGENT);
    }
    return;
  }

  const int hw = wave;
  const int hr = (hw + QROT * b) & 7;
  __builtin_amdgcn_s_setprio(3);
  u32x4 wr[20];
  unsigned xp[10];
#define PROJ_MFMA(PH)                                                                                              \
  {                                                                                                                \
    f32x4 ga = {0.f, 0.f, 0.f, 0.f};                                                                               \
    _Pragma("unroll") for (int i = 0; i < 10; ++i) ga = mfma(make_a(xp[i], sel), wr[i], ga);                       \
    if ((g >> 1) == (c >> 3))                                                                                      \
      *(LAS f32x4*)(L + L_P + ((PH) * 1024 + (hr * 2 + (g >> 1)) * 64 + 8 * (c & 7) + 4 * (g & 1)) * 4) = ga;      \
    LGKM0();                                                                                                       \
    if (lane == 0) __hip_atomic_fetch_add((LAS unsigned*)(L + ((PH) ? F_P1 : F_P0)), 1u, RLX_WG);                   \
  }
#define PROJ_SUM(PH, R0, R1)                                                                                       \
  {                                                                                                                \
    lds_wait_ge(L + ((PH) ? F_P1 : F_P0), 8u);                                                                     \
    f32x4 t4 = {0.f, 0.f, 0.f, 0.f};                                                                               \
    _Pragma("unroll") for (int k = 0; k < 4; ++k) t4 += *(const LAS f32x4*)(L + L_P + ((PH) * 1024 + (4 * g + k) * 64 + 4 * c) * 4); \
    _Pragma("unroll") for (int k = 0; k < 4; ++k) t4[k] = xrow_sum(t4[k]);                                         \
    R0 = pk2(t4[0], t4[1]);                                                                                        \
    R1 = pk2(t4[2], t4[3]);                                                                                        \
  }
  unsigned tag = 0;
  {
    unsigned xr[5];
#pragma unroll
    for (int k = 0; k < 5; ++k) xr[k] = ((const GAS unsigned*)(x + b * DM))[lane + 64 * k];
    unsigned e = 0;
    if (hw < 2) e = *(const GAS unsigned*)(ws + G_E);
    const __amdgpu_buffer_rsrc_t rq = RSRC(wq + (size_t)80 * hr * (NH * DH) + h * DH + 64 * s, 80 * NH * DH * 2 - (h * DH + 64 * s) * 2);
    const int vq = ((20 * g + (c >> 3)) * NH * DH + 8 * (c & 7)) * 2;
#pragma unroll
    for (int i = 0; i < 10; ++i) {
      wr[i] = __builtin_amdgcn_raw_buffer_load_b128(rq, vq, i * 2 * NH * DH * 2, 0);
      SB();
    }
    ln_row(xr, L + L_XQ, lane);
    SB();
    if (hw == 0) {
      const unsigned t1 = CLOCK() - XORG;
      if (lane == 0) *(LAS unsigned*)(L + F_T0) = t1;
      if (bid < 8 && lane == 0) put(ws + G_T + bid, __uint_as_float(t1), e + 1u);
    }
    LGKM0();
#pragma unroll
    for (int k = 0; k < 5; ++k) {
      const u32x2 t = *(const LAS u32x2*)(L + L_XQ + (80 * hr + 20 * g + 4 * k) * 2);
      xp[2 * k] = t[0];
      xp[2 * k + 1] = t[1];
    }
    PROJ_MFMA(0)
    if (hw < 2) tag = __builtin_amdgcn_readfirstlane(e) + 1u;
    if (hw == 0) {
      unsigned r0, r1;
      PROJ_SUM(0, r0, r1)
      if (g == 0) {
        __builtin_amdgcn_raw_buffer_store_b128(u32x4{r0, tag, r1, tag}, RSRC(xme, 512), c * 16, 0, AUX_SC1);
        *(LAS u32x2*)(L + L_Q + 128 * s + 8 * c) = u32x2{r0, r1};
      }
      if (lane == 0) *(LAS unsigned*)(L + F_TAG) = tag;
      LGKM0();
      if (lane == 0) __hip_atomic_fetch_add((LAS unsigned*)(L + F_Q), 1u, RLX_WG);
    } else if (hw == 1) {
      lds_wait_ge(L + F_P0, 8u);
      float v[1];
      sweep<1, 1>(xot + (lane & 31), 1, tag, v);
      if (lane < 32) *(LAS float*)(L + L_Q + 128 * (s ^ 1) + 4 * lane) = v[0];
      LGKM0();
      if (lane == 0) __hip_atomic_fetch_add((LAS unsigned*)(L + F_Q), 1u, RLX_WG);
    }
  }
  __builtin_amdgcn_s_setprio(0);
  lds_wait_ge(L + F_Q, 2u);
  tag = __builtin_amdgcn_readfirstlane(lds_ld(L + F_TAG));
  unsigned tb = __builtin_amdgcn_readfirstlane(lds_ld(L + F_T0));
  if (hr == ((mem + 4) & 7)) {
    const u64 tv = get(ws + G_T + (lane & 7));
    const bool ok = (unsigned)(tv >> 32) == tag;
    float d = ok ? (float)(int)(tb - (unsigned)tv) : 0.f, n = ok ? 1.f : 0.f;
    d += dppf<XOR1>(d); d += dppf<XOR2>(d); d += dppf<XOR7>(d);
    n += dppf<XOR1>(n); n += dppf<XOR2>(n); n += dppf<XOR7>(n);
    const int adj = (int)fminf(fmaxf(d / fmaxf(n, 1.f), -400.f), 400.f);
    if (lane == 0) *(LAS int*)(L + F_ADJ) = adj;
  }
  const __amdgpu_buffer_rsrc_t rpub = RSRC(ws + G_Y + (size_t)(b * 64 + h * 2 + s) * DM + 80 * hw, 80 * 8);
  {
    const u32x4 pre = __builtin_amdgcn_raw_buffer_load_b128(rpub, lane * 16, 0, 0);
    asm volatile("" ::"v"(pre));
    if (hr == ((mem + 5) & 7)) {
      const unsigned short pre2 = __builtin_nontemporal_load(vc + (size_t)(b * NH + h) * SK * DH);
      asm volatile("" ::"v"(pre2));
      const unsigned short pre3 = out[b * DM + 10 * lane];
      asm volatile("" ::"v"(pre3));
    }
  }
#define WAIT_UNTIL(t) while (CLOCK() - tb < (t)) __builtin_amdgcn_s_sleep(4)
  const int pc0 = 80 * hw + (g < 2 ? 32 * g + c : 64 + (c & 7)), pc1 = pc0 + (g < 2 ? 16 : 8);
  float ys0 = 0.f, ys1 = 0.f;
  {
#pragma unroll
    for (int i = 0; i < 10; ++i) FRESH(xp[i]);
    if (hr == ((mem + 2) & 7)) {
      const __amdgpu_buffer_rsrc_t rp = RSRC(wo + (size_t)(h * DH + 16 * mem) * DM, 16 * DM * 2);
#pragma unroll
      for (int i = 0; i < 20; ++i) {
        VMWAIT(FT_DEPTH - 1);
        wr[i] = __builtin_amdgcn_raw_buffer_load_b128(rp, lane * 16, i * 1024, 0);
        SB();
      }
#pragma unroll
      for (int i = 0; i < 20; ++i) asm volatile("" ::"v"(wr[i]));
    }
    const unsigned isb = (unsigned)(hr >> 1) ^ (unsigned)b;
    if (isb) WAIT_UNTIL(STAG1_0 + turn * STAG1);
    const __amdgpu_buffer_rsrc_t rg = RSRC((s ? wu : wg) + (size_t)80 * hr * FF + 64 * hs, 80 * FF * 2 - 64 * hs * 2);
    const int vg = ((20 * g + (c >> 3)) * FF + 8 * (c & 7)) * 2;
#pragma unroll
    for (int i = 0; i < 10; ++i) {
      VMWAIT_UNLESS(FT_DEPTH - 1, isb, 1, W_DEPTH - 1);
      wr[i] = __builtin_amdgcn_raw_buffer_load_b128(rg, vg, i * 2 * FF * 2, 0);
      SB();
    }
    const __amdgpu_buffer_rsrc_t rd = RSRC(wd + (size_t)(64 * hs + 32 * s + 4 * hr) * DM, 4 * DM * 2);
#pragma unroll
    for (int cb = 0; cb < 5; ++cb) {
      VMWAIT_UNLESS(FT_DEPTH - 1, isb, 1, W_DEPTH - 1);
      wr[10 + cb] = __builtin_amdgcn_raw_buffer_load_b128(rd, (g * DM + 8 * c) * 2, cb * 256, 0);
      SB();
    }
    PROJ_MFMA(1)
    if (hw == 2) {
      unsigned r0, r1;
      PROJ_SUM(1, r0, r1)
      if (g == 0 && (c >> 3) != s) __builtin_amdgcn_raw_buffer_store_b128(u32x4{r0, tag, r1, tag}, RSRC(xme + 32, 128), (c & 7) * 16, 0, AUX_SC1);
      float v[2];
      sweep<2, 16>(xot + 32 + 2 * (c & 7), 1, tag, v);
      const unsigned o0 = __float_as_uint(v[0]), o1 = __float_as_uint(v[1]);
      if (g == 0 && (c >> 3) == s)
        *(LAS u32x2*)(L + L_H + 8 * (c & 7)) = u32x2{pk2(bflo(r0) * bflo(o0), bfhi(r0) * bfhi(o0)), pk2(bflo(r1) * bflo(o1), bfhi(r1) * bfhi(o1))};
      LGKM0();
      if (lane == 0) __hip_atomic_store((LAS unsigned*)(L + F_H), 1u, RLX_WG);
    }
    lds_wait_ge(L + F_H, 1u);
    const unsigned X = *(const LAS unsigned short*)(L + L_H + 2 * (4 * hr + g));
    const u32x4 a = make_a(X, sel);
#pragma unroll
    for (int cb = 0; cb < 5; ++cb) {
      const f32x4 yf = mfma(a, wr[10 + cb], f32x4{0.f, 0.f, 0.f, 0.f});
      if (g < 2) *(LAS f32x4*)(L + L_FF + (hr * DM + 128 * cb + 8 * c + 4 * g) * 4) = yf;
    }
    LGKM0();
    if (lane == 0) __hip_atomic_fetch_add((LAS unsigned*)(L + F_FF), 1u, RLX_WG);
    if (h < NSL) {
      lds_wait_ge(L + F_FF, 8u);
#pragma unroll
      for (int p = 0; p < 8; ++p) {
        ys0 += *(const LAS float*)(L + L_FF + (p * DM + pc0) * 4);
        ys1 += *(const LAS float*)(L + L_FF + (p * DM + pc1) * 4);
      }
    }
  }
#define ML_EXCHANGE()                                                                                                       \
  {                                                                                                                         \
    lds_wait_ge(L + F_ML, 8u);                                                                                              \
    const float mv = *(const LAS float*)(L + L_ML + 4 * (lane & 7)), lv = *(const LAS float*)(L + L_ML + 32 + 4 * (lane & 7)); \
    float M = fmaxf(mv, dppf<XOR1>(mv));                                                                                    \
    M = fmaxf(M, dppf<XOR2>(M));                                                                                            \
    M = fmaxf(M, dppf<XOR7>(M));                                                                                            \
    float Ls = __builtin_amdgcn_exp2f(mv - M) * lv;                                                                         \
    FRESH(Ls);                                                                                                              \
    Ls += dppf<XOR1>(Ls);                                                                                                   \
    Ls += dppf<XOR2>(Ls);                                                                                                   \
    Ls += dppf<XOR7>(Ls);                                                                                                   \
    if (lane < 2) put(xme + 48 + lane, lane == 0 ? M : Ls, tag);                                                            \
    float o2[2];                                                                                                            \
    sweep<2, 16>(xot + 48, 1, tag, o2);                                                                                     \
    const float Mt = fmaxf(M, o2[0]);                                                                                       \
    float e0 = __builtin_amdgcn_exp2f(M - Mt) * Ls, e1 = __builtin_amdgcn_exp2f(o2[0] - Mt) * o2[1];                        \
    FRESH(e0); FRESH(e1);                                                                                                   \
    if (lane < 8) *(LAS float*)(L + L_EW + 4 * lane) = __builtin_amdgcn_exp2f(mv - Mt) / (e0 + e1);                         \
    LGKM0();                                                                                                                \
    if (lane == 0) __hip_atomic_store((LAS unsigned*)(L + F_EW), 1u, RLX_WG);                                               \
  }
#define HELPER_SYNC(n)                                                                \
  {                                                                                   \
    LGKM0();                                                                          \
    if (lane == 0) __hip_atomic_fetch_add((LAS unsigned*)(L + F_HS), 1u, RLX_WG);     \
    lds_wait_ge(L + F_HS, 8u * (n));                                                  \
  }
  if (hw == 6 && turn >= 4) ML_EXCHANGE()
  {
    tb -= __builtin_amdgcn_readfirstlane(lds_ld(L + F_ADJ));
    WAIT_UNTIL(STAG2_0 + turn * STAG2);
    const __amdgpu_buffer_rsrc_t ro = RSRC(wo + (size_t)(h * DH + 8 * hw) * DM, 72 * DM * 2);
    const int vo = (g * DM + 8 * c) * 2;
#pragma unroll
    for (int i = 0; i < 20; ++i) {
      VMWAIT(W_DEPTH - 1);
      wr[i] = __builtin_amdgcn_raw_buffer_load_b128(ro, vo, ((64 * (i / 10) + 4 * ((i / 5) & 1)) * DM + 128 * (i % 5)) * 2, 0);
      SB();
    }
    const int a_w = L_ST + (8 * hw + g) * ST_LD + 16 * c;
    const int a_r = L_ST + (8 * g + (c >> 2)) * ST_LD + (80 * hw + 4 * (c & 3)) * 2;
#pragma unroll
    for (int hf = 0; hf < 2; ++hf) {
#pragma unroll
      for (int i = 0; i < 10; ++i) *(LAS u32x4*)(L + a_w + (i / 5) * 4 * ST_LD + (i % 5) * 256) = wr[10 * hf + i];
      HELPER_SYNC(2 * hf + 1)
#pragma unroll
      for (int i = 0; i < 10; ++i) {
        const u32x2 r0 = __builtin_bit_cast(u32x2, __builtin_amdgcn_ds_read_tr16_b64_v4i16((LAS s16x4*)(L + a_r + (i / 5) * 32 * ST_LD + (i % 5) * 32)));
        const u32x2 r1 = __builtin_bit_cast(u32x2, __builtin_amdgcn_ds_read_tr16_b64_v4i16((LAS s16x4*)(L + a_r + ((i / 5) * 32 + 4) * ST_LD + (i % 5) * 32)));
        wr[10 * hf + i] = u32x4{r0[0], r0[1], r1[0], r1[1]};
      }
      if (hf == 0) HELPER_SYNC(2)
    }
  }
  if (hw == 6 && turn < 4) ML_EXCHANGE()
  if (hw == 7 && bid == 0) {
    float f4[4];
    sweep<4, 64>(ws + G_X + lane * 64 + 48, 64 * 64, tag, f4);
    if (lane == 0) __hip_atomic_store((GAS unsigned*)(ws + G_E), tag, RLX_AGENT);
  }
  int a_o = L_O + c * O_LD + 16 * g, v_st = lane * 16;
  FRESH(a_o); FRESH(v_st);
  WG_BARRIER();
  {
    u32x4 a[4];
#pragma unroll
    for (int u = 0; u < 4; ++u) a[u] = *(const LAS u32x4*)(L + a_o + 64 * u);
    float z[5];
#pragma unroll
    for (int i = 0; i < 5; ++i) {
      f32x4 y = {0.f, 0.f, 0.f, 0.f};
#pragma unroll
      for (int u = 0; u < 4; ++u) y = mfma(a[u], wr[5 * u + i], y);
      z[i] = (y[0] + y[1]) + (y[2] + y[3]);
    }
    const float e = swap16_sum(swap32_sum(z[0], z[4]), swap32_sum(z[2], z[4]));
    float o = swap16_sum(swap32_sum(z[1], z[4]), swap32_sum(z[3], z[4]));
    const float o8 = dppf<XOR8>(o);
    o = g < 2 ? o : o8;
    __builtin_amdgcn_raw_buffer_store_b128(u32x4{__float_as_uint(e + ys0), tag, __float_as_uint(o + ys1), tag}, rpub, v_st, 0, AUX_SC1);
  }
  if (bid == 0) while ((int)((unsigned)__builtin_amdgcn_s_memrealtime() - t_org - 6000u) < 0) __builtin_amdgcn_s_sleep(1);
}

extern "C" void kernel_launch(void* const* d_in, const int* in_sizes, int n_in, void* d_out, int out_size, void* d_ws,
                              size_t ws_size, hipStream_t stream) {
  (void)in_sizes; (void)n_in; (void)out_size; (void)ws_size;
  typedef const GAS unsigned short* cp;
  k_main<<<NWG, 1024, 0, stream>>>((cp)d_in[0], (cp)d_in[1], (cp)d_in[2], (cp)d_in[3], (cp)d_in[4], (cp)d_in[6], (cp)d_in[7],
                                   (cp)d_in[8], (GAS u64*)d_ws, (GAS unsigned short*)d_out);
}
```

```cpp
#include <hip/hip_runtime.h>
#include <hip/hip_bf16.h>
#include <stdint.h>

#define DEPTH 3
#define DEPTH_LO 3
#define DEPTH_HI 5
#define LAG1 110
#define W_DEPTH 6
#define XORG 105
#define PACE0 450
#define PACE_A 128
#define PACE 104
#define STAG1_0 950
#define STAG1 125
#define JUMP1 140
#define STAG2_0 1950
#define STAG2 205
#define JUMP2 240
#define TAILFREE 8
#define YDELAY 26
#define YPOLL_N 8
#define YPOLL_S 3
#define YPOLL_S0 1
#define WSTAG 12
#define WIN1 110
#define WIN2 200
#define WINPRE 40
#define QROT 2
#define FT_DEPTH 5
constexpr int AUX_NT = 2, AUX_SC1 = 16;

typedef unsigned u32x4 __attribute__((ext_vector_type(4)));
typedef unsigned u32x2 __attribute__((ext_vector_type(2)));
typedef float f32x4 __attribute__((ext_vector_type(4)));
typedef float f32x2 __attribute__((ext_vector_type(2)));
typedef short bf16x8 __attribute__((ext_vector_type(8)));
typedef __bf16 bf16x2_t __attribute__((ext_vector_type(2)));
typedef short s16x4 __attribute__((ext_vector_type(4)));
typedef unsigned long long u64;
#define GAS __attribute__((address_space(1)))
#define LAS __attribute__((address_space(3)))
#define RLX_AGENT __ATOMIC_RELAXED, __HIP_MEMORY_SCOPE_AGENT
#define RLX_WG __ATOMIC_RELAXED, __HIP_MEMORY_SCOPE_WORKGROUP

constexpr int NB = 4, DM = 640, NH = 32, SK = 4096, DH = 128, FF = 1728;
constexpr int NSL = FF / 64;
constexpr int NWG = 256;
constexpr float LOG2E = 1.4426950408889634f;
constexpr float QSCALE = 0.08838834764831845f;
constexpr unsigned SPIN_CAP = 1u << 18;

constexpr size_t G_E = 0;
constexpr size_t G_T = 16;
constexpr size_t G_X = 32;
constexpr size_t G_Y = G_X + (size_t)NWG * 64;

constexpr int L_XQ = 0;
constexpr int L_Q = L_XQ + DM * 2;
constexpr int L_P = L_Q + 256;
constexpr int O_LD = 272;
constexpr int L_O = L_P + 2 * 16 * 64 * 4;
constexpr int L_ML = L_O + 16 * O_LD;
constexpr int L_EW = L_ML + 64;
constexpr int L_FLAG = L_EW + 32;
constexpr int F_Q = L_FLAG, F_ML = L_FLAG + 4, F_H = L_FLAG + 8, F_P0 = L_FLAG + 12, F_P1 = L_FLAG + 16, F_FF = L_FLAG + 20,
              F_RED = L_FLAG + 24, F_TAG = L_FLAG + 28, F_T0 = L_FLAG + 32, F_EW = L_FLAG + 36, F_HS = L_FLAG + 40, F_ADJ = L_FLAG + 44;
constexpr int L_H = L_FLAG + 64;
constexpr int L_RED = L_H + 64;
constexpr int L_FF = L_RED + 512;
constexpr int ST_LD = DM * 2 + 16;
constexpr int L_ST = L_FF + 8 * DM * 4;
constexpr int L_END = L_ST + 64 * ST_LD;

__device__ __forceinline__ float bflo(unsigned u) { return __builtin_bit_cast(float, u << 16); }
__device__ __forceinline__ float bfhi(unsigned u) { return __builtin_bit_cast(float, u & 0xffff0000u); }
__device__ __forceinline__ unsigned pk2(float lo, float hi) {
  bf16x2_t r;
  r[0] = (__bf16)lo;
  r[1] = (__bf16)hi;
  return __builtin_bit_cast(unsigned, r);
}
__device__ __forceinline__ float rbf(float x) { return (float)(__bf16)x; }
__device__ __forceinline__ float dot2(unsigned a, unsigned b, float c) {
  return __builtin_amdgcn_fdot2_f32_bf16(__builtin_bit_cast(bf16x2_t, a), __builtin_bit_cast(bf16x2_t, b), c, false);
}
template <int CTRL> __device__ __forceinline__ float dppf(float x) {
  return __builtin_bit_cast(float, __builtin_amdgcn_mov_dpp(__builtin_bit_cast(int, x), CTRL, 0xf, 0xf, true));
}
template <int CTRL> __device__ __forceinline__ unsigned dppu(unsigned x) {
  return (unsigned)__builtin_amdgcn_mov_dpp((int)x, CTRL, 0xf, 0xf, true);
}
constexpr int XOR1 = 0xB1, XOR2 = 0x4E, XOR7 = 0x141, XOR8 = 0x128;
template <int CTRL> __device__ __forceinline__ float tr_merge(float lo, float hi, bool up) {
  const float t1 = lo + dppf<CTRL>(lo);
  const float t2 = hi + dppf<CTRL>(hi);
  return up ? t2 : t1;
}
__device__ __forceinline__ float row16_sum(float x) {
  x += dppf<XOR1>(x); x += dppf<XOR2>(x); x += dppf<XOR7>(x); x += dppf<XOR8>(x);
  return x;
}
__device__ __forceinline__ float row16_max(float x) {
  x = fmaxf(x, dppf<XOR1>(x)); x = fmaxf(x, dppf<XOR2>(x)); x = fmaxf(x, dppf<XOR7>(x)); x = fmaxf(x, dppf<XOR8>(x));
  return x;
}
__device__ __forceinline__ float half_sum(float x) {
  auto t = __builtin_amdgcn_permlane32_swap(__float_as_uint(x), __float_as_uint(x), false, false);
  return __uint_as_float(t[0]) + __uint_as_float(t[1]);
}
__device__ __forceinline__ float xrow_max(float x) {
  auto s = __builtin_amdgcn_permlane16_swap(__float_as_uint(x), __float_as_uint(x), false, false);
  x = fmaxf(__uint_as_float(s[0]), __uint_as_float(s[1]));
  auto t = __builtin_amdgcn_permlane32_swap(__float_as_uint(x), __float_as_uint(x), false, false);
  return fmaxf(__uint_as_float(t[0]), __uint_as_float(t[1]));
}
__device__ __forceinline__ float xrow_sum(float x) {
  auto s = __builtin_amdgcn_permlane16_swap(__float_as_uint(x), __float_as_uint(x), false, false);
  x = __uint_as_float(s[0]) + __uint_as_float(s[1]);
  return half_sum(x);
}
__device__ __forceinline__ float swap32_sum(float a, float b) {
  auto t = __builtin_amdgcn_permlane32_swap(__float_as_uint(a), __float_as_uint(b), false, false);
  return __uint_as_float(t[0]) + __uint_as_float(t[1]);
}
__device__ __forceinline__ float swap16_sum(float a, float b) {
  auto t = __builtin_amdgcn_permlane16_swap(__float_as_uint(a), __float_as_uint(b), false, false);
  return __uint_as_float(t[0]) + __uint_as_float(t[1]);
}
__device__ __forceinline__ f32x4 mfma(u32x4 a, u32x4 b, f32x4 c) {
  return __builtin_amdgcn_mfma_f32_16x16x32_bf16(__builtin_bit_cast(bf16x8, a), __builtin_bit_cast(bf16x8, b), c, 0, 0, 0);
}
#define LGKM0() asm volatile("s_waitcnt lgkmcnt(0)" ::: "memory")
#define WG_BARRIER()                \
  do {                              \
    LGKM0();                        \
    __builtin_amdgcn_s_barrier();   \
    asm volatile("" ::: "memory");  \
  } while (0)
#define SB() __builtin_amdgcn_sched_barrier(0)
#define RSRC(p, n) __builtin_amdgcn_make_buffer_rsrc((void*)(p), 0, (n), 0x00020000)
#define FRESH(v) asm volatile("" : "+v"(v))
#define VMWAIT_UNLESS(n, v, k, m) \
  asm volatile("s_waitcnt vmcnt(%3)\n\ts_cmp_ge_u32 %1, %2\n\ts_cbranch_scc1 1f\n\ts_waitcnt vmcnt(%0)\n1:" ::"i"(n), "s"(v), "i"(k), "i"(m) : "memory", "scc")
#define VMWAIT(n) asm volatile("s_waitcnt vmcnt(%0)" ::"i"(n) : "memory")

__device__ __forceinline__ void put(GAS u64* p, float v, unsigned tag) {
  __hip_atomic_store(p, ((u64)tag << 32) | (u64)__float_as_uint(v), RLX_AGENT);
}
__device__ __forceinline__ u64 get(const GAS u64* p) { return __hip_atomic_load(p, RLX_AGENT); }
__device__ __forceinline__ unsigned lds_ld(LAS unsigned char* p) { return __hip_atomic_load((LAS unsigned*)p, RLX_WG); }
__device__ __forceinline__ void lds_wait_ge(LAS unsigned char* p, unsigned want) {
  while (lds_ld(p) < want) __builtin_amdgcn_s_sleep(1);
  asm volatile("" ::: "memory");
}
template <int N, int SLP> __device__ __forceinline__ void sweep(const GAS u64* p, int S, unsigned tag, float (&v)[N]) {
  for (unsigned spins = 0;; ++spins) {
    bool ok = true;
#pragma unroll
    for (int k = 0; k < N; ++k) {
      const u64 x = get(p + k * S);
      v[k] = __uint_as_float((unsigned)x);
      ok &= (unsigned)(x >> 32) == tag;
    }
    if (__all(ok) || spins > SPIN_CAP) return;
    __builtin_amdgcn_s_sleep(SLP);
  }
}

struct Sel { unsigned s[4]; };
__device__ __forceinline__ Sel make_sel(int lane) {
  const int m = lane & 15;
  const unsigned src = (m & 8) ? 0x0706u : 0x0504u;
  const unsigned placed = (m & 1) ? ((src << 16) | 0x0c0cu) : (0x0c0c0000u | src);
  Sel r;
#pragma unroll
  for (int jj = 0; jj < 4; ++jj) r.s[jj] = (((m & 7) >> 1) == jj) ? placed : 0x0c0c0c0cu;
  return r;
}
__device__ __forceinline__ u32x4 make_a(unsigned X, const Sel& s) {
  u32x4 a;
#pragma unroll
  for (int jj = 0; jj < 4; ++jj) a[jj] = __builtin_amdgcn_perm(X, 0u, s.s[jj]);
  return a;
}
__device__ __forceinline__ unsigned split_hi_lo(float v) {
  const float h = rbf(v);
  return pk2(h, v - h);
}


__device__ __forceinline__ void ln_row(const unsigned (&xr)[5], LAS unsigned char* dst, int lane) {
  float sum = 0.f;
#pragma unroll
  for (int k = 0; k < 5; ++k) sum += bflo(xr[k]) + bfhi(xr[k]);
  const float mean = xrow_sum(row16_sum(sum)) * (1.0f / DM);
  float ss = 0.f;
#pragma unroll
  for (int k = 0; k < 5; ++k) {
    const float a = bflo(xr[k]) - mean, bb = bfhi(xr[k]) - mean;
    ss = fmaf(a, a, ss);
    ss = fmaf(bb, bb, ss);
  }
  const float var = xrow_sum(row16_sum(ss)) * (1.0f / DM);
  const float mu = rbf(mean);
  const float rinv = 1.0f / rbf(sqrtf(rbf(rbf(var) + 1e-5f)));
#pragma unroll
  for (int k = 0; k < 5; ++k)
    *(LAS unsigned*)(dst + 4 * (lane + 64 * k)) = pk2(rbf(bflo(xr[k]) - mu) * rinv, rbf(bfhi(xr[k]) - mu) * rinv);
}

__global__ void __launch_bounds__(1024, 4)
k_main(const GAS unsigned short* x_, const GAS unsigned short* kc_, const GAS unsigned short* wq_, GAS u64* ws_, const GAS unsigned short* vc_,
       const GAS unsigned short* wo_, const GAS unsigned short* wg_, const GAS unsigned short* wu_, const GAS unsigned short* wd_,
       GAS unsigned short* out_) {
  typedef u64 u64x8 __attribute__((ext_vector_type(8)));
  typedef const GAS unsigned short* cptr;
  const u64x8 ka = *(const __attribute__((address_space(4))) u64x8*)__builtin_amdgcn_kernarg_segment_ptr();
  const cptr x = (cptr)ka[0], kc = (cptr)ka[1], wq = (cptr)ka[2], vc = (cptr)ka[4], wo = (cptr)ka[5], wg = (cptr)ka[6], wu = (cptr)ka[7];
  GAS u64* const ws = (GAS u64*)ka[3];
  __shared__ __attribute__((aligned(16))) unsigned char smem[L_END];
  LAS unsigned char* L = (LAS unsigned char*)smem;
  const int tid = threadIdx.x, lane = tid & 63, wave = __builtin_amdgcn_readfirstlane(tid >> 6);
  const int g = lane >> 4, c = lane & 15;
  const int bid = blockIdx.x;
  const int jx = bid >> 3;
  const int h = (jx >> 3) * 8 + (bid & 7), mem = jx & 7, b = mem >> 1, s = mem & 1;
  const int slot = (b * NH + h) * 2 + s;
  const int hs = h < NSL ? h : h - 8;
  const unsigned turn = (mem + 2 * (jx >> 3)) & 7;
  if (tid < 16) *(LAS unsigned*)(L + L_FLAG + 4 * tid) = 0u;
  asm volatile("" ::"s"(ka));
  WG_BARRIER();
  Sel sel;
#define MAKE_SEL() { int lane2 = lane; FRESH(lane2); sel = make_sel(lane2); }
  GAS u64* const xme = ws + G_X + (size_t)slot * 64;
  const GAS u64* const xot = ws + G_X + (size_t)(slot ^ 1) * 64;
#define LATE_ARG(T, name, i)                                                                                              \
  const __attribute__((address_space(4))) u64* ka_##name = (const __attribute__((address_space(4))) u64*)__builtin_amdgcn_kernarg_segment_ptr(); \
  asm volatile("" : "+s"(ka_##name));                                                                                     \
  T name = (T)ka_##name[i]
#define CLOCK() ((unsigned)__builtin_amdgcn_s_memrealtime())

  if (wave >= 8) {
    const int w = wave - 8;
    const size_t base = (size_t)(b * NH + h) * SK * DH;
    const GAS unsigned short* kb = kc + base;
    const GAS unsigned short* vb = vc + base;
    int voff = lane * 16 + ((w & 3) + 4 * s + 8 * (w >> 2)) * 4096;
#define TILE_LD(r, so, t) __builtin_amdgcn_raw_buffer_load_b128((r), voff + ((t) & 3) * 1024, (so) + ((t) >> 2) * 65536, AUX_NT)
    u32x4 ring[16];
    {
      const __amdgpu_buffer_rsrc_t rk = RSRC(kb, SK * DH * 2);
#pragma unroll
      for (int t = 0; t < 16; ++t) {
        if (t >= DEPTH) VMWAIT(DEPTH - 1);
        ring[t] = TILE_LD(rk, 0, t);
        SB();
      }
    }
    MAKE_SEL()
    lds_wait_ge(L + F_Q, 2u);
    const u32x4 q = *(const LAS u32x4*)(L + L_Q + 16 * c);
    unsigned tb = __builtin_amdgcn_readfirstlane(lds_ld(L + F_T0));
    unsigned pstep = PACE_A, lag = 0;
    unsigned tgt = PACE0 + w * WSTAG, j1 = STAG1_0 + turn * STAG1, j2 = STAG2_0 + turn * STAG2;
    unsigned w1 = j1 - WINPRE;
    const unsigned w2 = j2 - WINPRE;
#define PACED_WAIT(t)                                                         \
  if (((t) & 3) == 0) {                                                       \
    tgt += ((t) > 16 - TAILFREE) ? ptail : pstep;                             \
    if (tgt >= j1) { tgt += JUMP1; j1 = ~0u; }                                \
    if (tgt >= j2) { tgt += JUMP2; j2 = ~0u; }                                \
    unsigned now;                                                             \
    while (((now = CLOCK() - tb) < tgt || now - w1 < WIN1 || now - w2 < WIN2) && now < (1u << 20)) __builtin_amdgcn_s_sleep(1); \
    lag = now - tgt;                                                          \
  }                                                                           \
  if ((t) == 16 - TAILFREE) lag |= dtail;                                     \
  VMWAIT_UNLESS(DEPTH_LO - 1, lag, LAG1, DEPTH_HI - 1);
    float sc0 = 0.f, sc1 = 0.f, sc2 = 0.f, sc3 = 0.f;
#define MERGE(LO, HI, CTRL, BIT) tr_merge<CTRL>((LO), (HI), (c & (BIT)) != 0)
#define QK_GROUP()                                      \
  {                                                     \
    const unsigned ptail = pstep, dtail = 0;            \
    FRESH(voff);                                        \
    float d1 = 0.f, d2 = 0.f, d4 = 0.f, d8 = 0.f;       \
    _Pragma("unroll") for (int t = 0; t < 16; ++t) {    \
      float a = dot2(ring[t][0], q[0], 0.f);            \
      a = dot2(ring[t][1], q[1], a);                    \
      a = dot2(ring[t][2], q[2], a);                    \
      a = dot2(ring[t][3], q[3], a);                    \
      FRESH(a);                                         \
      SB();                                             \
      PACED_WAIT(t)                                     \
      ring[t] = TILE_LD(rn, so, t);                     \
      if (!(t & 1)) d1 = a;                             \
      else {                                            \
        a = MERGE(d1, a, XOR8, 8);                      \
        if (!(t & 2)) d2 = a;                           \
        else {                                          \
          a = MERGE(d2, a, XOR7, 4);                    \
          if (!(t & 4)) d4 = a;                         \
          else {                                        \
            a = MERGE(d4, a, XOR2, 2);                  \
            if (!(t & 8)) d8 = a;                       \
            else {                                      \
              a = MERGE(d8, a, XOR1, 1);                \
              sc0 = sc1; sc1 = sc2; sc2 = sc3;          \
              sc3 = rbf(rbf(a) * QSCALE) * LOG2E;       \
            }                                           \
          }                                             \
        }                                               \
      }                                                 \
      SB();                                             \
    }                                                   \
  }
#define ADOPT_ORIGIN()                                                                \
  {                                                                                   \
    const unsigned adj = __builtin_amdgcn_readfirstlane(lds_ld(L + F_ADJ));           \
    tb -= adj;                                                                        \
    if (j1 != ~0u) j1 += adj;                                                         \
    w1 += adj;                                                                        \
  }
#pragma unroll 1
    for (int grp = 0; grp < 4; ++grp) {
      const __amdgpu_buffer_rsrc_t rn = RSRC(grp < 3 ? kb : vb, SK * DH * 2);
      const int so = ((grp + 1) & 3) * 262144;
      QK_GROUP()
      pstep = PACE;
      if (grp == 0) ADOPT_ORIGIN()
    }
#undef QK_GROUP
#undef MERGE
    float mw = fmaxf(fmaxf(sc0, sc1), fmaxf(sc2, sc3));
    mw = xrow_max(row16_max(mw));
    const float p0 = __builtin_amdgcn_exp2f(sc0 - mw), p1 = __builtin_amdgcn_exp2f(sc1 - mw);
    const float p2 = __builtin_amdgcn_exp2f(sc2 - mw), p3 = __builtin_amdgcn_exp2f(sc3 - mw);
    unsigned pk0 = split_hi_lo(p0), pk1 = split_hi_lo(p1), pk2_ = split_hi_lo(p2), pk3 = split_hi_lo(p3);
    const float lw = xrow_sum(row16_sum((p0 + p1) + (p2 + p3)));
    if (lane == 0) {
      *(LAS float*)(L + L_ML + w * 4) = mw;
      *(LAS float*)(L + L_ML + 32 + w * 4) = lw;
      LGKM0();
      __hip_atomic_fetch_add((LAS unsigned*)(L + F_ML), 1u, RLX_WG);
    }
    SB();
    f32x4 acc = {0.f, 0.f, 0.f, 0.f};
#define BREV4(T) ((((T) & 1) << 3) | (((T) & 2) << 1) | (((T) & 4) >> 1) | (((T) & 8) >> 3))
#define PV_STEP(S, PK, T)                               \
  {                                                     \
    const unsigned X = dppu<0x150 + BREV4(S)>(PK);      \
    acc = mfma(make_a(X, sel), ring[S], acc);           \
    if ((T) >= 0) {                                     \
      SB();                                             \
      PACED_WAIT(T)                                     \
      ring[(T) & 15] = TILE_LD(rv, so, T);              \
    }                                                   \
    SB();                                               \
  }
#define PV_HALF(S, PK, T, D)                                                                               \
  PV_STEP((S), PK, (T)) PV_STEP((S) + 1, PK, (T) + (D)) PV_STEP((S) + 2, PK, (T) + 2 * (D)) PV_STEP((S) + 3, PK, (T) + 3 * (D)) \
  PV_STEP((S) + 4, PK, (T) + 4 * (D)) PV_STEP((S) + 5, PK, (T) + 5 * (D)) PV_STEP((S) + 6, PK, (T) + 6 * (D)) PV_STEP((S) + 7, PK, (T) + 7 * (D))
    const __amdgpu_buffer_rsrc_t rv = RSRC(vb, SK * DH * 2);
    {
      const int so = 0;
      const unsigned ptail = 0, dtail = 0;
      PV_HALF(0, pk0, -1, 0)
    }
#pragma unroll 1
    for (int grp = 0; grp < 3; ++grp) {
      const int so = (grp + 1) * 262144;
      const unsigned ptail = grp == 2 ? 0u : pstep, dtail = (unsigned)(grp >> 1) << 20;
      FRESH(voff);
      PV_HALF(8, pk0, 0, 1)
      PV_HALF(0, pk1, 8, 1)
      pk0 = pk1; pk1 = pk2_; pk2_ = pk3;
    }
    LATE_ARG(GAS unsigned short*, out, 9);
    asm volatile("" ::"s"(out));
    lds_wait_ge(L + F_EW, 1u);
    const float ew = *(const LAS float*)(L + L_EW + 4 * w);
    int a_o = L_O + (w * 2 + (g >> 1)) * O_LD + (8 * c + 4 * (g & 1)) * 2;
    FRESH(a_o);
    SB();
    {
      const int so = 0;
      const unsigned ptail = 0, dtail = 0;
      PV_HALF(8, pk0, -1, 0)
    }
#undef PV_HALF
#undef PV_STEP
    *(LAS u32x2*)(L + a_o) = u32x2{pk2(acc[0] * ew, acc[1] * ew), pk2(acc[2] * ew, acc[3] * ew)};
    WG_BARRIER();
    const int j = h * 2 + s;
    if (j >= 40) return;
    const int r5 = j % 5, k8 = lane & 7;
    const int colq = b * DM + 80 * (j / 5) + (r5 < 4 ? 32 * (r5 >> 1) + 8 * (r5 & 1) : 64) + (lane & 1) * 4 + ((lane >> 1) & 1) * (r5 < 4 ? 16 : 8);
    const u32x2 xq = *(const GAS u32x2*)(x + colq);
    const unsigned tag = lds_ld(L + F_TAG);
    const __amdgpu_buffer_rsrc_t ry = RSRC(ws + G_Y + (size_t)(b * 64 + 8 * w) * DM + 16 * j, 8 * DM * 8);
    u32x4 yv = {0u, 0u, 0u, 0u};
    bool miss = true;
    __builtin_amdgcn_s_sleep(YDELAY);
    for (unsigned spins = 0; spins < SPIN_CAP; ++spins) {
      if (miss) {
        yv = __builtin_amdgcn_raw_buffer_load_b128(ry, (lane >> 3) * (DM * 8) + k8 * 16, 0, AUX_SC1);
        miss = yv[1] != tag || yv[3] != tag;
      }
      const int nmiss = __builtin_popcountll(__builtin_amdgcn_ballot_w64(miss));
      if (nmiss == 0) break;
      if (nmiss > YPOLL_N) __builtin_amdgcn_s_sleep(YPOLL_S); else __builtin_amdgcn_s_sleep(YPOLL_S0);
    }
    float s0 = __uint_as_float(yv[0]), s1 = __uint_as_float(yv[2]);
    s0 += dppf<XOR8>(s0); s1 += dppf<XOR8>(s1);
    s0 = xrow_sum(s0); s1 = xrow_sum(s1);
    if (lane < 8) {
      *(LAS float*)(L + L_RED + (w * 16 + lane) * 4) = s0;
      *(LAS float*)(L + L_RED + (w * 16 + 8 + lane) * 4) = s1;
    }
    LGKM0();
    unsigned old = 0;
    if (lane == 0) old = __hip_atomic_fetch_add((LAS unsigned*)(L + F_RED), 1u, RLX_WG);
    asm volatile("" ::: "memory");
    if (__builtin_amdgcn_readfirstlane(old) == 7u) {
      f32x4 t = *(const LAS f32x4*)(L + L_RED + ((lane >> 2) & 7) * 64 + (lane & 3) * 16);
#pragma unroll
      for (int k = 0; k < 4; ++k) t[k] += dppf<0x124>(t[k]);
#pragma unroll
      for (int k = 0; k < 4; ++k) t[k] += dppf<XOR8>(t[k]);
#pragma unroll
      for (int k = 0; k < 4; ++k) t[k] = swap16_sum(t[k], t[k]);
      const u64 r = (u64)pk2(bflo(xq[0]) + t[0], bfhi(xq[0]) + t[1]) | ((u64)pk2(bflo(xq[1]) + t[2], bfhi(xq[1]) + t[3]) << 32);
      if (lane < 4) __hip_atomic_store((GAS u64*)(out + colq), r, RLX_AGENT);
    }
    return;
  }

  const int hw = wave;
  const int hr = (hw + QROT * b) & 7;
  __builtin_amdgcn_s_setprio(3);
  u32x4 wr[20];
  unsigned xp[10];
#define PROJ_MFMA(PH)                                                                                              \
  {                                                                                                                \
    f32x4 ga = {0.f, 0.f, 0.f, 0.f};                                                                               \
    _Pragma("unroll") for (int i = 0; i < 10; ++i) ga = mfma(make_a(xp[i], sel), wr[i], ga);                       \
    if ((g >> 1) == (c >> 3))                                                                                      \
      *(LAS f32x4*)(L + L_P + ((PH) * 1024 + (hr * 2 + (g >> 1)) * 64 + 8 * (c & 7) + 4 * (g & 1)) * 4) = ga;      \
    LGKM0();                                                                                                       \
    if (lane == 0) __hip_atomic_fetch_add((LAS unsigned*)(L + ((PH) ? F_P1 : F_P0)), 1u, RLX_WG);                   \
  }
#define PROJ_SUM(PH, R0, R1)                                                                                       \
  {                                                                                                                \
    lds_wait_ge(L + ((PH) ? F_P1 : F_P0), 8u);                                                                     \
    f32x4 t4 = {0.f, 0.f, 0.f, 0.f};                                                                               \
    _Pragma("unroll") for (int k = 0; k < 4; ++k) t4 += *(const LAS f32x4*)(L + L_P + ((PH) * 1024 + (4 * g + k) * 64 + 4 * c) * 4); \
    _Pragma("unroll") for (int k = 0; k < 4; ++k) t4[k] = xrow_sum(t4[k]);                                         \
    R0 = pk2(t4[0], t4[1]);                                                                                        \
    R1 = pk2(t4[2], t4[3]);                                                                                        \
  }
  unsigned tag = 0;
  {
    unsigned xr[5];
#pragma unroll
    for (int k = 0; k < 5; ++k) xr[k] = ((const GAS unsigned*)(x + b * DM))[lane + 64 * k];
    unsigned e = 0;
    if (hw < 2) e = *(const GAS unsigned*)(ws + G_E);
    const __amdgpu_buffer_rsrc_t rq = RSRC(wq + (size_t)80 * hr * (NH * DH) + h * DH + 64 * s, 80 * NH * DH * 2 - (h * DH + 64 * s) * 2);
    const int vq = ((20 * g + (c >> 3)) * NH * DH + 8 * (c & 7)) * 2;
#pragma unroll
    for (int i = 0; i < 10; ++i) {
      wr[i] = __builtin_amdgcn_raw_buffer_load_b128(rq, vq, i * 2 * NH * DH * 2, 0);
      SB();
    }
    MAKE_SEL()
    ln_row(xr, L + L_XQ, lane);
    SB();
    if (hw == 0) {
      const unsigned t1 = CLOCK() - XORG;
      if (lane == 0) *(LAS unsigned*)(L + F_T0) = t1;
      if (bid < 8 && lane == 0) put(ws + G_T + bid, __uint_as_float(t1), e + 1u);
    }
    LGKM0();
#pragma unroll
    for (int k = 0; k < 5; ++k) {
      const u32x2 t = *(const LAS u32x2*)(L + L_XQ + (80 * hr + 20 * g + 4 * k) * 2);
      xp[2 * k] = t[0];
      xp[2 * k + 1] = t[1];
    }
    PROJ_MFMA(0)
    if (hw < 2) tag = __builtin_amdgcn_readfirstlane(e) + 1u;
    if (hw == 0) {
      unsigned r0, r1;
      PROJ_SUM(0, r0, r1)
      if (g == 0) {
        __builtin_amdgcn_raw_buffer_store_b128(u32x4{r0, tag, r1, tag}, RSRC(xme, 512), c * 16, 0, AUX_SC1);
        *(LAS u32x2*)(L + L_Q + 128 * s + 8 * c) = u32x2{r0, r1};
      }
      if (lane == 0) *(LAS unsigned*)(L + F_TAG) = tag;
      LGKM0();
      if (lane == 0) __hip_atomic_fetch_add((LAS unsigned*)(L + F_Q), 1u, RLX_WG);
    } else if (hw == 1) {
      lds_wait_ge(L + F_P0, 8u);
      float v[1];
      sweep<1, 1>(xot + (lane & 31), 1, tag, v);
      if (lane < 32) *(LAS float*)(L + L_Q + 128 * (s ^ 1) + 4 * lane) = v[0];
      LGKM0();
      if (lane == 0) __hip_atomic_fetch_add((LAS unsigned*)(L + F_Q), 1u, RLX_WG);
    }
  }
  __builtin_amdgcn_s_setprio(0);
  lds_wait_ge(L + F_Q, 2u);
  LATE_ARG(const GAS unsigned short*, wd, 8);
  LATE_ARG(GAS unsigned short*, out, 9);
  tag = __builtin_amdgcn_readfirstlane(lds_ld(L + F_TAG));
  unsigned tb = __builtin_amdgcn_readfirstlane(lds_ld(L + F_T0));
  if (hr == ((mem + 4) & 7)) {
    const u64 tv = get(ws + G_T + (lane & 7));
    const bool ok = (unsigned)(tv >> 32) == tag;
    float d = ok ? (float)(int)(tb - (unsigned)tv) : 0.f, n = ok ? 1.f : 0.f;
    d += dppf<XOR1>(d); d += dppf<XOR2>(d); d += dppf<XOR7>(d);
    n += dppf<XOR1>(n); n += dppf<XOR2>(n); n += dppf<XOR7>(n);
    const int adj = (int)fminf(fmaxf(d / fmaxf(n, 1.f), -400.f), 400.f);
    if (lane == 0) *(LAS int*)(L + F_ADJ) = adj;
  }
  const __amdgpu_buffer_rsrc_t rpub = RSRC(ws + G_Y + (size_t)(b * 64 + h * 2 + s) * DM + 80 * hw, 80 * 8);
  {
    const u32x4 pre = __builtin_amdgcn_raw_buffer_load_b128(rpub, lane * 16, 0, 0);
    asm volatile("" ::"v"(pre));
    if (hr == ((mem + 5) & 7)) {
      const unsigned short pre2 = __builtin_nontemporal_load(vc + (size_t)(b * NH + h) * SK * DH);
      asm volatile("" ::"v"(pre2));
      const unsigned short pre3 = out[b * DM + 10 * lane];
      asm volatile("" ::"v"(pre3));
    }
  }
#define WAIT_UNTIL(t) while (CLOCK() - tb < (t)) __builtin_amdgcn_s_sleep(4)
  const int pc0 = 80 * hw + (g < 2 ? 32 * g + c : 64 + (c & 7)), pc1 = pc0 + (g < 2 ? 16 : 8);
  float ys0 = 0.f, ys1 = 0.f;
  {
#pragma unroll
    for (int i = 0; i < 10; ++i) FRESH(xp[i]);
    if (hr == ((mem + 2) & 7)) {
      const __amdgpu_buffer_rsrc_t rp = RSRC(wo + (size_t)(h * DH + 16 * mem) * DM, 16 * DM * 2);
#pragma unroll
      for (int i = 0; i < 20; ++i) {
        VMWAIT(FT_DEPTH - 1);
        wr[i] = __builtin_amdgcn_raw_buffer_load_b128(rp, lane * 16, i * 1024, 0);
        SB();
      }
#pragma unroll
      for (int i = 0; i < 20; ++i) asm volatile("" ::"v"(wr[i]));
    }
    const unsigned isb = (unsigned)(hr >> 1) ^ (unsigned)b;
    if (isb) WAIT_UNTIL(STAG1_0 + turn * STAG1);
    const __amdgpu_buffer_rsrc_t rg = RSRC((s ? wu : wg) + (size_t)80 * hr * FF + 64 * hs, 80 * FF * 2 - 64 * hs * 2);
    const int vg = ((20 * g + (c >> 3)) * FF + 8 * (c & 7)) * 2;
#pragma unroll
    for (int i = 0; i < 10; ++i) {
      VMWAIT_UNLESS(FT_DEPTH - 1, isb, 1, W_DEPTH - 1);
      wr[i] = __builtin_amdgcn_raw_buffer_load_b128(rg, vg, i * 2 * FF * 2, 0);
      SB();
    }
    const __amdgpu_buffer_rsrc_t rd = RSRC(wd + (size_t)(64 * hs + 32 * s + 4 * hr) * DM, 4 * DM * 2);
#pragma unroll
    for (int cb = 0; cb < 5; ++cb) {
      VMWAIT_UNLESS(FT_DEPTH - 1, isb, 1, W_DEPTH - 1);
      wr[10 + cb] = __builtin_amdgcn_raw_buffer_load_b128(rd, (g * DM + 8 * c) * 2, cb * 256, 0);
      SB();
    }
    PROJ_MFMA(1)
    if (hw == 2) {
      unsigned r0, r1;
      PROJ_SUM(1, r0, r1)
      if (g == 0 && (c >> 3) != s) __builtin_amdgcn_raw_buffer_store_b128(u32x4{r0, tag, r1, tag}, RSRC(xme + 32, 128), (c & 7) * 16, 0, AUX_SC1);
      float v[2];
      sweep<2, 16>(xot + 32 + 2 * (c & 7), 1, tag, v);
      const unsigned o0 = __float_as_uint(v[0]), o1 = __float_as_uint(v[1]);
      if (g == 0 && (c >> 3) == s)
        *(LAS u32x2*)(L + L_H + 8 * (c & 7)) = u32x2{pk2(bflo(r0) * bflo(o0), bfhi(r0) * bfhi(o0)), pk2(bflo(r1) * bflo(o1), bfhi(r1) * bfhi(o1))};
      LGKM0();
      if (lane == 0) __hip_atomic_store((LAS unsigned*)(L + F_H), 1u, RLX_WG);
    }
    lds_wait_ge(L + F_H, 1u);
    const unsigned X = *(const LAS unsigned short*)(L + L_H + 2 * (4 * hr + g));
    const u32x4 a = make_a(X, sel);
#pragma unroll
    for (int cb = 0; cb < 5; ++cb) {
      const f32x4 yf = mfma(a, wr[10 + cb], f32x4{0.f, 0.f, 0.f, 0.f});
      if (g < 2) *(LAS f32x4*)(L + L_FF + (hr * DM + 128 * cb + 8 * c + 4 * g) * 4) = yf;
    }
    LGKM0();
    if (lane == 0) __hip_atomic_fetch_add((LAS unsigned*)(L + F_FF), 1u, RLX_WG);
    if (h < NSL) {
      lds_wait_ge(L + F_FF, 8u);
#pragma unroll
      for (int p = 0; p < 8; ++p) {
        ys0 += *(const LAS float*)(L + L_FF + (p * DM + pc0) * 4);
        ys1 += *(const LAS float*)(L + L_FF + (p * DM + pc1) * 4);
      }
    }
  }
#define ML_EXCHANGE()                                                                                                       \
  {                                                                                                                         \
    lds_wait_ge(L + F_ML, 8u);                                                                                              \
    const float mv = *(const LAS float*)(L + L_ML + 4 * (lane & 7)), lv = *(const LAS float*)(L + L_ML + 32 + 4 * (lane & 7)); \
    float M = fmaxf(mv, dppf<XOR1>(mv));                                                                                    \
    M = fmaxf(M, dppf<XOR2>(M));                                                                                            \
    M = fmaxf(M, dppf<XOR7>(M));                                                                                            \
    float Ls = __builtin_amdgcn_exp2f(mv - M) * lv;                                                                         \
    FRESH(Ls);                                                                                                              \
    Ls += dppf<XOR1>(Ls);                                                                                                   \
    Ls += dppf<XOR2>(Ls);                                                                                                   \
    Ls += dppf<XOR7>(Ls);                                                                                                   \
    if (lane < 2) put(xme + 48 + lane, lane == 0 ? M : Ls, tag);                                                            \
    float o2[2];                                                                                                            \
    sweep<2, 16>(xot + 48, 1, tag, o2);                                                                                     \
    const float Mt = fmaxf(M, o2[0]);                                                                                       \
    float e0 = __builtin_amdgcn_exp2f(M - Mt) * Ls, e1 = __builtin_amdgcn_exp2f(o2[0] - Mt) * o2[1];                        \
    FRESH(e0); FRESH(e1);                                                                                                   \
    if (lane < 8) *(LAS float*)(L + L_EW + 4 * lane) = __builtin_amdgcn_exp2f(mv - Mt) / (e0 + e1);                         \
    LGKM0();                                                                                                                \
    if (lane == 0) __hip_atomic_store((LAS unsigned*)(L + F_EW), 1u, RLX_WG);                                               \
  }
#define HELPER_SYNC(n)                                                                \
  {                                                                                   \
    LGKM0();                                                                          \
    if (lane == 0) __hip_atomic_fetch_add((LAS unsigned*)(L + F_HS), 1u, RLX_WG);     \
    lds_wait_ge(L + F_HS, 8u * (n));                                                  \
  }
  if (hw == 6 && turn >= 4) ML_EXCHANGE()
  {
    tb -= __builtin_amdgcn_readfirstlane(lds_ld(L + F_ADJ));
    WAIT_UNTIL(STAG2_0 + turn * STAG2);
    const __amdgpu_buffer_rsrc_t ro = RSRC(wo + (size_t)(h * DH + 8 * hw) * DM, 72 * DM * 2);
    const int vo = (g * DM + 8 * c) * 2;
#pragma unroll
    for (int i = 0; i < 20; ++i) {
      VMWAIT(W_DEPTH - 1);
      wr[i] = __builtin_amdgcn_raw_buffer_load_b128(ro, vo, ((64 * (i / 10) + 4 * ((i / 5) & 1)) * DM + 128 * (i % 5)) * 2, 0);
      SB();
    }
    const int a_w = L_ST + (8 * hw + g) * ST_LD + 16 * c;
    const int a_r = L_ST + (8 * g + (c >> 2)) * ST_LD + (80 * hw + 4 * (c & 3)) * 2;
#pragma unroll
    for (int hf = 0; hf < 2; ++hf) {
#pragma unroll
      for (int i = 0; i < 10; ++i) *(LAS u32x4*)(L + a_w + (i / 5) * 4 * ST_LD + (i % 5) * 256) = wr[10 * hf + i];
      HELPER_SYNC(2 * hf + 1)
#pragma unroll
      for (int i = 0; i < 10; ++i) {
        const u32x2 r0 = __builtin_bit_cast(u32x2, __builtin_amdgcn_ds_read_tr16_b64_v4i16((LAS s16x4*)(L + a_r + (i / 5) * 32 * ST_LD + (i % 5) * 32)));
        const u32x2 r1 = __builtin_bit_cast(u32x2, __builtin_amdgcn_ds_read_tr16_b64_v4i16((LAS s16x4*)(L + a_r + ((i / 5) * 32 + 4) * ST_LD + (i % 5) * 32)));
        wr[10 * hf + i] = u32x4{r0[0], r0[1], r1[0], r1[1]};
      }
      if (hf == 0) HELPER_SYNC(2)
    }
  }
  if (hw == 6 && turn < 4) ML_EXCHANGE()
  if (hw == 7 && bid == 0) {
    float f4[4];
    sweep<4, 64>(ws + G_X + lane * 64 + 48, 64 * 64, tag, f4);
    if (lane == 0) __hip_atomic_store((GAS unsigned*)(ws + G_E), tag, RLX_AGENT);
  }
  int a_o = L_O + c * O_LD + 16 * g, v_st = lane * 16;
  FRESH(a_o); FRESH(v_st);
  WG_BARRIER();
  {
    u32x4 a[4];
#pragma unroll
    for (int u = 0; u < 4; ++u) a[u] = *(const LAS u32x4*)(L + a_o + 64 * u);
    float z[5];
#pragma unroll
    for (int i = 0; i < 5; ++i) {
      f32x4 y = {0.f, 0.f, 0.f, 0.f};
#pragma unroll
      for (int u = 0; u < 4; ++u) y = mfma(a[u], wr[5 * u + i], y);
      z[i] = (y[0] + y[1]) + (y[2] + y[3]);
    }
    const float e = swap16_sum(swap32_sum(z[0], z[4]), swap32_sum(z[2], z[4]));
    float o = swap16_sum(swap32_sum(z[1], z[4]), swap32_sum(z[3], z[4]));
    const float o8 = dppf<XOR8>(o);
    o = g < 2 ? o : o8;
    __builtin_amdgcn_raw_buffer_store_b128(u32x4{__float_as_uint(e + ys0), tag, __float_as_uint(o + ys1), tag}, rpub, v_st, 0, AUX_SC1);
  }
}

extern "C" void kernel_launch(void* const* d_in, const int* in_sizes, int n_in, void* d_out, int out_size, void* d_ws,
                              size_t ws_size, hipStream_t stream) {
  (void)in_sizes; (void)n_in; (void)out_size; (void)ws_size;
  typedef const GAS unsigned short* cp;
  k_main<<<NWG, 1024, 0, stream>>>((cp)d_in[0], (cp)d_in[1], (cp)d_in[3], (GAS u64*)d_ws, (cp)d_in[2], (cp)d_in[4], (cp)d_in[6], (cp)d_in[7],
                                   (cp)d_in[8], (GAS unsigned short*)d_out);
}
```

```cpp
#include <hip/hip_runtime.h>
#include <hip/hip_bf16.h>
#include <stdint.h>

#define DEPTH 3
#define DEPTH_LO 3
#define DEPTH_HI 5
#define LAG1 120
#define W_DEPTH 8
#define XORG 105
#define PACE0 450
#define PACE_A 128
#define PACE 100
#define STAG1_0 950
#define STAG1 125
#define JUMP1 140
#define STAG2_0 1950
#define STAG2 205
#define JUMP2 240
#define TAILFREE 8
#define YDELAY 24
#define YPOLL_N 8
#define YPOLL_S 3
#define YPOLL_S0 1
#define WSTAG 12
#define WIN1 110
#define WIN2 200
#define WINPRE 40
#define QROT 2
#define FT_DEPTH 5
constexpr int AUX_NT = 2, AUX_SC1 = 16;

typedef unsigned u32x4 __attribute__((ext_vector_type(4)));
typedef unsigned u32x2 __attribute__((ext_vector_type(2)));
typedef float f32x4 __attribute__((ext_vector_type(4)));
typedef float f32x2 __attribute__((ext_vector_type(2)));
typedef short bf16x8 __attribute__((ext_vector_type(8)));
typedef __bf16 bf16x2_t __attribute__((ext_vector_type(2)));
typedef short s16x4 __attribute__((ext_vector_type(4)));
typedef unsigned long long u64;
#define GAS __attribute__((address_space(1)))
#define LAS __attribute__((address_space(3)))
#define RLX_AGENT __ATOMIC_RELAXED, __HIP_MEMORY_SCOPE_AGENT
#define RLX_WG __ATOMIC_RELAXED, __HIP_MEMORY_SCOPE_WORKGROUP

constexpr int NB = 4, DM = 640, NH = 32, SK = 4096, DH = 128, FF = 1728;
constexpr int NSL = FF / 64;
constexpr int NWG = 256;
constexpr float LOG2E = 1.4426950408889634f;
constexpr float QSCALE = 0.08838834764831845f;
constexpr unsigned SPIN_CAP = 1u << 18;

constexpr size_t G_E = 0;
constexpr size_t G_T = 16;
constexpr size_t G_X = 32;
constexpr size_t G_Y = G_X + (size_t)NWG * 64;

constexpr int L_XQ = 0;
constexpr int L_Q = L_XQ + DM * 2;
constexpr int L_P = L_Q + 256;
constexpr int O_LD = 272;
constexpr int L_O = L_P + 2 * 16 * 64 * 4;
constexpr int L_ML = L_O + 16 * O_LD;
constexpr int L_EW = L_ML + 64;
constexpr int L_FLAG = L_EW + 32;
constexpr int F_Q = L_FLAG, F_ML = L_FLAG + 4, F_H = L_FLAG + 8, F_P0 = L_FLAG + 12, F_P1 = L_FLAG + 16, F_FF = L_FLAG + 20,
              F_RED = L_FLAG + 24, F_TAG = L_FLAG + 28, F_T0 = L_FLAG + 32, F_EW = L_FLAG + 36, F_HS = L_FLAG + 40, F_ADJ = L_FLAG + 44;
constexpr int L_H = L_FLAG + 64;
constexpr int L_RED = L_H + 64;
constexpr int L_FF = L_RED + 512;
constexpr int ST_LD = DM * 2 + 16;
constexpr int L_ST = L_FF + 8 * DM * 4;
constexpr int L_END = L_ST + 64 * ST_LD;

__device__ __forceinline__ float bflo(unsigned u) { return __builtin_bit_cast(float, u << 16); }
__device__ __forceinline__ float bfhi(unsigned u) { return __builtin_bit_cast(float, u & 0xffff0000u); }
__device__ __forceinline__ unsigned pk2(float lo, float hi) {
  bf16x2_t r;
  r[0] = (__bf16)lo;
  r[1] = (__bf16)hi;
  return __builtin_bit_cast(unsigned, r);
}
__device__ __forceinline__ float rbf(float x) { return (float)(__bf16)x; }
__device__ __forceinline__ float dot2(unsigned a, unsigned b, float c) {
  return __builtin_amdgcn_fdot2_f32_bf16(__builtin_bit_cast(bf16x2_t, a), __builtin_bit_cast(bf16x2_t, b), c, false);
}
template <int CTRL> __device__ __forceinline__ float dppf(float x) {
  return __builtin_bit_cast(float, __builtin_amdgcn_mov_dpp(__builtin_bit_cast(int, x), CTRL, 0xf, 0xf, true));
}
template <int CTRL> __device__ __forceinline__ unsigned dppu(unsigned x) {
  return (unsigned)__builtin_amdgcn_mov_dpp((int)x, CTRL, 0xf, 0xf, true);
}
constexpr int XOR1 = 0xB1, XOR2 = 0x4E, XOR7 = 0x141, XOR8 = 0x128;
template <int CTRL> __device__ __forceinline__ float tr_merge(float lo, float hi, bool up) {
  const float t1 = lo + dppf<CTRL>(lo);
  const float t2 = hi + dppf<CTRL>(hi);
  return up ? t2 : t1;
}
__device__ __forceinline__ float row16_sum(float x) {
  x += dppf<XOR1>(x); x += dppf<XOR2>(x); x += dppf<XOR7>(x); x += dppf<XOR8>(x);
  return x;
}
__device__ __forceinline__ float row16_max(float x) {
  x = fmaxf(x, dppf<XOR1>(x)); x = fmaxf(x, dppf<XOR2>(x)); x = fmaxf(x, dppf<XOR7>(x)); x = fmaxf(x, dppf<XOR8>(x));
  return x;
}
__device__ __forceinline__ float half_sum(float x) {
  auto t = __builtin_amdgcn_permlane32_swap(__float_as_uint(x), __float_as_uint(x), false, false);
  return __uint_as_float(t[0]) + __uint_as_float(t[1]);
}
__device__ __forceinline__ float xrow_max(float x) {
  auto s = __builtin_amdgcn_permlane16_swap(__float_as_uint(x), __float_as_uint(x), false, false);
  x = fmaxf(__uint_as_float(s[0]), __uint_as_float(s[1]));
  auto t = __builtin_amdgcn_permlane32_swap(__float_as_uint(x), __float_as_uint(x), false, false);
  return fmaxf(__uint_as_float(t[0]), __uint_as_float(t[1]));
}
__device__ __forceinline__ float xrow_sum(float x) {
  auto s = __builtin_amdgcn_permlane16_swap(__float_as_uint(x), __float_as_uint(x), false, false);
  x = __uint_as_float(s[0]) + __uint_as_float(s[1]);
  return half_sum(x);
}
__device__ __forceinline__ float swap32_sum(float a, float b) {
  auto t = __builtin_amdgcn_permlane32_swap(__float_as_uint(a), __float_as_uint(b), false, false);
  return __uint_as_float(t[0]) + __uint_as_float(t[1]);
}
__device__ __forceinline__ float swap16_sum(float a, float b) {
  auto t = __builtin_amdgcn_permlane16_swap(__float_as_uint(a), __float_as_uint(b), false, false);
  return __uint_as_float(t[0]) + __uint_as_float(t[1]);
}
__device__ __forceinline__ f32x4 mfma(u32x4 a, u32x4 b, f32x4 c) {
  return __builtin_amdgcn_mfma_f32_16x16x32_bf16(__builtin_bit_cast(bf16x8, a), __builtin_bit_cast(bf16x8, b), c, 0, 0, 0);
}
#define LGKM0() asm volatile("s_waitcnt lgkmcnt(0)" ::: "memory")
#define WG_BARRIER()                \
  do {                              \
    LGKM0();                        \
    __builtin_amdgcn_s_barrier();   \
    asm volatile("" ::: "memory");  \
  } while (0)
#define SB() __builtin_amdgcn_sched_barrier(0)
#define RSRC(p, n) __builtin_amdgcn_make_buffer_rsrc((void*)(p), 0, (n), 0x00020000)
#define FRESH(v) asm volatile("" : "+v"(v))
#define VMWAIT_UNLESS(n, v, k, m) \
  asm volatile("s_waitcnt vmcnt(%3)\n\ts_cmp_ge_u32 %1, %2\n\ts_cbranch_scc1 1f\n\ts_waitcnt vmcnt(%0)\n1:" ::"i"(n), "s"(v), "i"(k), "i"(m) : "memory", "scc")
#define VMWAIT(n) asm volatile("s_waitcnt vmcnt(%0)" ::"i"(n) : "memory")

__device__ __forceinline__ void put(GAS u64* p, float v, unsigned tag) {
  __hip_atomic_store(p, ((u64)tag << 32) | (u64)__float_as_uint(v), RLX_AGENT);
}
__device__ __forceinline__ u64 get(const GAS u64* p) { return __hip_atomic_load(p, RLX_AGENT); }
__device__ __forceinline__ unsigned lds_ld(LAS unsigned char* p) { return __hip_atomic_load((LAS unsigned*)p, RLX_WG); }
__device__ __forceinline__ void lds_wait_ge(LAS unsigned char* p, unsigned want) {
  while (lds_ld(p) < want) __builtin_amdgcn_s_sleep(1);
  asm volatile("" ::: "memory");
}
template <int N, int SLP> __device__ __forceinline__ void sweep(const GAS u64* p, int S, unsigned tag, float (&v)[N]) {
  for (unsigned spins = 0;; ++spins) {
    bool ok = true;
#pragma unroll
    for (int k = 0; k < N; ++k) {
      const u64 x = get(p + k * S);
      v[k] = __uint_as_float((unsigned)x);
      ok &= (unsigned)(x >> 32) == tag;
    }
    if (__all(ok) || spins > SPIN_CAP) return;
    __builtin_amdgcn_s_sleep(SLP);
  }
}

struct Sel { unsigned s[4]; };
__device__ __forceinline__ Sel make_sel(int lane) {
  const int m = lane & 15;
  const unsigned src = (m & 8) ? 0x0706u : 0x0504u;
  const unsigned placed = (m & 1) ? ((src << 16) | 0x0c0cu) : (0x0c0c0000u | src);
  Sel r;
#pragma unroll
  for (int jj = 0; jj < 4; ++jj) r.s[jj] = (((m & 7) >> 1) == jj) ? placed : 0x0c0c0c0cu;
  return r;
}
__device__ __forceinline__ u32x4 make_a(unsigned X, const Sel& s) {
  u32x4 a;
#pragma unroll
  for (int jj = 0; jj < 4; ++jj) a[jj] = __builtin_amdgcn_perm(X, 0u, s.s[jj]);
  return a;
}
__device__ __forceinline__ unsigned split_hi_lo(float v) {
  const float h = rbf(v);
  return pk2(h, v - h);
}


__device__ __forceinline__ void ln_row(const unsigned (&xr)[5], LAS unsigned char* dst, int lane) {
  float sum = 0.f;
#pragma unroll
  for (int k = 0; k < 5; ++k) sum += bflo(xr[k]) + bfhi(xr[k]);
  const float mean = xrow_sum(row16_sum(sum)) * (1.0f / DM);
  float ss = 0.f;
#pragma unroll
  for (int k = 0; k < 5; ++k) {
    const float a = bflo(xr[k]) - mean, bb = bfhi(xr[k]) - mean;
    ss = fmaf(a, a, ss);
    ss = fmaf(bb, bb, ss);
  }
  const float var = xrow_sum(row16_sum(ss)) * (1.0f / DM);
  const float mu = rbf(mean);
  const float rinv = 1.0f / rbf(sqrtf(rbf(rbf(var) + 1e-5f)));
#pragma unroll
  for (int k = 0; k < 5; ++k)
    *(LAS unsigned*)(dst + 4 * (lane + 64 * k)) = pk2(rbf(bflo(xr[k]) - mu) * rinv, rbf(bfhi(xr[k]) - mu) * rinv);
}

__global__ void __launch_bounds__(1024, 4)
k_main(const GAS unsigned short* x_, const GAS unsigned short* kc_, const GAS unsigned short* wq_, GAS u64* ws_, const GAS unsigned short* vc_,
       const GAS unsigned short* wo_, const GAS unsigned short* wg_, const GAS unsigned short* wu_, const GAS unsigned short* wd_,
       GAS unsigned short* out_) {
  typedef u64 u64x8 __attribute__((ext_vector_type(8)));
  typedef const GAS unsigned short* cptr;
  const u64x8 ka = *(const __attribute__((address_space(4))) u64x8*)__builtin_amdgcn_kernarg_segment_ptr();
  const cptr x = (cptr)ka[0], kc = (cptr)ka[1], wq = (cptr)ka[2], vc = (cptr)ka[4], wo = (cptr)ka[5], wg = (cptr)ka[6], wu = (cptr)ka[7];
  GAS u64* const ws = (GAS u64*)ka[3];
  __shared__ __attribute__((aligned(16))) unsigned char smem[L_END];
  LAS unsigned char* L = (LAS unsigned char*)smem;
  const int tid = threadIdx.x, lane = tid & 63, wave = __builtin_amdgcn_readfirstlane(tid >> 6);
  const int g = lane >> 4, c = lane & 15;
  const int bid = blockIdx.x;
  const int jx = bid >> 3;
  const int h = (jx >> 3) * 8 + (bid & 7), mem = jx & 7, b = mem >> 1, s = mem & 1;
  const int slot = (b * NH + h) * 2 + s;
  const int hs = h < NSL ? h : h - 8;
  const unsigned turn = (mem + 2 * (jx >> 3)) & 7;
  if (tid < 16) *(LAS unsigned*)(L + L_FLAG + 4 * tid) = 0u;
  asm volatile("" ::"s"(ka));
  WG_BARRIER();
  Sel sel;
#define MAKE_SEL() { int lane2 = lane; FRESH(lane2); sel = make_sel(lane2); }
  GAS u64* const xme = ws + G_X + (size_t)slot * 64;
  const GAS u64* const xot = ws + G_X + (size_t)(slot ^ 1) * 64;
#define LATE_ARG(T, name, i)                                                                                              \
  const __attribute__((address_space(4))) u64* ka_##name = (const __attribute__((address_space(4))) u64*)__builtin_amdgcn_kernarg_segment_ptr(); \
  asm volatile("" : "+s"(ka_##name));                                                                                     \
  T name = (T)ka_##name[i]
#define CLOCK() ((unsigned)__builtin_amdgcn_s_memrealtime())

  if (wave >= 8) {
    const int w = wave - 8;
    const size_t base = (size_t)(b * NH + h) * SK * DH;
    const GAS unsigned short* kb = kc + base;
    const GAS unsigned short* vb = vc + base;
    int voff = lane * 16 + ((w & 3) + 4 * s + 8 * (w >> 2)) * 4096;
#define TILE_LD(r, so, t) __builtin_amdgcn_raw_buffer_load_b128((r), voff + ((t) & 3) * 1024, (so) + ((t) >> 2) * 65536, AUX_NT)
    u32x4 ring[16];
    {
      const __amdgpu_buffer_rsrc_t rk = RSRC(kb, SK * DH * 2);
#pragma unroll
      for (int t = 0; t < 16; ++t) {
        if (t >= DEPTH) VMWAIT(DEPTH - 1);
        ring[t] = TILE_LD(rk, 0, t);
        SB();
      }
    }
    MAKE_SEL()
    lds_wait_ge(L + F_Q, 2u);
    const u32x4 q = *(const LAS u32x4*)(L + L_Q + 16 * c);
    unsigned tb = __builtin_amdgcn_readfirstlane(lds_ld(L + F_T0));
    unsigned pstep = PACE_A, lag = 0;
    unsigned tgt = PACE0 + w * WSTAG, j1 = STAG1_0 + turn * STAG1, j2 = STAG2_0 + turn * STAG2;
    unsigned w1 = j1 - WINPRE;
    const unsigned w2 = j2 - WINPRE;
#define PACED_WAIT(t)                                                         \
  if (((t) & 3) == 0) {                                                       \
    tgt += ((t) > 16 - TAILFREE) ? ptail : pstep;                             \
    if (tgt >= j1) { tgt += JUMP1; j1 = ~0u; }                                \
    if (tgt >= j2) { tgt += JUMP2; j2 = ~0u; }                                \
    unsigned now;                                                             \
    while (((now = CLOCK() - tb) < tgt || now - w1 < WIN1 || now - w2 < WIN2) && now < (1u << 20)) __builtin_amdgcn_s_sleep(1); \
    lag = now - tgt;                                                          \
  }                                                                           \
  if ((t) == 16 - TAILFREE) lag |= dtail;                                     \
  VMWAIT_UNLESS(DEPTH_LO - 1, lag, LAG1, DEPTH_HI - 1);
    float sc0 = 0.f, sc1 = 0.f, sc2 = 0.f, sc3 = 0.f;
#define MERGE(LO, HI, CTRL, BIT) tr_merge<CTRL>((LO), (HI), (c & (BIT)) != 0)
#define QK_GROUP()                                      \
  {                                                     \
    const unsigned ptail = pstep, dtail = 0;            \
    FRESH(voff);                                        \
    float d1 = 0.f, d2 = 0.f, d4 = 0.f, d8 = 0.f;       \
    _Pragma("unroll") for (int t = 0; t < 16; ++t) {    \
      float a = dot2(ring[t][0], q[0], 0.f);            \
      a = dot2(ring[t][1], q[1], a);                    \
      a = dot2(ring[t][2], q[2], a);                    \
      a = dot2(ring[t][3], q[3], a);                    \
      FRESH(a);                                         \
      SB();                                             \
      PACED_WAIT(t)                                     \
      ring[t] = TILE_LD(rn, so, t);                     \
      if (!(t & 1)) d1 = a;                             \
      else {                                            \
        a = MERGE(d1, a, XOR8, 8);                      \
        if (!(t & 2)) d2 = a;                           \
        else {                                          \
          a = MERGE(d2, a, XOR7, 4);                    \
          if (!(t & 4)) d4 = a;                         \
          else {                                        \
            a = MERGE(d4, a, XOR2, 2);                  \
            if (!(t & 8)) d8 = a;                       \
            else {                                      \
              a = MERGE(d8, a, XOR1, 1);                \
              sc0 = sc1; sc1 = sc2; sc2 = sc3;          \
              sc3 = rbf(rbf(a) * QSCALE) * LOG2E;       \
            }                                           \
          }                                             \
        }                                               \
      }                                                 \
      SB();                                             \
    }                                                   \
  }
#define ADOPT_ORIGIN()                                                                \
  {                                                                                   \
    const unsigned adj = __builtin_amdgcn_readfirstlane(lds_ld(L + F_ADJ));           \
    tb -= adj;                                                                        \
    if (j1 != ~0u) j1 += adj;                                                         \
    w1 += adj;                                                                        \
  }
#pragma unroll 1
    for (int grp = 0; grp < 4; ++grp) {
      const __amdgpu_buffer_rsrc_t rn = RSRC(grp < 3 ? kb : vb, SK * DH * 2);
      const int so = ((grp + 1) & 3) * 262144;
      QK_GROUP()
      pstep = PACE;
      if (grp == 0) ADOPT_ORIGIN()
    }
#undef QK_GROUP
#undef MERGE
    float mw = fmaxf(fmaxf(sc0, sc1), fmaxf(sc2, sc3));
    mw = xrow_max(row16_max(mw));
    const float p0 = __builtin_amdgcn_exp2f(sc0 - mw), p1 = __builtin_amdgcn_exp2f(sc1 - mw);
    const float p2 = __builtin_amdgcn_exp2f(sc2 - mw), p3 = __builtin_amdgcn_exp2f(sc3 - mw);
    unsigned pk0 = split_hi_lo(p0), pk1 = split_hi_lo(p1), pk2_ = split_hi_lo(p2), pk3 = split_hi_lo(p3);
    const float lw = xrow_sum(row16_sum((p0 + p1) + (p2 + p3)));
    if (lane == 0) {
      *(LAS float*)(L + L_ML + w * 4) = mw;
      *(LAS float*)(L + L_ML + 32 + w * 4) = lw;
      LGKM0();
      __hip_atomic_fetch_add((LAS unsigned*)(L + F_ML), 1u, RLX_WG);
    }
    SB();
    f32x4 acc = {0.f, 0.f, 0.f, 0.f};
#define BREV4(T) ((((T) & 1) << 3) | (((T) & 2) << 1) | (((T) & 4) >> 1) | (((T) & 8) >> 3))
#define PV_STEP(S, PK, T)                               \
  {                                                     \
    const unsigned X = dppu<0x150 + BREV4(S)>(PK);      \
    acc = mfma(make_a(X, sel), ring[S], acc);           \
    if ((T) >= 0) {                                     \
      SB();                                             \
      PACED_WAIT(T)                                     \
      ring[(T) & 15] = TILE_LD(rv, so, T);              \
    }                                                   \
    SB();                                               \
  }
#define PV_HALF(S, PK, T, D)                                                                               \
  PV_STEP((S), PK, (T)) PV_STEP((S) + 1, PK, (T) + (D)) PV_STEP((S) + 2, PK, (T) + 2 * (D)) PV_STEP((S) + 3, PK, (T) + 3 * (D)) \
  PV_STEP((S) + 4, PK, (T) + 4 * (D)) PV_STEP((S) + 5, PK, (T) + 5 * (D)) PV_STEP((S) + 6, PK, (T) + 6 * (D)) PV_STEP((S) + 7, PK, (T) + 7 * (D))
    const __amdgpu_buffer_rsrc_t rv = RSRC(vb, SK * DH * 2);
    {
      const int so = 0;
      const unsigned ptail = 0, dtail = 0;
      PV_HALF(0, pk0, -1, 0)
    }
#pragma unroll 1
    for (int grp = 0; grp < 3; ++grp) {
      const int so = (grp + 1) * 262144;
      const unsigned ptail = grp == 2 ? 0u : pstep, dtail = (unsigned)(grp >> 1) << 20;
      FRESH(voff);
      PV_HALF(8, pk0, 0, 1)
      PV_HALF(0, pk1, 8, 1)
      pk0 = pk1; pk1 = pk2_; pk2_ = pk3;
    }
    LATE_ARG(GAS unsigned short*, out, 9);
    asm volatile("" ::"s"(out));
    lds_wait_ge(L + F_EW, 1u);
    const float ew = *(const LAS float*)(L + L_EW + 4 * w);
    int a_o = L_O + (w * 2 + (g >> 1)) * O_LD + (8 * c + 4 * (g & 1)) * 2;
    FRESH(a_o);
    SB();
    {
      const int so = 0;
      const unsigned ptail = 0, dtail = 0;
      PV_HALF(8, pk0, -1, 0)
    }
#undef PV_HALF
#undef PV_STEP
    *(LAS u32x2*)(L + a_o) = u32x2{pk2(acc[0] * ew, acc[1] * ew), pk2(acc[2] * ew, acc[3] * ew)};
    WG_BARRIER();
    const int j = h * 2 + s;
    if (j >= 40) return;
    const int r5 = j % 5, k8 = lane & 7;
    const int colq = b * DM + 80 * (j / 5) + (r5 < 4 ? 32 * (r5 >> 1) + 8 * (r5 & 1) : 64) + (lane & 1) * 4 + ((lane >> 1) & 1) * (r5 < 4 ? 16 : 8);
    const u32x2 xq = *(const GAS u32x2*)(x + colq);
    const unsigned tag = lds_ld(L + F_TAG);
    const __amdgpu_buffer_rsrc_t ry = RSRC(ws + G_Y + (size_t)(b * 64 + 8 * w) * DM + 16 * j, 8 * DM * 8);
    u32x4 yv = {0u, 0u, 0u, 0u};
    bool miss = true;
    __builtin_amdgcn_s_sleep(YDELAY);
    for (unsigned spins = 0; spins < SPIN_CAP; ++spins) {
      asm volatile("" ::: "memory");
      if (miss) {
        yv = __builtin_amdgcn_raw_buffer_load_b128(ry, (lane >> 3) * (DM * 8) + k8 * 16, 0, AUX_SC1);
        miss = yv[1] != tag || yv[3] != tag;
      }
      const int nmiss = __builtin_popcountll(__builtin_amdgcn_ballot_w64(miss));
      if (nmiss == 0) break;
      if (nmiss > YPOLL_N) __builtin_amdgcn_s_sleep(YPOLL_S); else __builtin_amdgcn_s_sleep(YPOLL_S0);
    }
    float s0 = __uint_as_float(yv[0]), s1 = __uint_as_float(yv[2]);
    s0 += dppf<XOR8>(s0); s1 += dppf<XOR8>(s1);
    s0 = xrow_sum(s0); s1 = xrow_sum(s1);
    if (lane < 8) {
      *(LAS float*)(L + L_RED + (w * 16 + lane) * 4) = s0;
      *(LAS float*)(L + L_RED + (w * 16 + 8 + lane) * 4) = s1;
    }
    LGKM0();
    unsigned old = 0;
    if (lane == 0) old = __hip_atomic_fetch_add((LAS unsigned*)(L + F_RED), 1u, RLX_WG);
    asm volatile("" ::: "memory");
    if (__builtin_amdgcn_readfirstlane(old) == 7u) {
      f32x4 t = *(const LAS f32x4*)(L + L_RED + ((lane >> 2) & 7) * 64 + (lane & 3) * 16);
#pragma unroll
      for (int k = 0; k < 4; ++k) t[k] += dppf<0x124>(t[k]);
#pragma unroll
      for (int k = 0; k < 4; ++k) t[k] += dppf<XOR8>(t[k]);
#pragma unroll
      for (int k = 0; k < 4; ++k) t[k] = swap16_sum(t[k], t[k]);
      const u64 r = (u64)pk2(bflo(xq[0]) + t[0], bfhi(xq[0]) + t[1]) | ((u64)pk2(bflo(xq[1]) + t[2], bfhi(xq[1]) + t[3]) << 32);
      if (lane < 4) __hip_atomic_store((GAS u64*)(out + colq), r, RLX_AGENT);
    }
    return;
  }

  const int hw = wave;
  const int hr = (hw + QROT * b) & 7;
  __builtin_amdgcn_s_setprio(3);
  u32x4 wr[20];
  unsigned xp[10];
#define PROJ_MFMA(PH)                                                                                              \
  {                                                                                                                \
    f32x4 ga = {0.f, 0.f, 0.f, 0.f};                                                                               \
    _Pragma("unroll") for (int i = 0; i < 10; ++i) ga = mfma(make_a(xp[i], sel), wr[i], ga);                       \
    if ((g >> 1) == (c >> 3))                                                                                      \
      *(LAS f32x4*)(L + L_P + ((PH) * 1024 + (hr * 2 + (g >> 1)) * 64 + 8 * (c & 7) + 4 * (g & 1)) * 4) = ga;      \
    LGKM0();                                                                                                       \
    if (lane == 0) __hip_atomic_fetch_add((LAS unsigned*)(L + ((PH) ? F_P1 : F_P0)), 1u, RLX_WG);                   \
  }
#define PROJ_SUM(PH, R0, R1)                                                                                       \
  {                                                                                                                \
    lds_wait_ge(L + ((PH) ? F_P1 : F_P0), 8u);                                                                     \
    f32x4 t4 = {0.f, 0.f, 0.f, 0.f};                                                                               \
    _Pragma("unroll") for (int k = 0; k < 4; ++k) t4 += *(const LAS f32x4*)(L + L_P + ((PH) * 1024 + (4 * g + k) * 64 + 4 * c) * 4); \
    _Pragma("unroll") for (int k = 0; k < 4; ++k) t4[k] = xrow_sum(t4[k]);                                         \
    R0 = pk2(t4[0], t4[1]);                                                                                        \
    R1 = pk2(t4[2], t4[3]);                                                                                        \
  }
  unsigned tag = 0;
  {
    unsigned xr[5];
#pragma unroll
    for (int k = 0; k < 5; ++k) xr[k] = ((const GAS unsigned*)(x + b * DM))[lane + 64 * k];
    unsigned e = 0;
    if (hw < 2) e = *(const GAS unsigned*)(ws + G_E);
    const __amdgpu_buffer_rsrc_t rq = RSRC(wq + (size_t)80 * hr * (NH * DH) + h * DH + 64 * s, 80 * NH * DH * 2 - (h * DH + 64 * s) * 2);
    const int vq = ((20 * g + (c >> 3)) * NH * DH + 8 * (c & 7)) * 2;
#pragma unroll
    for (int i = 0; i < 10; ++i) {
      wr[i] = __builtin_amdgcn_raw_buffer_load_b128(rq, vq, i * 2 * NH * DH * 2, 0);
      SB();
    }
    MAKE_SEL()
    ln_row(xr, L + L_XQ, lane);
    SB();
    if (hw == 0) {
      const unsigned t1 = CLOCK() - XORG;
      if (lane == 0) *(LAS unsigned*)(L + F_T0) = t1;
      if (bid < 8 && lane == 0) put(ws + G_T + bid, __uint_as_float(t1), e + 1u);
    }
    LGKM0();
#pragma unroll
    for (int k = 0; k < 5; ++k) {
      const u32x2 t = *(const LAS u32x2*)(L + L_XQ + (80 * hr + 20 * g + 4 * k) * 2);
      xp[2 * k] = t[0];
      xp[2 * k + 1] = t[1];
    }
    PROJ_MFMA(0)
    if (hw < 2) tag = __builtin_amdgcn_readfirstlane(e) + 1u;
    if (hw == 0) {
      unsigned r0, r1;
      PROJ_SUM(0, r0, r1)
      if (g == 0) {
        __builtin_amdgcn_raw_buffer_store_b128(u32x4{r0, tag, r1, tag}, RSRC(xme, 512), c * 16, 0, AUX_SC1);
        *(LAS u32x2*)(L + L_Q + 128 * s + 8 * c) = u32x2{r0, r1};
      }
      if (lane == 0) *(LAS unsigned*)(L + F_TAG) = tag;
      LGKM0();
      if (lane == 0) __hip_atomic_fetch_add((LAS unsigned*)(L + F_Q), 1u, RLX_WG);
    } else if (hw == 1) {
      lds_wait_ge(L + F_P0, 8u);
      float v[1];
      sweep<1, 1>(xot + (lane & 31), 1, tag, v);
      if (lane < 32) *(LAS float*)(L + L_Q + 128 * (s ^ 1) + 4 * lane) = v[0];
      LGKM0();
      if (lane == 0) __hip_atomic_fetch_add((LAS unsigned*)(L + F_Q), 1u, RLX_WG);
    }
  }
  __builtin_amdgcn_s_setprio(0);
  lds_wait_ge(L + F_Q, 2u);
  LATE_ARG(const GAS unsigned short*, wd, 8);
  LATE_ARG(GAS unsigned short*, out, 9);
  tag = __builtin_amdgcn_readfirstlane(lds_ld(L + F_TAG));
  unsigned tb = __builtin_amdgcn_readfirstlane(lds_ld(L + F_T0));
  if (hr == ((mem + 4) & 7)) {
    const u64 tv = get(ws + G_T + (lane & 7));
    const bool ok = (unsigned)(tv >> 32) == tag;
    float d = ok ? (float)(int)(tb - (unsigned)tv) : 0.f, n = ok ? 1.f : 0.f;
    d += dppf<XOR1>(d); d += dppf<XOR2>(d); d += dppf<XOR7>(d);
    n += dppf<XOR1>(n); n += dppf<XOR2>(n); n += dppf<XOR7>(n);
    const int adj = (int)fminf(fmaxf(d / fmaxf(n, 1.f), -400.f), 400.f);
    if (lane == 0) *(LAS int*)(L + F_ADJ) = adj;
  }
  const __amdgpu_buffer_rsrc_t rpub = RSRC(ws + G_Y + (size_t)(b * 64 + h * 2 + s) * DM + 80 * hw, 80 * 8);
  {
    const u32x4 pre = __builtin_amdgcn_raw_buffer_load_b128(rpub, lane * 16, 0, 0);
    asm volatile("" ::"v"(pre));
    if (hr == ((mem + 5) & 7)) {
      const unsigned short pre2 = __builtin_nontemporal_load(vc + (size_t)(b * NH + h) * SK * DH);
      asm volatile("" ::"v"(pre2));
      const unsigned short pre3 = out[b * DM + 10 * lane];
      asm volatile("" ::"v"(pre3));
    }
  }
#define WAIT_UNTIL(t) while (CLOCK() - tb < (t)) __builtin_amdgcn_s_sleep(4)
  const int pc0 = 80 * hw + (g < 2 ? 32 * g + c : 64 + (c & 7)), pc1 = pc0 + (g < 2 ? 16 : 8);
  float ys0 = 0.f, ys1 = 0.f;
  {
#pragma unroll
    for (int i = 0; i < 10; ++i) FRESH(xp[i]);
    if (hr == ((mem + 2) & 7)) {
      const __amdgpu_buffer_rsrc_t rp = RSRC(wo + (size_t)(h * DH + 16 * mem) * DM, 16 * DM * 2);
#pragma unroll
      for (int i = 0; i < 20; ++i) {
        VMWAIT(FT_DEPTH - 1);
        wr[i] = __builtin_amdgcn_raw_buffer_load_b128(rp, lane * 16, i * 1024, 0);
        SB();
      }
#pragma unroll
      for (int i = 0; i < 20; ++i) asm volatile("" ::"v"(wr[i]));
    }
    const unsigned isb = (unsigned)(hr >> 1) ^ (unsigned)b;
    if (isb) WAIT_UNTIL(STAG1_0 + turn * STAG1);
    const __amdgpu_buffer_rsrc_t rg = RSRC((s ? wu : wg) + (size_t)80 * hr * FF + 64 * hs, 80 * FF * 2 - 64 * hs * 2);
    const int vg = ((20 * g + (c >> 3)) * FF + 8 * (c & 7)) * 2;
#pragma unroll
    for (int i = 0; i < 10; ++i) {
      VMWAIT_UNLESS(FT_DEPTH - 1, isb, 1, W_DEPTH - 1);
      wr[i] = __builtin_amdgcn_raw_buffer_load_b128(rg, vg, i * 2 * FF * 2, 0);
      SB();
    }
    const __amdgpu_buffer_rsrc_t rd = RSRC(wd + (size_t)(64 * hs + 32 * s + 4 * hr) * DM, 4 * DM * 2);
#pragma unroll
    for (int cb = 0; cb < 5; ++cb) {
      VMWAIT_UNLESS(FT_DEPTH - 1, isb, 1, W_DEPTH - 1);
      wr[10 + cb] = __builtin_amdgcn_raw_buffer_load_b128(rd, (g * DM + 8 * c) * 2, cb * 256, 0);
      SB();
    }
    PROJ_MFMA(1)
    if (hw == 2) {
      unsigned r0, r1;
      PROJ_SUM(1, r0, r1)
      if (g == 0 && (c >> 3) != s) __builtin_amdgcn_raw_buffer_store_b128(u32x4{r0, tag, r1, tag}, RSRC(xme + 32, 128), (c & 7) * 16, 0, AUX_SC1);
      float v[2];
      sweep<2, 16>(xot + 32 + 2 * (c & 7), 1, tag, v);
      const unsigned o0 = __float_as_uint(v[0]), o1 = __float_as_uint(v[1]);
      if (g == 0 && (c >> 3) == s)
        *(LAS u32x2*)(L + L_H + 8 * (c & 7)) = u32x2{pk2(bflo(r0) * bflo(o0), bfhi(r0) * bfhi(o0)), pk2(bflo(r1) * bflo(o1), bfhi(r1) * bfhi(o1))};
      LGKM0();
      if (lane == 0) __hip_atomic_store((LAS unsigned*)(L + F_H), 1u, RLX_WG);
    }
    lds_wait_ge(L + F_H, 1u);
    const unsigned X = *(const LAS unsigned short*)(L + L_H + 2 * (4 * hr + g));
    const u32x4 a = make_a(X, sel);
#pragma unroll
    for (int cb = 0; cb < 5; ++cb) {
      const f32x4 yf = mfma(a, wr[10 + cb], f32x4{0.f, 0.f, 0.f, 0.f});
      if (g < 2) *(LAS f32x4*)(L + L_FF + (hr * DM + 128 * cb + 8 * c + 4 * g) * 4) = yf;
    }
    LGKM0();
    if (lane == 0) __hip_atomic_fetch_add((LAS unsigned*)(L + F_FF), 1u, RLX_WG);
    if (h < NSL) {
      lds_wait_ge(L + F_FF, 8u);
#pragma unroll
      for (int p = 0; p < 8; ++p) {
        ys0 += *(const LAS float*)(L + L_FF + (p * DM + pc0) * 4);
        ys1 += *(const LAS float*)(L + L_FF + (p * DM + pc1) * 4);
      }
    }
  }
#define ML_EXCHANGE()                                                                                                       \
  {                                                                                                                         \
    lds_wait_ge(L + F_ML, 8u);                                                                                              \
    const float mv = *(const LAS float*)(L + L_ML + 4 * (lane & 7)), lv = *(const LAS float*)(L + L_ML + 32 + 4 * (lane & 7)); \
    float M = fmaxf(mv, dppf<XOR1>(mv));                                                                                    \
    M = fmaxf(M, dppf<XOR2>(M));                                                                                            \
    M = fmaxf(M, dppf<XOR7>(M));                                                                                            \
    float Ls = __builtin_amdgcn_exp2f(mv - M) * lv;                                                                         \
    FRESH(Ls);                                                                                                              \
    Ls += dppf<XOR1>(Ls);                                                                                                   \
    Ls += dppf<XOR2>(Ls);                                                                                                   \
    Ls += dppf<XOR7>(Ls);                                                                                                   \
    if (lane < 2) put(xme + 48 + lane, lane == 0 ? M : Ls, tag);                                                            \
    float o2[2];                                                                                                            \
    sweep<2, 16>(xot + 48, 1, tag, o2);                                                                                     \
    const float Mt = fmaxf(M, o2[0]);                                                                                       \
    float e0 = __builtin_amdgcn_exp2f(M - Mt) * Ls, e1 = __builtin_amdgcn_exp2f(o2[0] - Mt) * o2[1];                        \
    FRESH(e0); FRESH(e1);                                                                                                   \
    if (lane < 8) *(LAS float*)(L + L_EW + 4 * lane) = __builtin_amdgcn_exp2f(mv - Mt) / (e0 + e1);                         \
    LGKM0();                                                                                                                \
    if (lane == 0) __hip_atomic_store((LAS unsigned*)(L + F_EW), 1u, RLX_WG);                                               \
  }
#define HELPER_SYNC(n)                                                                \
  {                                                                                   \
    LGKM0();                                                                          \
    if (lane == 0) __hip_atomic_fetch_add((LAS unsigned*)(L + F_HS), 1u, RLX_WG);     \
    lds_wait_ge(L + F_HS, 8u * (n));                                                  \
  }
  if (hw == 6 && turn >= 4) ML_EXCHANGE()
  {
    tb -= __builtin_amdgcn_readfirstlane(lds_ld(L + F_ADJ));
    WAIT_UNTIL(STAG2_0 + turn * STAG2);
    const __amdgpu_buffer_rsrc_t ro = RSRC(wo + (size_t)(h * DH + 8 * hw) * DM, 72 * DM * 2);
    const int vo = (g * DM + 8 * c) * 2;
#pragma unroll
    for (int i = 0; i < 20; ++i) {
      VMWAIT(W_DEPTH - 1);
      wr[i] = __builtin_amdgcn_raw_buffer_load_b128(ro, vo, ((64 * (i / 10) + 4 * ((i / 5) & 1)) * DM + 128 * (i % 5)) * 2, 0);
      SB();
    }
    const int a_w = L_ST + (8 * hw + g) * ST_LD + 16 * c;
    const int a_r = L_ST + (8 * g + (c >> 2)) * ST_LD + (80 * hw + 4 * (c & 3)) * 2;
#pragma unroll
    for (int hf = 0; hf < 2; ++hf) {
#pragma unroll
      for (int i = 0; i < 10; ++i) *(LAS u32x4*)(L + a_w + (i / 5) * 4 * ST_LD + (i % 5) * 256) = wr[10 * hf + i];
      HELPER_SYNC(2 * hf + 1)
#pragma unroll
      for (int i = 0; i < 10; ++i) {
        const u32x2 r0 = __builtin_bit_cast(u32x2, __builtin_amdgcn_ds_read_tr16_b64_v4i16((LAS s16x4*)(L + a_r + (i / 5) * 32 * ST_LD + (i % 5) * 32)));
        const u32x2 r1 = __builtin_bit_cast(u32x2, __builtin_amdgcn_ds_read_tr16_b64_v4i16((LAS s16x4*)(L + a_r + ((i / 5) * 32 + 4) * ST_LD + (i % 5) * 32)));
        wr[10 * hf + i] = u32x4{r0[0], r0[1], r1[0], r1[1]};
      }
      if (hf == 0) HELPER_SYNC(2)
    }
  }
  if (hw == 6 && turn < 4) ML_EXCHANGE()
  if (hw == 7 && bid == 0) {
    WAIT_UNTIL(3200u);
    float f4[4];
    sweep<4, 64>(ws + G_X + lane * 64 + 48, 64 * 64, tag, f4);
    if (lane == 0) __hip_atomic_store((GAS unsigned*)(ws + G_E), tag, RLX_AGENT);
  }
  int a_o = L_O + c * O_LD + 16 * g, v_st = lane * 16;
  FRESH(a_o); FRESH(v_st);
  WG_BARRIER();
  {
    u32x4 a[4];
#pragma unroll
    for (int u = 0; u < 4; ++u) a[u] = *(const LAS u32x4*)(L + a_o + 64 * u);
    float z[5];
#pragma unroll
    for (int i = 0; i < 5; ++i) {
      f32x4 y = {0.f, 0.f, 0.f, 0.f};
#pragma unroll
      for (int u = 0; u < 4; ++u) y = mfma(a[u], wr[5 * u + i], y);
      z[i] = (y[0] + y[1]) + (y[2] + y[3]);
    }
    const float e = swap16_sum(swap32_sum(z[0], z[4]), swap32_sum(z[2], z[4]));
    float o = swap16_sum(swap32_sum(z[1], z[4]), swap32_sum(z[3], z[4]));
    const float o8 = dppf<XOR8>(o);
    o = g < 2 ? o : o8;
    __builtin_amdgcn_raw_buffer_store_b128(u32x4{__float_as_uint(e + ys0), tag, __float_as_uint(o + ys1), tag}, rpub, v_st, 0, AUX_SC1);
  }
}

extern "C" void kernel_launch(void* const* d_in, const int* in_sizes, int n_in, void* d_out, int out_size, void* d_ws,
                              size_t ws_size, hipStream_t stream) {
  (void)in_sizes; (void)n_in; (void)out_size; (void)ws_size;
  typedef const GAS unsigned short* cp;
  k_main<<<NWG, 1024, 0, stream>>>((cp)d_in[0], (cp)d_in[1], (cp)d_in[3], (GAS u64*)d_ws, (cp)d_in[2], (cp)d_in[4], (cp)d_in[6], (cp)d_in[7],
                                   (cp)d_in[8], (GAS unsigned short*)d_out);
}
```
